```python
import torch

def _softmax(x, axis=-1):
    return torch.softmax(x, dim=axis)
def _gelu(x):
    return torch.nn.functional.gelu(x, approximate="tanh")
def _silu(x):
    return torch.nn.functional.silu(x)
def _sigmoid(x):
    return torch.sigmoid(x)
def _layernorm(x, axis=-1, eps=1e-5):
    m = x.mean(dim=axis, keepdim=True)
    v = x.var(dim=axis, keepdim=True, unbiased=False)
    return (x - m) / torch.sqrt(v + eps)
def _scatter_add(tgt, idx, src, axis=0):
    return tgt.clone().index_add_(axis, idx, src)
def _segment_sum(src, seg, n):
    out = torch.zeros((n,) + src.shape[1:], dtype=src.dtype)
    return out.index_add_(0, seg, src)
def _pad(x, pw):
    flat = tuple(v for pair in reversed(pw) for v in pair)
    return torch.nn.functional.pad(x, flat)

kernel_name = 'gated_mix__linear_attn_block__spectral_gfnet__prompt'

def setup_inputs():

    _w = (lambda a: (lambda t: t.to(torch.bfloat16) if t.is_floating_point() else t)(torch.as_tensor(a)))
    def _A_setup():
        B, S, D = 1, 2048, 640
        return {
            "x": _w((lambda *s: torch.randn(*s, dtype=torch.bfloat16) * 0.05)(B, S, D)),
            "wqkv": _w((lambda *s: torch.randn(*s, dtype=torch.bfloat16) * 0.4)(D, 3 * D)),
            "wo": _w((lambda *s: torch.randn(*s, dtype=torch.bfloat16) * 0.05)(D, D)),
        }
    _A_in = _A_setup()

    def _B_setup():
        B, S, D = 1, 2048, 640
        Df = D // 2 + 1
        return {
            "x": _w((lambda *s: torch.randn(*s, dtype=torch.bfloat16) * 0.05)(B, S, D)),
            "w_r": _w((lambda *s: torch.randn(*s, dtype=torch.bfloat16) * 1.6)(S, Df) * 0.02),
            "w_i": _w((lambda *s: torch.randn(*s, dtype=torch.bfloat16) * 1.6)(S, Df) * 0.02),
        }
    _B_in = _B_setup()

    return {
        "A_x": _A_in["x"],
        "A_wqkv": _A_in["wqkv"],
        "A_wo": _A_in["wo"],
        "B_x": _B_in["x"],
        "B_w_r": _B_in["w_r"],
        "B_w_i": _B_in["w_i"],
        "gate_w": _w((lambda *s: torch.randn(*s, dtype=torch.bfloat16) * 0.05)(640) * 16.0),
    }

def reference(A_x, A_wqkv, A_wo, B_x, B_w_r, B_w_i, gate_w):
    def _A_ref(x, wqkv, wo):
        B, S, D = x.shape
        H = 32; Dh = D // H
        qkv = (lambda x, *d: x.permute(*d))((x @ wqkv).reshape(B, S, 3, H, Dh), 2, 0, 3, 1, 4)
        q, k, v = qkv[0], qkv[1], qkv[2]
        phi = lambda t: torch.where(t > 0, t + 1, torch.exp(t)) + 1e-6
        pq, pk = phi(q), phi(k)
        kv = torch.einsum("bhsd,bhse->bhde", pk, v)
        z = torch.einsum("bhsd->bhd", pk)
        num = torch.einsum("bhqd,bhde->bhqe", pq, kv)
        den = torch.einsum("bhqd,bhd->bhq", pq, z)[..., None] + 1e-6
        o = num / den
        return (lambda x, *d: x.permute(*d))(o, 0, 2, 1, 3).reshape(B, S, D) @ wo

    def _B_ref(x, w_r, w_i):
        B, S, D = x.shape
        X = (lambda x, axes=(-2,-1): torch.fft.rfft2(x.float(), dim=axes))(x, axes=(-2, -1))
        W = w_r + 1j * w_i
        X = X * W
        y = (lambda x, s, axes=(-2,-1): torch.fft.irfft2(x, s=s, dim=axes).to(torch.bfloat16))(X, (S, D), axes=(-2, -1))
        return y

    x = A_x
    g = _sigmoid((x * gate_w).sum(-1, keepdims=True))
    a = _A_ref(x, A_wqkv, A_wo)
    b = _B_ref(x, B_w_r, B_w_i)
    return g * a + (1 - g) * b
```

```cpp
#include <hip/hip_runtime.h>
#include <hip/hip_bf16.h>
#include <utility>

typedef unsigned short u16;
typedef __attribute__((ext_vector_type(8))) short bf16x8;
typedef __attribute__((ext_vector_type(4))) short s16x4;
typedef __attribute__((ext_vector_type(4))) float f32x4;
typedef __attribute__((ext_vector_type(2))) float f32x2;
typedef __attribute__((ext_vector_type(2))) __bf16 bf16x2_t;
typedef __attribute__((ext_vector_type(4))) unsigned u32x4;
typedef __attribute__((ext_vector_type(2))) unsigned u32x2;
#define LDS_AS __attribute__((address_space(3)))
#define DI __device__ __forceinline__
#define MFMA16(a, b, c) __builtin_amdgcn_mfma_f32_16x16x32_bf16((a), (b), (c), 0, 0, 0)

constexpr double kPi = 3.14159265358979323846;
constexpr double ccos_s(double x) { double x2 = x * x, t = 1, s = 1; for (int i = 1; i < 12; ++i) { t *= -x2 / ((2 * i - 1) * (2 * i)); s += t; } return s; }
constexpr double csin_s(double x) { double x2 = x * x, t = x, s = x; for (int i = 1; i < 12; ++i) { t *= -x2 / ((2 * i) * (2 * i + 1)); s += t; } return s; }
constexpr double cos640(int m) {
  m %= 640; if (m > 320) m = 640 - m;
  bool neg = false; if (m > 160) { m = 320 - m; neg = true; }
  double v = (m <= 80) ? ccos_s(2 * kPi * m / 640) : csin_s(2 * kPi * (160 - m) / 640);
  return neg ? -v : v;
}
constexpr u16 to_bf16c(double v) { float f = (float)v; unsigned u = __builtin_bit_cast(unsigned, f); u += 0x7fffu + ((u >> 16) & 1u); return (u16)(u >> 16); }
struct C640 { u16 v[640]; };
constexpr C640 make_c640() { C640 c{}; for (int i = 0; i < 640; ++i) c.v[i] = to_bf16c(cos640(i)); return c; }
constexpr C640 COS640 = make_c640();

struct alignas(16) FRow { u16 v[20 * 512]; };
constexpr FRow make_frow(int dt) {
  FRow t{};
  for (int r = 0; r < 16; ++r) {
    const int d = dt * 16 + r;
    for (int kk = 0; kk < 640; ++kk) {
      const int m = (kk == 1) ? (d * 320) % 640 : (d * (kk >> 1) + 160 * (kk & 1)) % 640;
      t.v[(kk >> 3) * 128 + r * 8 + (kk & 7)] = COS640.v[m];
    }
  }
  return t;
}
template <int I> constexpr FRow FROW = make_frow(I);
struct alignas(16) FTab { FRow r[40]; };
template <int... I> constexpr FTab make_ftab(std::integer_sequence<int, I...>) { return FTab{{FROW<I>...}}; }
__device__ const FTab FT = make_ftab(std::make_integer_sequence<int, 40>{});

struct alignas(16) STab { u16 f64[4 * 4 * 512]; u16 f32[2 * 2 * 512]; };
constexpr STab make_stab() {
  STab t{};
  for (int w = 0; w < 4; ++w) for (int ks = 0; ks < 4; ++ks) for (int l = 0; l < 64; ++l) for (int j = 0; j < 8; ++j) {
    const int a = 16 * w + (l & 15), b = 16 * ks + 4 * (l >> 4) + (j >> 1);
    t.f64[((w * 4 + ks) * 64 + l) * 8 + j] = COS640.v[(10 * ((a * b) % 64) + ((j & 1) ? 480 : 0)) % 640];
  }
  for (int rt = 0; rt < 2; ++rt) for (int ty = 0; ty < 2; ++ty) for (int l = 0; l < 64; ++l) for (int j = 0; j < 8; ++j) {
    const int a = 16 * rt + (l & 15), g = l >> 4, b = (j < 4) ? 4 * g + j : 16 + 4 * g + j - 4;
    t.f32[((rt * 2 + ty) * 64 + l) * 8 + j] = COS640.v[(20 * ((a * b) % 32) + (ty ? 480 : 0)) % 640];
  }
  return t;
}
__device__ const STab ST = make_stab();

constexpr size_t WS_P   = 0;
constexpr size_t WS_Y   = WS_P + 2621440;
constexpr size_t WS_X1  = WS_Y + 2621440;
constexpr size_t WS_WT  = WS_X1 + 2621440;
constexpr size_t WS_KVP = WS_WT + 321 * 8192;
constexpr size_t WS_M   = WS_KVP + 983040;
constexpr size_t WS_G   = WS_M + 819200;

DI unsigned pk2(float a, float b) { f32x2 v = {a, b}; return __builtin_bit_cast(unsigned, __builtin_convertvector(v, bf16x2_t)); }
DI float bf_lo(unsigned u) { return __uint_as_float(u << 16); }
DI float bf_hi(unsigned u) { return __uint_as_float(u & 0xffff0000u); }
DI float rbf(float a) { return bf_lo(pk2(a, a)); }
constexpr float kEps = 1e-6f, kL2E = 1.4426950408889634f;
DI float phi_f(float a) {
  float e;
  const float u = __builtin_fmaf(a, kL2E, -kEps * kL2E);
  asm("v_exp_f32_e64 %0, %1 clamp" : "=v"(e) : "v"(u));
  return __builtin_fmaxf(a, kEps) + e;
}
DI bf16x8 pack8(const f32x4& a, const f32x4& b) {
  u32x4 p = {pk2(a[0], a[1]), pk2(a[2], a[3]), pk2(b[0], b[1]), pk2(b[2], b[3])};
  return __builtin_bit_cast(bf16x8, p);
}
#define WAIT_V(n) asm volatile("s_waitcnt vmcnt(" #n ")" ::: "memory")
#define BC(v) __builtin_bit_cast(bf16x8, (v))
#define WAIT_L0() asm volatile("s_waitcnt lgkmcnt(0)" ::: "memory")
#define BAR() __builtin_amdgcn_s_barrier()
#define SCHED() __builtin_amdgcn_sched_barrier(0)

constexpr int K1_STAGE = 36864;
constexpr int K1_LDS = 4 * K1_STAGE;
typedef LDS_AS char* lptr;
typedef __amdgpu_buffer_rsrc_t rsrc_t;
DI rsrc_t mk_rsrc(const void* p, unsigned bytes) { return __builtin_amdgcn_make_buffer_rsrc((void*)p, 0, bytes, 0x00020000); }

struct Frags { bf16x8 a[4]; bf16x8 b[5]; };

DI void k1_read(Frags& F, lptr sm, unsigned aoff, unsigned boff0, unsigned boff1) {
#pragma unroll
  for (int mt = 0; mt < 4; ++mt) F.a[mt] = *(LDS_AS bf16x8*)(sm + aoff + mt * 2048);
#pragma unroll
  for (int nt = 0; nt < 5; ++nt) {
    s16x4 lo = __builtin_amdgcn_ds_read_tr16_b64_v4i16((LDS_AS s16x4*)(sm + boff0 + nt * 256));
    s16x4 hi = __builtin_amdgcn_ds_read_tr16_b64_v4i16((LDS_AS s16x4*)(sm + boff1 + nt * 256));
    F.b[nt] = __builtin_shufflevector(lo, hi, 0, 1, 2, 3, 4, 5, 6, 7);
  }
}
template <bool SWAP> DI void k1_mma(f32x4 (&acc)[4][5], const Frags& F) {
#pragma unroll
  for (int mt = 0; mt < 4; ++mt)
#pragma unroll
    for (int nt = 0; nt < 5; ++nt)
      acc[mt][nt] = SWAP ? MFMA16(F.b[nt], F.a[mt], acc[mt][nt]) : MFMA16(F.a[mt], F.b[nt], acc[mt][nt]);
}
template <bool SWAP> DI void k1_main(f32x4 (&acc)[4][5], lptr sm, int lane, int wm, int wn) {
  const int r = lane & 15, g = lane >> 4, q = (lane & 15) >> 2, p = lane & 3;
  const unsigned a0 = (64 * wm + r) * 128 + ((g ^ (r >> 1)) * 16);
  const unsigned a1 = (64 * wm + r) * 128 + (((4 + g) ^ (r >> 1)) * 16);
  const unsigned b0 = 16384 + (g * 10 + 5 * wn) * 256 + ((q ^ ((g & 1) << 2)) * 32) + p * 8;
  const unsigned b1 = 16384 + (g * 10 + 5 * wn) * 256 + (((4 + q) ^ ((g & 1) << 2)) * 32) + p * 8;
  Frags F0, F1;
  BAR();
  SCHED();
  k1_read(F0, sm, a0, b0, b1);
  unsigned so = 0;
#pragma unroll 1
  for (int it = 0; it < 9; ++it) {
    k1_read(F1, sm, so + a1, so + b0 + 10240, so + b1 + 10240);
    SCHED();
    k1_mma<SWAP>(acc, F0);
    SCHED();
    so += K1_STAGE; if (so == 4 * K1_STAGE) so = 0;
    WAIT_L0();
    BAR();
    SCHED();
    k1_read(F0, sm, so + a0, so + b0, so + b1);
    SCHED();
    k1_mma<SWAP>(acc, F1);
    SCHED();
  }
  k1_read(F1, sm, K1_STAGE + a1, K1_STAGE + b0 + 10240, K1_STAGE + b1 + 10240);
  SCHED();
  k1_mma<SWAP>(acc, F0);
  k1_mma<SWAP>(acc, F1);
}

__global__ __launch_bounds__(512, 2) void k1_kernel(const u16* __restrict__ x, const u16* __restrict__ wqkv,
                                                    const u16* __restrict__ wr, const u16* __restrict__ wi,
                                                    const u16* __restrict__ gw, char* __restrict__ ws) {
  extern __shared__ __attribute__((aligned(16))) char smem[];
  lptr sm = (lptr)smem;
  const int tid = threadIdx.x, lane = tid & 63;
  const int wave = __builtin_amdgcn_readfirstlane(tid >> 6);
  const int b = blockIdx.x;
  const int xcd = b & 7, idx = b >> 3;
  const int rt = (xcd & 1) * 8 + (idx & 7);
  const int ct = (xcd >> 1) * 4 + (idx >> 3);

  if (wave >= 4) {
    const int lw = wave - 4;
    const bool dft = ct >= 12;
    const rsrc_t ra = mk_rsrc(x, 2048 * 1280);
    const rsrc_t rb = dft ? mk_rsrc(&FT, 819200) : mk_rsrc(wqkv, 640 * 3840);
    unsigned va[4], vb[5];
#pragma unroll
    for (int i = 0; i < 4; ++i) {
      const int row = 32 * lw + 8 * i + (lane >> 3);
      const int chunk = (lane & 7) ^ ((row >> 1) & 7);
      const int gr = dft ? 32 * (4 * (row & 15) + ((row >> 4) & 3)) + 2 * rt + (row >> 6) : 128 * rt + row;
      va[i] = gr * 1280 + chunk * 16;
    }
    const int bstep = dft ? 81920 : 64 * 3840;
#pragma unroll
    for (int i = 0; i < 5; ++i) {
      const int st = 4 * i + (lane >> 4);
      const int hi = st >= 10 ? 1 : 0;
      const int k = 16 * lw + 8 * hi + (((lane & 15) >> 1) ^ (hi << 2));
      const int cc = 2 * (st - 10 * hi) + (lane & 1);
      const int wn = cc >= 10 ? 1 : 0, c2 = cc - 10 * wn;
      const int colb = (ct < 4) ? 320 * ct + 16 * cc : 1280 + 160 * (ct - 4) + 80 * wn + 16 * c2 + (c2 >= 5 ? 1200 : 0);
      vb[i] = dft ? lw * 20480 + (k & 15) * 16 + (20 * (ct - 12) + cc) * 256 : k * 3840 + colb;
    }
    int soa = 0, sob = 0;
    auto issue = [&](int buf) {
      lptr base = sm + buf * K1_STAGE;
#pragma unroll
      for (int i = 0; i < 4; ++i)
        __builtin_amdgcn_raw_ptr_buffer_load_lds(ra, (LDS_AS void*)(base + (4 * lw + i) * 1024), 16, va[i], soa, 0, 0);
#pragma unroll
      for (int i = 0; i < 5; ++i)
        __builtin_amdgcn_raw_ptr_buffer_load_lds(rb, (LDS_AS void*)(base + 16384 + (5 * lw + i) * 1024), 16, vb[i], sob, 0, 0);
      soa += 128; sob += bstep;
    };
    issue(0); issue(1); issue(2); issue(3);
    WAIT_V(27); BAR();
    WAIT_V(18); BAR();
#pragma unroll 1
    for (int n = 1; n <= 6; ++n) {
      issue((n - 1) & 3);
      WAIT_V(18); BAR();
    }
    const int lt = tid - 256;
    const int grow = b * 8 + lw * 2 + (lane >> 5), l32 = lane & 31;
    u32x2 gx[5], gg[5];
    unsigned tr[3][4], ti[3][4];
    {
      const unsigned ox = grow * 1280 + l32 * 40, og = l32 * 40;
#pragma unroll
      for (int i = 0; i < 5; ++i) {
        asm volatile("global_load_dwordx2 %0, %1, %2 offset:%3" : "=v"(gx[i]) : "v"(ox), "s"(x), "i"(i * 8));
        asm volatile("global_load_dwordx2 %0, %1, %2 offset:%3" : "=v"(gg[i]) : "v"(og), "s"(gw), "i"(i * 8));
      }
#pragma unroll
      for (int i = 0; i < 3; ++i) {
        const int qd = min((i * 256 + b) * 256 + lt, 321 * 512 - 1);
        const int m = qd / 321, f = qd - m * 321;
        const int k1 = 16 * (m >> 7) + (m & 15), k2b = 16 * ((m >> 6) & 1) + 4 * ((m >> 4) & 3);
#pragma unroll
        for (int j = 0; j < 4; ++j) {
          const unsigned e = ((k1 + 64 * (k2b + j)) * 321 + f) * 2;
          asm volatile("global_load_ushort %0, %1, %2" : "=v"(tr[i][j]) : "v"(e), "s"(wr));
          asm volatile("global_load_short_d16_hi %0, %1, %2" : "=v"(ti[i][j]) : "v"(e), "s"(wi));
        }
      }
    }
    WAIT_V(43); BAR();
    WAIT_V(34); BAR();
    asm volatile("s_waitcnt vmcnt(0)"
                 : "+v"(gx[0]), "+v"(gx[1]), "+v"(gx[2]), "+v"(gx[3]), "+v"(gx[4]), "+v"(gg[0]), "+v"(gg[1]), "+v"(gg[2]), "+v"(gg[3]), "+v"(gg[4]),
                   "+v"(tr[0][0]), "+v"(tr[0][1]), "+v"(tr[0][2]), "+v"(tr[0][3]), "+v"(tr[1][0]), "+v"(tr[1][1]), "+v"(tr[1][2]), "+v"(tr[1][3]),
                   "+v"(tr[2][0]), "+v"(tr[2][1]), "+v"(tr[2][2]), "+v"(tr[2][3]));
    asm volatile(""
                 : "+v"(ti[0][0]), "+v"(ti[0][1]), "+v"(ti[0][2]), "+v"(ti[0][3]), "+v"(ti[1][0]), "+v"(ti[1][1]), "+v"(ti[1][2]), "+v"(ti[1][3]),
                   "+v"(ti[2][0]), "+v"(ti[2][1]), "+v"(ti[2][2]), "+v"(ti[2][3]));
#pragma unroll
    for (int i = 0; i < 3; ++i) {
      const int qd = (i * 256 + b) * 256 + lt;
      const int m = qd / 321, f = qd - m * 321;
      u32x4 o;
#pragma unroll
      for (int j = 0; j < 4; ++j) o[j] = (tr[i][j] & 0xffffu) | (ti[i][j] & 0xffff0000u);
      if (qd < 321 * 512) *(u32x4*)(ws + WS_WT + (size_t)f * 8192 + m * 16) = o;
    }
    {
      float s = 0.f;
#pragma unroll
      for (int i = 0; i < 5; ++i) {
        const u32x2 a = gx[i], c = gg[i];
        s += bf_lo(a[0]) * bf_lo(c[0]); s += bf_hi(a[0]) * bf_hi(c[0]);
        s += bf_lo(a[1]) * bf_lo(c[1]); s += bf_hi(a[1]) * bf_hi(c[1]);
      }
#pragma unroll
      for (int o = 16; o > 0; o >>= 1) s += __shfl_xor(s, o);
      const float gq = 1.f / (1.f + __expf(-s));
      if (l32 == 0) ((float*)(ws + WS_G))[grow] = gq;
    }
    return;
  }

  const int wm = wave >> 1, wn = wave & 1;
  const int r = lane & 15, g = lane >> 4;
  f32x4 acc[4][5];

  if (ct >= 4 && ct < 12) {
#pragma unroll
    for (int nt = 0; nt < 5; ++nt) {
      const float i0 = ((nt < 2) || (nt == 2 && r < 8)) ? kEps : 0.f;
#pragma unroll
      for (int mt = 0; mt < 4; ++mt) acc[mt][nt] = (f32x4){i0, i0, i0, i0};
    }
    k1_main<false>(acc, sm, lane, wm, wn);
    bf16x8 fr[2][5];
#pragma unroll
    for (int nt = 0; nt < 5; ++nt) {
#pragma unroll
      for (int mt = 0; mt < 4; ++mt)
#pragma unroll
        for (int j = 0; j < 4; ++j) {
          if (nt < 2) acc[mt][nt][j] = phi_f(acc[mt][nt][j]);
          if (nt == 2) { const float ph = phi_f(acc[mt][nt][j]); acc[mt][nt][j] = (r < 8) ? ph : acc[mt][nt][j]; }
        }
      fr[0][nt] = pack8(acc[0][nt], acc[1][nt]);
      fr[1][nt] = pack8(acc[2][nt], acc[3][nt]);
    }
    const bf16x8 ones = {0x3f80, 0x3f80, 0x3f80, 0x3f80, 0x3f80, 0x3f80, 0x3f80, 0x3f80};
    lptr kvw = sm + 2 * K1_STAGE + (wm * 2 + wn) * 3840;
    const int dump = 4 * 3840 - (wm * 2 + wn) * 3840 + lane * 16;
    auto pairf = [&](int ti, int tj) {
      f32x4 d = {0.f, 0.f, 0.f, 0.f};
      d = MFMA16(tj < 0 ? ones : fr[0][tj < 0 ? 0 : tj], fr[0][ti], d);
      d = MFMA16(tj < 0 ? ones : fr[1][tj < 0 ? 0 : tj], fr[1][ti], d);
      const int ur = 16 * ti + r, hk = ur >= 20 ? 1 : 0;
      const int uc = 16 * tj + 4 * g - 40, hv = uc >= 20 ? 1 : 0;
      const bool valid = tj < 0 ? (ur < 40 && g == 0) : (ur < 40 && uc >= 0 && hk == hv);
      const int off = hk * 1920 + (ur - 20 * hk) * 96 + (tj < 0 ? 80 : (uc - 20 * hv) * 4);
      *(LDS_AS f32x4*)(kvw + (valid ? off : dump)) = d;
    };
    pairf(0, 2); pairf(0, 3); pairf(1, 2); pairf(1, 3); pairf(1, 4); pairf(2, 3); pairf(2, 4);
    pairf(0, -1); pairf(1, -1); pairf(2, -1);
    WAIT_L0();
    BAR();
    f32x4* dst = (f32x4*)(ws + WS_KVP + ((size_t)rt * 32 + 4 * (ct - 4)) * 1920);
    LDS_AS f32x4* src = (LDS_AS f32x4*)(sm + 2 * K1_STAGE);
    dst[tid] = src[tid] + src[tid + 480];
    if (tid < 224) dst[tid + 256] = src[tid + 256] + src[tid + 736];
  } else {
    const float i0 = ct < 4 ? kEps : 0.f;
#pragma unroll
    for (int mt = 0; mt < 4; ++mt)
#pragma unroll
      for (int nt = 0; nt < 5; ++nt) acc[mt][nt] = (f32x4){i0, i0, i0, i0};
    k1_main<true>(acc, sm, lane, wm, wn);
    if (ct < 4) {
      char* dst = ws + WS_P + (size_t)(8 * rt + 4 * wm) * 20480 + (20 * ct + 10 * wn + (g >> 1)) * 256 + r * 16 + (g & 1) * 8;
#pragma unroll
      for (int mt = 0; mt < 4; ++mt)
#pragma unroll
        for (int nt = 0; nt < 5; ++nt) {
          const f32x4 v = acc[mt][nt];
          u32x2 o = {pk2(phi_f(v[0]), phi_f(v[1])), pk2(phi_f(v[2]), phi_f(v[3]))};
          *(u32x2*)(dst + mt * 20480 + nt * 512) = o;
        }
    } else {
      char* dst = ws + WS_X1 + (size_t)(80 * (ct - 12) + 40 * wn + 2 * g) * 8192 + (2 * rt + wm) * 256 + r * 16;
#pragma unroll
      for (int nt = 0; nt < 5; ++nt) {
        u32x4 o0 = {pk2(acc[0][nt][0], acc[0][nt][1]), pk2(acc[1][nt][0], acc[1][nt][1]), pk2(acc[2][nt][0], acc[2][nt][1]), pk2(acc[3][nt][0], acc[3][nt][1])};
        u32x4 o1 = {pk2(acc[0][nt][2], acc[0][nt][3]), pk2(acc[1][nt][2], acc[1][nt][3]), pk2(acc[2][nt][2], acc[2][nt][3]), pk2(acc[3][nt][2], acc[3][nt][3])};
        *(u32x4*)(dst + nt * 8 * 8192) = o0;
        *(u32x4*)(dst + nt * 8 * 8192 + 8192) = o1;
      }
    }
  }
}

constexpr int K2_NFFT = 328, K2_NM = 100, K2_NP = 256;

__global__ __launch_bounds__(256) void k2_kernel(const u16* __restrict__ wo, char* __restrict__ ws) {
  __shared__ __attribute__((aligned(16))) char smem[8192];
  const int tid = threadIdx.x, lane = tid & 63;
  const int wave = __builtin_amdgcn_readfirstlane(tid >> 6);
  const int b = blockIdx.x;
  const int r = lane & 15, g = lane >> 4;
  if (b < K2_NFFT) {
    const int i8 = b >> 3;
    int f = (i8 < 40) ? 4 * ((b & 7) + 8 * (i8 >> 2)) + (i8 & 3) : 320 + (b & 7);
    if (f > 320) return;
    const char* xp = ws + WS_X1 + (size_t)(f == 320 ? 0 : f) * 8192 + r * 256 + g * 16;
    u32x4 xa[2][4];
#pragma unroll
    for (int t = 0; t < 2; ++t)
#pragma unroll
      for (int ks = 0; ks < 4; ++ks) xa[t][ks] = *(const u32x4*)(xp + t * 4096 + ks * 64);
    u32x4 fre[4], fim[4];
#pragma unroll
    for (int ks = 0; ks < 4; ++ks) fre[ks] = *(const u32x4*)((const char*)ST.f64 + (wave * 4 + ks) * 1024 + lane * 16);
    u32x4 fc[2], fs[2], fn[2];
#pragma unroll
    for (int t = 0; t < 2; ++t) {
      fc[t] = *(const u32x4*)((const char*)ST.f32 + (t * 2 + 0) * 1024 + lane * 16);
      fs[t] = *(const u32x4*)((const char*)ST.f32 + (t * 2 + 1) * 1024 + lane * 16);
    }
    u32x4 wv[2];
#pragma unroll
    for (int t = 0; t < 2; ++t) wv[t] = *(const u32x4*)(ws + WS_WT + (size_t)f * 8192 + wave * 2048 + t * 1024 + lane * 16);
    float tc[2][4], tsn[2][4];
#pragma unroll
    for (int t = 0; t < 2; ++t)
#pragma unroll
      for (int j = 0; j < 4; ++j) {
        const float a = (float)((16 * t + 4 * g + j) * (16 * wave + r)) * (1.f / 2048.f);
        tc[t][j] = __builtin_amdgcn_cosf(a);
        tsn[t][j] = __builtin_amdgcn_sinf(a);
      }
    if (f == 0 || f == 320) {
#pragma unroll
      for (int t = 0; t < 2; ++t)
#pragma unroll
        for (int ks = 0; ks < 4; ++ks)
#pragma unroll
          for (int e = 0; e < 4; ++e) xa[t][ks][e] = (f == 0) ? (xa[t][ks][e] & 0xffffu) : (xa[t][ks][e] >> 16);
    }
#pragma unroll
    for (int ks = 0; ks < 4; ++ks)
#pragma unroll
      for (int e = 0; e < 4; ++e) fim[ks][e] = __builtin_amdgcn_alignbit(fre[ks][e], fre[ks][e], 16) ^ 0x8000u;
#pragma unroll
    for (int t = 0; t < 2; ++t)
#pragma unroll
      for (int e = 0; e < 4; ++e) fn[t][e] = fs[t][e] ^ 0x80008000u;
    const f32x4 z4 = {0.f, 0.f, 0.f, 0.f};
    f32x4 re[2], im[2];
#pragma unroll
    for (int t = 0; t < 2; ++t) {
      re[t] = z4; im[t] = z4;
#pragma unroll
      for (int ks = 0; ks < 4; ++ks) {
        re[t] = MFMA16(BC(xa[t][ks]), BC(fre[ks]), re[t]);
        im[t] = MFMA16(BC(xa[t][ks]), BC(fim[ks]), im[t]);
      }
    }
#pragma unroll
    for (int t = 0; t < 2; ++t)
#pragma unroll
      for (int j = 0; j < 4; ++j) {
        const float a = re[t][j], c = im[t][j];
        re[t][j] = a * tc[t][j] + c * tsn[t][j];
        im[t][j] = c * tc[t][j] - a * tsn[t][j];
      }
    bf16x8 bre = pack8(re[0], re[1]), bim = pack8(im[0], im[1]);
#pragma unroll
    for (int t = 0; t < 2; ++t) {
      re[t] = MFMA16(BC(fc[t]), bre, z4); re[t] = MFMA16(BC(fs[t]), bim, re[t]);
      im[t] = MFMA16(BC(fc[t]), bim, z4); im[t] = MFMA16(BC(fn[t]), bre, im[t]);
    }
    const float sc = ((f == 0 || f == 320) ? 1.f : 2.f) / (2048.f * 640.f);
#pragma unroll
    for (int t = 0; t < 2; ++t)
#pragma unroll
      for (int j = 0; j < 4; ++j) {
        const float a = re[t][j] * sc, c = im[t][j] * sc, wre = bf_lo(wv[t][j]), wim = bf_hi(wv[t][j]);
        re[t][j] = a * wre - c * wim;
        im[t][j] = a * wim + c * wre;
      }
    bre = pack8(re[0], re[1]); bim = pack8(im[0], im[1]);
#pragma unroll
    for (int t = 0; t < 2; ++t) {
      re[t] = MFMA16(BC(fc[t]), bre, z4); re[t] = MFMA16(BC(fn[t]), bim, re[t]);
      im[t] = MFMA16(BC(fc[t]), bim, z4); im[t] = MFMA16(BC(fs[t]), bre, im[t]);
    }
#pragma unroll
    for (int t = 0; t < 2; ++t)
#pragma unroll
      for (int j = 0; j < 4; ++j) {
        const float a = re[t][j], c = im[t][j];
        const float ur = a * tc[t][j] - c * tsn[t][j];
        const float ui = -(c * tc[t][j] + a * tsn[t][j]);
        const int s2 = 16 * t + 4 * g + j;
        *(unsigned*)(smem + s2 * 256 + (((4 * wave + (r >> 2)) ^ (s2 & 15)) * 16) + (r & 3) * 4) = pk2(ur, ui);
      }
    __syncthreads();
    char* yp = ws + WS_Y + (size_t)(2 * (16 * wave + 4 * g)) * 20480 + (f == 320 ? 2 : (f >> 2) * 256 + (f & 3) * 4) + r * 16;
#pragma unroll
    for (int t = 0; t < 2; ++t) {
      f32x4 yr = z4, yi = z4;
#pragma unroll
      for (int ks = 0; ks < 4; ++ks) {
        const bf16x8 u = *(const bf16x8*)(smem + (16 * t + r) * 256 + (((4 * ks + g) ^ r) * 16));
        yr = MFMA16(BC(fre[ks]), u, yr);
        yi = MFMA16(BC(fim[ks]), u, yi);
      }
#pragma unroll
      for (int j = 0; j < 4; ++j) {
        const unsigned o = pk2(yr[j], -yi[j]);
        char* p = yp + (2 * j + t) * 20480;
        if (f == 0 || f == 320) *(u16*)p = (u16)o;
        else *(unsigned*)p = o;
      }
    }
  } else if (b < K2_NFFT + K2_NM) {
    const int mb = b - K2_NFFT, ks = mb / 5, cg = mb - ks * 5;
    const int hf = (32 * ks) / 20, e0 = 20 * hf;
    u32x4 bw[2][2];
#pragma unroll
    for (int n = 0; n < 2; ++n)
#pragma unroll
      for (int s = 0; s < 2; ++s)
#pragma unroll
        for (int e = 0; e < 4; ++e) {
          const int col = 128 * cg + 32 * wave + 16 * n + r;
          const int r0 = min(e0 + 32 * s + 8 * g + 2 * e, 639), r1 = min(e0 + 32 * s + 8 * g + 2 * e + 1, 639);
          bw[n][s][e] = (unsigned)wo[r0 * 640 + col] | ((unsigned)wo[r1 * 640 + col] << 16);
        }
    f32x4 sum = {0.f, 0.f, 0.f, 0.f};
    const int kl = tid / 5, qd = tid - kl * 5;
    const int k = 32 * ks + kl, h = k / 20, d = k - 20 * h;
    if (tid < 160) {
      const char* p = ws + WS_KVP + (size_t)h * 1920 + d * 96 + qd * 16;
      f32x4 v[16];
#pragma unroll
      for (int i = 0; i < 16; ++i) v[i] = *(const f32x4*)(p + (size_t)i * 61440);
#pragma unroll
      for (int i = 0; i < 16; ++i) sum += v[i];
    }
    for (int i = tid; i < 32 * 9; i += 256) ((u32x4*)smem)[i] = (u32x4){0u, 0u, 0u, 0u};
    __syncthreads();
    if (tid < 160) {
      u32x2 o = {pk2(sum[0], sum[1]), pk2(sum[2], sum[3])};
      *(u32x2*)(smem + kl * 144 + (20 * (h - hf) + 4 * qd) * 2) = o;
    }
    __syncthreads();
    bf16x8 a[2][2];
#pragma unroll
    for (int t = 0; t < 2; ++t)
#pragma unroll
      for (int s = 0; s < 2; ++s) a[t][s] = *(const bf16x8*)(smem + (8 * (r >> 2) + (r & 3) + 4 * t) * 144 + s * 64 + g * 16);
    const f32x4 z4 = {0.f, 0.f, 0.f, 0.f};
#pragma unroll
    for (int n = 0; n < 2; ++n) {
      f32x4 d1 = MFMA16(a[0][0], BC(bw[n][0]), z4); d1 = MFMA16(a[0][1], BC(bw[n][1]), d1);
      f32x4 d2 = MFMA16(a[1][0], BC(bw[n][0]), z4); d2 = MFMA16(a[1][1], BC(bw[n][1]), d2);
      *(bf16x8*)(ws + WS_M + (size_t)(8 * cg + 2 * wave + n) * 20480 + ks * 1024 + lane * 16) = pack8(d1, d2);
    }
  } else {
    const int pb = b - K2_NFFT - K2_NM, rb = pb >> 4, hp = pb & 15;
    const int row = 128 * rb + (tid & 127), hl = tid >> 7, h = 2 * hp + hl;
    char* pp = ws + WS_P + (size_t)(row >> 4) * 20480 + (row & 15) * 16;
    u32x2 q[5];
#pragma unroll
    for (int i = 0; i < 5; ++i) { const int k = 20 * h + 4 * i; q[i] = *(const u32x2*)(pp + (k >> 3) * 256 + (k & 7) * 2); }
    float* zs = (float*)smem;
    if (tid < 40) {
      const int hh = 2 * hp + tid / 20, d = tid % 20;
      const char* p = ws + WS_KVP + (size_t)hh * 1920 + d * 96 + 80;
      float v[16];
#pragma unroll
      for (int i = 0; i < 16; ++i) v[i] = *(const float*)(p + (size_t)i * 61440);
      float s = 0.f;
#pragma unroll
      for (int i = 0; i < 16; ++i) s += v[i];
      zs[tid] = rbf(s);
    }
    __syncthreads();
    float den = 0.f;
#pragma unroll
    for (int i = 0; i < 5; ++i) {
      const f32x4 z = *(const f32x4*)(zs + 20 * hl + 4 * i);
      den += bf_lo(q[i][0]) * z[0]; den += bf_hi(q[i][0]) * z[1]; den += bf_lo(q[i][1]) * z[2]; den += bf_hi(q[i][1]) * z[3];
    }
    den = rbf(rbf(den) + 1e-6f);
    const float inv = 1.f / den;
#pragma unroll
    for (int i = 0; i < 5; ++i) {
      const int k = 20 * h + 4 * i;
      u32x2 o = {pk2(bf_lo(q[i][0]) * inv, bf_hi(q[i][0]) * inv), pk2(bf_lo(q[i][1]) * inv, bf_hi(q[i][1]) * inv)};
      *(u32x2*)(pp + (k >> 3) * 256 + (k & 7) * 2) = o;
    }
  }
}

constexpr int K3_LDS = 4 * 64 * 84 * 4 + 256;

__global__ __launch_bounds__(256, 1) void k3_kernel(const char* __restrict__ ws, u16* __restrict__ out) {
  extern __shared__ __attribute__((aligned(16))) char smem[];
  const int tid = threadIdx.x, lane = tid & 63;
  const int wave = __builtin_amdgcn_readfirstlane(tid >> 6);
  const int b = blockIdx.x, xcd = b & 7, idx = b >> 3;
  const int rt = (xcd & 3) * 8 + (idx & 7);
  const int ct = (xcd >> 2) * 4 + (idx >> 3);
  const int r = lane & 15, g = lane >> 4;
  const rsrc_t ra = mk_rsrc(ws + (wave >= 2 ? WS_Y : WS_P) + (size_t)(4 * rt) * 20480 + (wave & 1) * 10240, 4 * 20480);
  const rsrc_t rb = mk_rsrc((wave >= 2 ? (const char*)&FT : ws + WS_M) + (size_t)(5 * ct) * 20480 + (wave & 1) * 10240, 5 * 20480);
  const unsigned vo = lane * 16;
  f32x4 acc[4][5];
#pragma unroll
  for (int mt = 0; mt < 4; ++mt)
#pragma unroll
    for (int nt = 0; nt < 5; ++nt) acc[mt][nt] = (f32x4){0.f, 0.f, 0.f, 0.f};
  u32x4 fa[10][4], fb[10][5];
  constexpr int PF = 6;
  auto ld = [&](int ks) {
#pragma unroll
    for (int nt = 0; nt < 5; ++nt) fb[ks][nt] = __builtin_amdgcn_raw_buffer_load_b128(rb, vo + (ks & 3) * 1024, nt * 20480 + (ks >> 2) * 4096, 0);
#pragma unroll
    for (int mt = 0; mt < 4; ++mt) fa[ks][mt] = __builtin_amdgcn_raw_buffer_load_b128(ra, vo + (ks & 3) * 1024, mt * 20480 + (ks >> 2) * 4096, 0);
  };
#pragma unroll
  for (int ks = 0; ks < PF; ++ks) ld(ks);
  const float gq0 = ((const float*)(ws + WS_G))[64 * rt + lane];
  SCHED();
#pragma unroll
  for (int ks = 0; ks < 10; ++ks) {
#pragma unroll
    for (int mt = 0; mt < 4; ++mt)
#pragma unroll
      for (int nt = 0; nt < 5; ++nt) acc[mt][nt] = MFMA16(BC(fb[ks][nt]), BC(fa[ks][mt]), acc[mt][nt]);
    SCHED();
    if (ks + PF < 10) ld(ks + PF);
    SCHED();
  }
  LDS_AS float* red = (LDS_AS float*)smem;
#pragma unroll
  for (int mt = 0; mt < 4; ++mt)
#pragma unroll
    for (int nt = 0; nt < 5; ++nt) *(LDS_AS f32x4*)(red + wave * 5376 + (16 * mt + r) * 84 + 16 * nt + 4 * g) = acc[mt][nt];
  if (wave == 0) red[4 * 5376 + lane] = gq0;
  __syncthreads();
#pragma unroll
  for (int i = 0; i < 5; ++i) {
    const int it = i * 256 + tid, row = it / 20, c4 = it - row * 20;
    LDS_AS float* p = red + row * 84 + 4 * c4;
    const f32x4 a = *(LDS_AS f32x4*)p + *(LDS_AS f32x4*)(p + 5376);
    const f32x4 bb = *(LDS_AS f32x4*)(p + 2 * 5376) + *(LDS_AS f32x4*)(p + 3 * 5376);
    const float gq = red[4 * 5376 + row];
    const f32x4 o = bb + gq * (a - bb);
    u32x2 v = {pk2(o[0], o[1]), pk2(o[2], o[3])};
    *(u32x2*)(out + (size_t)(64 * rt + row) * 640 + 80 * ct + 4 * c4) = v;
  }
}

extern "C" void kernel_launch(void* const* d_in, const int* in_sizes, int n_in, void* d_out, int out_size, void* d_ws,
                              size_t ws_size, hipStream_t stream) {
  const u16* x = (const u16*)d_in[0];
  const u16* wqkv = (const u16*)d_in[1];
  const u16* wo = (const u16*)d_in[2];
  const u16* wr = (const u16*)d_in[4];
  const u16* wi = (const u16*)d_in[5];
  const u16* gw = (const u16*)d_in[6];
  char* ws = (char*)d_ws;
  (void)hipFuncSetAttribute((const void*)k1_kernel, hipFuncAttributeMaxDynamicSharedMemorySize, K1_LDS);
  (void)hipFuncSetAttribute((const void*)k3_kernel, hipFuncAttributeMaxDynamicSharedMemorySize, K3_LDS);
  k1_kernel<<<256, 512, K1_LDS, stream>>>(x, wqkv, wr, wi, gw, ws);
  k2_kernel<<<K2_NFFT + K2_NM + K2_NP, 256, 0, stream>>>(wo, ws);
  k3_kernel<<<256, 256, K3_LDS, stream>>>(ws, (u16*)d_out);
}
```

```cpp
#include <hip/hip_runtime.h>
#include <hip/hip_bf16.h>
#include <utility>

typedef unsigned short u16;
typedef __attribute__((ext_vector_type(8))) short bf16x8;
typedef __attribute__((ext_vector_type(4))) short s16x4;
typedef __attribute__((ext_vector_type(4))) float f32x4;
typedef __attribute__((ext_vector_type(2))) float f32x2;
typedef __attribute__((ext_vector_type(2))) __bf16 bf16x2_t;
typedef __attribute__((ext_vector_type(4))) unsigned u32x4;
typedef __attribute__((ext_vector_type(2))) unsigned u32x2;
#define LDS_AS __attribute__((address_space(3)))
#define DI __device__ __forceinline__
#define MFMA16(a, b, c) __builtin_amdgcn_mfma_f32_16x16x32_bf16((a), (b), (c), 0, 0, 0)
typedef LDS_AS char* lptr;
typedef __amdgpu_buffer_rsrc_t rsrc_t;

#define ST_AUX 16
#define OUT_AUX 0
#define K1_ROT 1

constexpr double kPi = 3.14159265358979323846;
constexpr double ccos_s(double x) { double x2 = x * x, t = 1, s = 1; for (int i = 1; i < 12; ++i) { t *= -x2 / ((2 * i - 1) * (2 * i)); s += t; } return s; }
constexpr double csin_s(double x) { double x2 = x * x, t = x, s = x; for (int i = 1; i < 12; ++i) { t *= -x2 / ((2 * i) * (2 * i + 1)); s += t; } return s; }
constexpr double cos640(int m) {
  m %= 640; if (m > 320) m = 640 - m;
  bool neg = false; if (m > 160) { m = 320 - m; neg = true; }
  double v = (m <= 80) ? ccos_s(2 * kPi * m / 640) : csin_s(2 * kPi * (160 - m) / 640);
  return neg ? -v : v;
}
constexpr u16 to_bf16c(double v) { float f = (float)v; unsigned u = __builtin_bit_cast(unsigned, f); u += 0x7fffu + ((u >> 16) & 1u); return (u16)(u >> 16); }
struct C640 { u16 v[640]; };
constexpr C640 make_c640() { C640 c{}; for (int i = 0; i < 640; ++i) c.v[i] = to_bf16c(cos640(i)); return c; }
constexpr C640 COS640 = make_c640();

struct alignas(128) FRow { u16 v[16 * 640]; };
constexpr FRow make_frow(int dt) {
  FRow t{};
  for (int r = 0; r < 16; ++r) {
    const int d = dt * 16 + r;
    for (int kk = 0; kk < 640; ++kk) {
      const int m = (kk == 1) ? (d * 320) % 640 : (d * (kk >> 1) + 160 * (kk & 1)) % 640;
      t.v[r * 640 + kk] = COS640.v[m];
    }
  }
  return t;
}
template <int I> constexpr FRow FROW = make_frow(I);
struct alignas(128) FTab { FRow r[40]; };
template <int... I> constexpr FTab make_ftab(std::integer_sequence<int, I...>) { return FTab{{FROW<I>...}}; }
__device__ const FTab FT = make_ftab(std::make_integer_sequence<int, 40>{});

struct alignas(128) GRow { u16 v[10 * 3 * 512]; };
constexpr GRow make_grow(int id) {
  GRow t{};
  const int ct = id >> 1, od = id & 1;
  for (int ks = 0; ks < 10; ++ks) for (int nt = 0; nt < 3; ++nt) for (int l = 0; l < 64; ++l) {
    const int ci = 16 * nt + (l & 15), d = 40 * ct + ci;
    for (int j = 0; j < 8; ++j) {
      const int f = 32 * ks + 8 * (l >> 4) + j;
      t.v[((ks * 3 + nt) * 64 + l) * 8 + j] = (ci <= 40) ? COS640.v[(f * d + (od ? 480 : 0)) % 640] : (u16)((od && ci == 41 && f == 0) ? 0x3f80 : 0);
    }
  }
  return t;
}
template <int I> constexpr GRow GROW = make_grow(I);
struct alignas(128) GTab { GRow r[16]; };
template <int... I> constexpr GTab make_gtab(std::integer_sequence<int, I...>) { return GTab{{GROW<I>...}}; }
__device__ const GTab GT = make_gtab(std::make_integer_sequence<int, 16>{});

constexpr size_t WS_P   = 0;
constexpr size_t WS_Y   = WS_P + 2621440;
constexpr size_t WS_X1  = WS_Y + 2621440;
constexpr size_t WS_WT  = WS_X1 + 2621440;
constexpr size_t WS_KVP = WS_WT + 352 * 8192;
constexpr size_t WS_M   = WS_KVP + 983040;
constexpr size_t WS_G   = WS_M + 819200;
constexpr size_t WS_END = WS_G + 8192;

DI rsrc_t mk_rsrc(const void* p, unsigned bytes) { return __builtin_amdgcn_make_buffer_rsrc((void*)p, 0, bytes, 0x00020000); }
DI unsigned pk2(float a, float b) { f32x2 v = {a, b}; return __builtin_bit_cast(unsigned, __builtin_convertvector(v, bf16x2_t)); }
DI float bf_lo(unsigned u) { return __uint_as_float(u << 16); }
DI float bf_hi(unsigned u) { return __uint_as_float(u & 0xffff0000u); }
DI float rbf(float a) { return bf_lo(pk2(a, a)); }
constexpr float kEps = 1e-6f, kL2E = 1.4426950408889634f;
DI float phi_f(float a) {
  float e;
  const float u = __builtin_fmaf(a, kL2E, -kEps * kL2E);
  asm("v_exp_f32_e64 %0, %1 clamp" : "=v"(e) : "v"(u));
  return __builtin_fmaxf(a, kEps) + e;
}
DI bf16x8 pack8(const f32x4& a, const f32x4& b) {
  u32x4 p = {pk2(a[0], a[1]), pk2(a[2], a[3]), pk2(b[0], b[1]), pk2(b[2], b[3])};
  return __builtin_bit_cast(bf16x8, p);
}
DI u32x4 widen(u32x2 a, u32x2 b) {
  auto x = __builtin_amdgcn_permlane16_swap(a[0], b[0], false, false);
  auto y = __builtin_amdgcn_permlane16_swap(a[1], b[1], false, false);
  return (u32x4){x[0], y[0], x[1], y[1]};
}
#define WAIT_V(n) asm volatile("s_waitcnt vmcnt(" #n ")" ::: "memory")
#define BC(v) __builtin_bit_cast(bf16x8, (v))
#define WAIT_L0() __builtin_amdgcn_s_waitcnt(0xC07F)
#define BAR() __builtin_amdgcn_s_barrier()
#define SCHED() __builtin_amdgcn_sched_barrier(0)
#define DMA16(rs, dst, vo, so) __builtin_amdgcn_raw_ptr_buffer_load_lds((rs), (LDS_AS void*)(dst), 16, (vo), (so), 0, 0)
#define TRREAD(p) __builtin_amdgcn_ds_read_tr16_b64_v4i16((LDS_AS s16x4*)(p))
#define ST16(v, rs, vo, so) __builtin_amdgcn_raw_buffer_store_b128((v), (rs), (vo), (so), ST_AUX)

#define TS_DECL
#define TS(i)
#define TS_FLUSH(kern)
#define TS_ARG
#define TS_PASS

constexpr int K1_STAGE = 40960;
constexpr int K1_LDS = 4 * K1_STAGE;

struct Frags { bf16x8 a[4]; bf16x8 b[5]; };

DI void k1_read(Frags& F, lptr sm, unsigned aoff, const unsigned (&bo)[5], unsigned so) {
#pragma unroll
  for (int mt = 0; mt < 4; ++mt) F.a[mt] = *(LDS_AS bf16x8*)(sm + aoff + mt * 2048);
#pragma unroll
  for (int nt = 0; nt < 5; ++nt) {
    s16x4 lo = TRREAD(sm + bo[nt] + so);
    s16x4 hi = TRREAD(sm + bo[nt] + so + 512);
    F.b[nt] = __builtin_shufflevector(lo, hi, 0, 1, 2, 3, 4, 5, 6, 7);
  }
}
template <bool SWAP> DI void k1_mma(f32x4 (&acc)[4][5], const Frags& F) {
#pragma unroll
  for (int mt = 0; mt < 4; ++mt)
#pragma unroll
    for (int nt = 0; nt < 5; ++nt)
      acc[mt][nt] = SWAP ? MFMA16(F.b[nt], F.a[mt], acc[mt][nt]) : MFMA16(F.a[mt], F.b[nt], acc[mt][nt]);
}
DI void k1_interleave() {
#pragma unroll
  for (int i = 0; i < 14; ++i) {
    __builtin_amdgcn_sched_group_barrier(0x008, 1, 0);
    __builtin_amdgcn_sched_group_barrier(0x100, 1, 0);
  }
  __builtin_amdgcn_sched_group_barrier(0x008, 6, 0);
}
template <bool SWAP> DI void k1_main(f32x4 (&acc)[4][5], lptr sm, int lane, int wm, unsigned (&bo)[5] TS_ARG) {
  const int r = lane & 15, g = lane >> 4;
  unsigned a0 = (64 * wm + r) * 128 + ((g ^ (r >> 1)) * 16);
  unsigned a1 = (64 * wm + r) * 128 + (((4 + g) ^ (r >> 1)) * 16);
  Frags F0, F1;
#pragma unroll
  for (int mt = 0; mt < 4; ++mt)
#pragma unroll
    for (int nt = 0; nt < 5; ++nt) asm volatile("" : "+v"(acc[mt][nt]));
  TS(1);
  BAR();
  TS(2);
  SCHED();
  k1_read(F0, sm, a0, bo, 0);
  WAIT_L0();
  SCHED();
#pragma unroll 1
  for (int it = 0; it < 9; ++it) {
    k1_read(F1, sm, a1, bo, 12288);
    k1_mma<SWAP>(acc, F0);
    k1_interleave();
    SCHED();
    const unsigned inc = ((it & 3) == 3) ? (unsigned)(-3 * K1_STAGE) : (unsigned)K1_STAGE;
    a0 += inc; a1 += inc;
#pragma unroll
    for (int nt = 0; nt < 5; ++nt) bo[nt] += inc;
    WAIT_L0();
    BAR();
    SCHED();
    k1_read(F0, sm, a0, bo, 0);
    k1_mma<SWAP>(acc, F1);
    k1_interleave();
    SCHED();
    WAIT_L0();
    SCHED();
  }
  k1_read(F1, sm, a1, bo, 12288);
  k1_mma<SWAP>(acc, F0);
  k1_interleave();
  SCHED();
  k1_mma<SWAP>(acc, F1);
}

__global__ __launch_bounds__(512, 2) void k1_kernel(const u16* __restrict__ x, const u16* __restrict__ wqkv,
                                                    const u16* __restrict__ wr, const u16* __restrict__ wi,
                                                    const u16* __restrict__ gw, char* __restrict__ ws) {
  extern __shared__ __attribute__((aligned(16))) char smem[];
  lptr sm = (lptr)smem;
  TS_DECL;
  TS(0);
  const int tid = threadIdx.x, lane = tid & 63;
  const int wave = __builtin_amdgcn_readfirstlane(tid >> 6);
  const int b = blockIdx.x;
  const int xcd = b & 7, idx = b >> 3;
  const int rt = (xcd & 1) * 8 + (idx & 7);
  const int ct = (xcd >> 1) * 4 + (idx >> 3);
  const bool dft = ct >= 12, iskv = ct >= 4 && ct < 12;
  const int c0 = iskv ? 80 * (ct - 4) : 160 * (ct & 3);
  const int L0 = c0 >> 6, p0 = (c0 & 63) >> 4;
  const int xs = (p0 == 0 || p0 == 3) ? 1 : 2;
  const int bigB = p0 >= 2 ? 1 : 0;

  if (wave < 4) {
    const int lw = wave;
    const rsrc_t ra = mk_rsrc(x, 2048 * 1280);
    const rsrc_t rb = dft ? mk_rsrc(&FT, 819200) : mk_rsrc(wqkv, 640 * 3840);
    const int rot = K1_ROT ? (((idx & 7) * 5 + (idx >> 3) * 10) >> 2) % 10 : 0;
    const int rs = dft ? 1280 : 3840;
    unsigned va[4], vb[6];
#pragma unroll
    for (int i = 0; i < 4; ++i) {
      const int row = 32 * lw + 8 * i + (lane >> 3);
      const int chunk = (lane & 7) ^ ((row >> 1) & 7);
      const int gr = dft ? 32 * (16 * (rt >> 2) + 8 * (row >> 6) + 4 * ((row >> 3) & 1) + ((row >> 4) & 3)) + 8 * (rt & 3) + (row & 7) : 128 * rt + row;
      va[i] = gr * 1280 + chunk * 16;
    }
    {
      const int row = lane >> 3, hf = lane & 1;
#pragma unroll
      for (int kb = 0; kb < 2; ++kb) {
        const int pair = ((lane & 7) >> 1) ^ (((row >> 1) & 1) | (kb << 1));
        const unsigned rowb = (16 * lw + 8 * kb + row) * rs;
        if (!iskv) {
#pragma unroll
          for (int pl = 0; pl < 3; ++pl) vb[kb * 3 + pl] = rowb + (L0 + pl) * 128 + pair * 32 + hf * 16;
        } else {
          vb[kb * 3 + 0] = rowb + 1280 + (L0 + bigB) * 128 + pair * 32 + hf * 16;
          vb[kb * 3 + 1] = rowb + 2560 + (L0 + bigB) * 128 + pair * 32 + (hf ^ 1) * 16;
          const bool kown = bigB ? (pair >= p0) : (pair <= p0);
          vb[kb * 3 + 2] = rowb + (L0 + 1 - bigB) * 128 + (kown ? 1280 + pair * 32 + hf * 16 : 2560 + (pair ^ xs) * 32 + (hf ^ 1) * 16);
        }
      }
    }
    int st = rot;
    auto issue = [&](int buf) {
      lptr base = sm + buf * K1_STAGE;
      const int soa = st * 128, sob = st * 64 * rs;
#pragma unroll
      for (int i = 0; i < 4; ++i) DMA16(ra, base + (4 * lw + i) * 1024, va[i], soa);
#pragma unroll
      for (int i = 0; i < 6; ++i) DMA16(rb, base + 16384 + (6 * lw + i) * 1024, vb[i], sob);
      st = (st == 9) ? 0 : st + 1;
    };
    TS(1);
    issue(0); issue(1); issue(2); issue(3);
    TS(2);
    WAIT_V(30); TS(3); BAR();
    WAIT_V(20); BAR();
#pragma unroll 1
    for (int n = 1; n <= 6; ++n) {
      issue((n - 1) & 3);
      WAIT_V(20); BAR();
    }
    const int grow = b * 8 + lw * 2 + (lane >> 5), l32 = lane & 31;
    const int tw = min(b * 4 + lw, 703);
    const int mo = tw / 11, fg = tw - mo * 11;
    const int tf = min(32 * fg + l32, 320);
    u32x2 gx[5], gg[5];
    unsigned tr[4][4], ti[4][4];
    {
      const unsigned ox = grow * 1280 + l32 * 40, og = l32 * 40;
#pragma unroll
      for (int i = 0; i < 5; ++i) {
        asm volatile("global_load_dwordx2 %0, %1, %2 offset:%3" : "=v"(gx[i]) : "v"(ox), "s"(x), "i"(i * 8));
        asm volatile("global_load_dwordx2 %0, %1, %2 offset:%3" : "=v"(gg[i]) : "v"(og), "s"(gw), "i"(i * 8));
      }
      const int k1b = 16 * (mo >> 4) + 8 * (mo & 1) + 4 * (lane >> 5), k2b = 16 * ((mo >> 3) & 1) + 4 * ((mo >> 1) & 3);
      const unsigned e0 = ((k1b + 64 * k2b) * 321 + tf) * 2;
#pragma unroll
      for (int j = 0; j < 4; ++j) {
        const unsigned e = e0 + j * 64 * 642;
#pragma unroll
        for (int i = 0; i < 4; ++i) {
          asm volatile("global_load_ushort %0, %1, %2 offset:%3" : "=v"(tr[i][j]) : "v"(e), "s"(wr), "i"(i * 642));
          asm volatile("global_load_short_d16_hi %0, %1, %2 offset:%3" : "=v"(ti[i][j]) : "v"(e), "s"(wi), "i"(i * 642));
        }
      }
    }
    WAIT_V(52); BAR();
    WAIT_V(42); TS(4); BAR();
    asm volatile("s_waitcnt vmcnt(0)"
                 : "+v"(gx[0]), "+v"(gx[1]), "+v"(gx[2]), "+v"(gx[3]), "+v"(gx[4]), "+v"(gg[0]), "+v"(gg[1]), "+v"(gg[2]), "+v"(gg[3]), "+v"(gg[4]),
                   "+v"(tr[0][0]), "+v"(tr[0][1]), "+v"(tr[0][2]), "+v"(tr[0][3]), "+v"(tr[1][0]), "+v"(tr[1][1]), "+v"(tr[1][2]), "+v"(tr[1][3]),
                   "+v"(tr[2][0]), "+v"(tr[2][1]), "+v"(tr[2][2]), "+v"(tr[2][3]), "+v"(tr[3][0]), "+v"(tr[3][1]), "+v"(tr[3][2]), "+v"(tr[3][3]));
    asm volatile(""
                 : "+v"(ti[0][0]), "+v"(ti[0][1]), "+v"(ti[0][2]), "+v"(ti[0][3]), "+v"(ti[1][0]), "+v"(ti[1][1]), "+v"(ti[1][2]), "+v"(ti[1][3]),
                   "+v"(ti[2][0]), "+v"(ti[2][1]), "+v"(ti[2][2]), "+v"(ti[2][3]), "+v"(ti[3][0]), "+v"(ti[3][1]), "+v"(ti[3][2]), "+v"(ti[3][3]));
    TS(5);
    {
      lptr tb = sm + 3 * K1_STAGE + lw * 4608;
#pragma unroll
      for (int i = 0; i < 4; ++i) {
        u32x4 o;
#pragma unroll
        for (int j = 0; j < 4; ++j) o[j] = (tr[i][j] & 0xffffu) | (ti[i][j] & 0xffff0000u);
        *(LDS_AS u32x4*)(tb + l32 * 144 + (4 * (lane >> 5) + i) * 16) = o;
      }
      const rsrc_t rw = mk_rsrc(ws + WS_WT, (b * 4 + lw < 704) ? 321 * 8192 : 0);
#pragma unroll
      for (int i = 0; i < 4; ++i) {
        const int fl = 8 * i + (lane >> 3);
        const u32x4 o = *(LDS_AS u32x4*)(tb + fl * 144 + (lane & 7) * 16);
        ST16(o, rw, (32 * fg + fl) * 8192 + (lane & 7) * 16, mo * 128);
      }
    }
    {
      float s = 0.f;
#pragma unroll
      for (int i = 0; i < 5; ++i) {
        const u32x2 a = gx[i], c = gg[i];
        s += bf_lo(a[0]) * bf_lo(c[0]); s += bf_hi(a[0]) * bf_hi(c[0]);
        s += bf_lo(a[1]) * bf_lo(c[1]); s += bf_hi(a[1]) * bf_hi(c[1]);
      }
#pragma unroll
      for (int o = 16; o > 0; o >>= 1) s += __shfl_xor(s, o);
      const float gq = 1.f / (1.f + __expf(-s));
      if (l32 == 0) ((float*)(ws + WS_G))[grow] = gq;
    }
    TS_FLUSH(0);
    return;
  }

  const int wm = (wave >> 1) & 1, wn = wave & 1;
  const int r = lane & 15, g = lane >> 4, q = (lane & 15) >> 2, p = lane & 3;
  const int hsw = ((q >> 1) & 1) | ((g & 1) << 1);
  const unsigned bbase = 16384 + g * 3072 + q * 128;
  unsigned bo[5];
  f32x4 acc[4][5];

  if (iskv) {
#pragma unroll
    for (int nt = 0; nt < 5; ++nt) {
      const int u = 4 * nt + p;
      const int isv = u >= 10 ? 1 : 0;
      const int pc = 4 * p0 + 10 * wn + u - 10 * isv;
      const int gp = pc >> 2, big = ((gp >> 2) == bigB);
      const int slot = (gp & 3) ^ ((!big && isv) ? xs : 0);
      bo[nt] = bbase + (big ? isv : 2) * 1024 + ((slot ^ hsw) * 32) + (((pc & 3) ^ (isv << 1)) * 8);
      const float i0 = ((nt < 2) || (nt == 2 && r < 8)) ? kEps : 0.f;
#pragma unroll
      for (int mt = 0; mt < 4; ++mt) acc[mt][nt] = (f32x4){i0, i0, i0, i0};
    }
    k1_main<false>(acc, sm, lane, wm, bo TS_PASS);
    TS(3);
    bf16x8 fr[2][5];
#pragma unroll
    for (int nt = 0; nt < 5; ++nt) {
#pragma unroll
      for (int mt = 0; mt < 4; ++mt)
#pragma unroll
        for (int j = 0; j < 4; ++j) {
          if (nt < 2) acc[mt][nt][j] = phi_f(acc[mt][nt][j]);
          if (nt == 2) { const float ph = phi_f(acc[mt][nt][j]); acc[mt][nt][j] = (r < 8) ? ph : acc[mt][nt][j]; }
        }
      fr[0][nt] = pack8(acc[0][nt], acc[1][nt]);
      fr[1][nt] = pack8(acc[2][nt], acc[3][nt]);
    }
    const bf16x8 ones = {0x3f80, 0x3f80, 0x3f80, 0x3f80, 0x3f80, 0x3f80, 0x3f80, 0x3f80};
    lptr kvw = sm + 2 * K1_STAGE + (wm * 2 + wn) * 3840;
    const int dump = 4 * 3840 - (wm * 2 + wn) * 3840 + lane * 16;
    auto pairf = [&](int ti, int tj) {
      f32x4 d = {0.f, 0.f, 0.f, 0.f};
      d = MFMA16(tj < 0 ? ones : fr[0][tj < 0 ? 0 : tj], fr[0][ti], d);
      d = MFMA16(tj < 0 ? ones : fr[1][tj < 0 ? 0 : tj], fr[1][ti], d);
      const int ur = 16 * ti + r, hk = ur >= 20 ? 1 : 0;
      const int uc = 16 * tj + 4 * g - 40, hv = uc >= 20 ? 1 : 0;
      const bool valid = tj < 0 ? (ur < 40 && g == 0) : (ur < 40 && uc >= 0 && hk == hv);
      const int off = hk * 1920 + (ur - 20 * hk) * 96 + (tj < 0 ? 80 : (uc - 20 * hv) * 4);
      *(LDS_AS f32x4*)(kvw + (valid ? off : dump)) = d;
    };
    pairf(0, 2); pairf(0, 3); pairf(1, 2); pairf(1, 3); pairf(1, 4); pairf(2, 3); pairf(2, 4);
    pairf(0, -1); pairf(1, -1); pairf(2, -1);
    WAIT_L0();
    BAR();
    const rsrc_t rk = mk_rsrc(ws + WS_KVP + ((size_t)rt * 32 + 4 * (ct - 4)) * 1920, 7680);
    LDS_AS f32x4* src = (LDS_AS f32x4*)(sm + 2 * K1_STAGE);
    const int t = tid - 256;
    const f32x4 s0 = src[t] + src[t + 480];
    const f32x4 s1 = src[t + 256] + src[t + 736];
    ST16(__builtin_bit_cast(u32x4, s0), rk, t * 16, 0);
    ST16(__builtin_bit_cast(u32x4, s1), rk, t * 16 + 4096, 0);
  } else {
#pragma unroll
    for (int nt = 0; nt < 5; ++nt) {
      const int gp = p0 + 5 * wn + nt;
      bo[nt] = bbase + (gp >> 2) * 1024 + (((gp & 3) ^ hsw) * 32) + p * 8;
    }
    const float i0 = ct < 4 ? kEps : 0.f;
#pragma unroll
    for (int mt = 0; mt < 4; ++mt)
#pragma unroll
      for (int nt = 0; nt < 5; ++nt) acc[mt][nt] = (f32x4){i0, i0, i0, i0};
    k1_main<true>(acc, sm, lane, wm, bo TS_PASS);
    TS(3);
    if (ct < 4) {
      const rsrc_t rq = mk_rsrc(ws + WS_P + (size_t)(8 * rt + 4 * wm) * 20480 + (20 * ct + 10 * wn) * 256, 4 * 20480);
      const unsigned vo = (g & 1) * 20480 + (g >> 1) * 256 + r * 16;
#pragma unroll
      for (int nt = 0; nt < 5; ++nt)
#pragma unroll
        for (int mp = 0; mp < 2; ++mp) {
          const f32x4 v = acc[2 * mp][nt], w = acc[2 * mp + 1][nt];
          const u32x2 a = {pk2(phi_f(v[0]), phi_f(v[1])), pk2(phi_f(v[2]), phi_f(v[3]))};
          const u32x2 c = {pk2(phi_f(w[0]), phi_f(w[1])), pk2(phi_f(w[2]), phi_f(w[3]))};
          ST16(widen(a, c), rq, vo + nt * 512, mp * 40960);
        }
    } else {
      const rsrc_t rx = mk_rsrc(ws + WS_X1 + (size_t)(80 * (ct - 12) + 40 * wn) * 8192 + ((rt >> 1) & 1) * 4096 + (rt >> 2) * 1024 + wm * 512 + (rt & 1) * 128, 40 * 8192);
      const unsigned vo = 2 * g * 8192 + (r >> 3) * 256 + (r & 7) * 16;
#pragma unroll
      for (int nt = 0; nt < 5; ++nt) {
        const u32x4 o0 = {pk2(acc[0][nt][0], acc[0][nt][1]), pk2(acc[1][nt][0], acc[1][nt][1]), pk2(acc[2][nt][0], acc[2][nt][1]), pk2(acc[3][nt][0], acc[3][nt][1])};
        const u32x4 o1 = {pk2(acc[0][nt][2], acc[0][nt][3]), pk2(acc[1][nt][2], acc[1][nt][3]), pk2(acc[2][nt][2], acc[2][nt][3]), pk2(acc[3][nt][2], acc[3][nt][3])};
        ST16(o0, rx, vo, nt * 8 * 8192);
        ST16(o1, rx, vo, nt * 8 * 8192 + 8192);
      }
    }
  }
  TS(4);
  TS_FLUSH(0);
}

constexpr int K2_NFFT = 321, K2_NM = 80, K2_NP = 128;
constexpr int K2_LDS = 32768 + 4608;

__global__ __launch_bounds__(256) void k2_kernel(const u16* __restrict__ wo, char* __restrict__ ws) {
  __shared__ __attribute__((aligned(16))) char smem[K2_LDS];
  lptr sm = (lptr)smem;
  TS_DECL;
  TS(0);
  const int tid = threadIdx.x, lane = tid & 63;
  const int wave = __builtin_amdgcn_readfirstlane(tid >> 6);
  const int b = blockIdx.x;
  const int r = lane & 15, g = lane >> 4;
  const f32x4 z4 = {0.f, 0.f, 0.f, 0.f};
  if (b < K2_NFFT) {
    const int f = b;
    const bool edge = (f == 0 || f == 320);
    {
      const rsrc_t rx = mk_rsrc(ws + WS_X1 + (size_t)(f == 320 ? 0 : f) * 8192, 8192);
      DMA16(rx, sm + wave * 2048, lane * 16, wave * 2048);
      DMA16(rx, sm + wave * 2048 + 1024, lane * 16, wave * 2048 + 1024);
    }
    const rsrc_t rw = mk_rsrc(ws + WS_WT + (size_t)f * 8192, 8192);
    u32x4 wv[2];
    wv[0] = __builtin_amdgcn_raw_buffer_load_b128(rw, lane * 16, wave * 2048, 0);
    wv[1] = __builtin_amdgcn_raw_buffer_load_b128(rw, lane * 16 + 1024, wave * 2048, 0);
    SCHED();
    u32x4 fre[4], fim[4], fc[2], fs[2], fn[2];
    float tc[2][4], tsn[2][4];
    {
      const float a64 = (float)(16 * wave + r) * (1.f / 64.f);
#pragma unroll
      for (int ks = 0; ks < 4; ++ks)
#pragma unroll
        for (int e = 0; e < 4; ++e) {
          const float ang = a64 * (float)(16 * ks + 4 * g + e);
          const float c = __builtin_amdgcn_cosf(ang), s = __builtin_amdgcn_sinf(ang);
          fre[ks][e] = pk2(c, s);
          fim[ks][e] = pk2(-s, c);
        }
#pragma unroll
      for (int t = 0; t < 2; ++t) {
        const float a32 = (float)(16 * t + r) * (1.f / 32.f);
#pragma unroll
        for (int e = 0; e < 4; ++e) {
          const int b0 = 16 * (e >> 1) + 4 * g + 2 * (e & 1);
          const float a0 = a32 * (float)b0, a1 = a0 + a32;
          const float s0 = __builtin_amdgcn_sinf(a0), s1 = __builtin_amdgcn_sinf(a1);
          fc[t][e] = pk2(__builtin_amdgcn_cosf(a0), __builtin_amdgcn_cosf(a1));
          fs[t][e] = pk2(s0, s1);
          fn[t][e] = fs[t][e] ^ 0x80008000u;
        }
#pragma unroll
        for (int j = 0; j < 4; ++j) {
          const float a = a64 * (1.f / 32.f) * (float)(16 * t + 4 * g + j);
          tc[t][j] = __builtin_amdgcn_cosf(a);
          tsn[t][j] = __builtin_amdgcn_sinf(a);
        }
      }
    }
    SCHED();
    WAIT_V(0);
    BAR();
    SCHED();
    TS(1);
    f32x4 re[2], im[2];
#pragma unroll
    for (int t = 0; t < 2; ++t) {
      re[t] = z4; im[t] = z4;
#pragma unroll
      for (int ks = 0; ks < 4; ++ks) {
        u32x4 xa = *(LDS_AS u32x4*)(sm + (t * 4 + ks) * 1024 + lane * 16);
        if (edge) {
#pragma unroll
          for (int e = 0; e < 4; ++e) xa[e] = (f == 0) ? (xa[e] & 0xffffu) : (xa[e] >> 16);
        }
        re[t] = MFMA16(BC(xa), BC(fre[ks]), re[t]);
        im[t] = MFMA16(BC(xa), BC(fim[ks]), im[t]);
      }
    }
    TS(2);
#pragma unroll
    for (int t = 0; t < 2; ++t)
#pragma unroll
      for (int j = 0; j < 4; ++j) {
        const float a = re[t][j], c = im[t][j];
        re[t][j] = a * tc[t][j] + c * tsn[t][j];
        im[t][j] = c * tc[t][j] - a * tsn[t][j];
      }
    bf16x8 bre = pack8(re[0], re[1]), bim = pack8(im[0], im[1]);
#pragma unroll
    for (int t = 0; t < 2; ++t) {
      re[t] = MFMA16(BC(fc[t]), bre, z4); im[t] = MFMA16(BC(fc[t]), bim, z4);
      re[t] = MFMA16(BC(fs[t]), bim, re[t]); im[t] = MFMA16(BC(fn[t]), bre, im[t]);
    }
    const float sc = (edge ? 1.f : 2.f) / (2048.f * 640.f);
#pragma unroll
    for (int t = 0; t < 2; ++t)
#pragma unroll
      for (int j = 0; j < 4; ++j) {
        const float a = re[t][j], c = im[t][j], wre = bf_lo(wv[t][j]) * sc, wim = bf_hi(wv[t][j]) * sc;
        re[t][j] = a * wre - c * wim;
        im[t][j] = a * wim + c * wre;
      }
    bre = pack8(re[0], re[1]); bim = pack8(im[0], im[1]);
#pragma unroll
    for (int t = 0; t < 2; ++t) {
      re[t] = MFMA16(BC(fc[t]), bre, z4); im[t] = MFMA16(BC(fc[t]), bim, z4);
      re[t] = MFMA16(BC(fn[t]), bim, re[t]); im[t] = MFMA16(BC(fs[t]), bre, im[t]);
    }
#pragma unroll
    for (int t = 0; t < 2; ++t)
#pragma unroll
      for (int j = 0; j < 4; ++j) {
        const float a = re[t][j], c = im[t][j];
        const float ur = a * tc[t][j] - c * tsn[t][j];
        const float ui = -(c * tc[t][j]) - a * tsn[t][j];
        const int s2 = 16 * t + 4 * g + j;
        *(LDS_AS unsigned*)(sm + 8192 + s2 * 256 + (((4 * wave + (r >> 2)) ^ (s2 & 15)) * 16) + (r & 3) * 4) = pk2(ur, ui);
      }
    WAIT_L0();
    BAR();
    TS(3);
    u32x2 pr[2], pi[2];
#pragma unroll
    for (int t = 0; t < 2; ++t) {
      f32x4 yr = z4, yi = z4;
#pragma unroll
      for (int ks = 0; ks < 4; ++ks) {
        const bf16x8 u = *(LDS_AS bf16x8*)(sm + 8192 + (16 * t + r) * 256 + (((4 * ks + g) ^ r) * 16));
        yr = MFMA16(u, BC(fre[ks]), yr);
        yi = MFMA16(u, BC(fim[ks]), yi);
      }
      pr[t] = (u32x2){pk2(yr[0], yr[1]), pk2(yr[2], yr[3])};
      pi[t] = (u32x2){pk2(-yi[0], -yi[1]), pk2(-yi[2], -yi[3])};
    }
    const unsigned vo = wave * 1024 + r * 64 + (g & 1) * 32 + (g >> 1) * 16;
    const rsrc_t ry = mk_rsrc(ws + WS_Y + (f == 320 ? (size_t)320 * 4096 : (size_t)f * 4096), 321 * 4096);
    ST16(widen(pr[0], pr[1]), ry, vo, 0);
    if (!edge) ST16(widen(pi[0], pi[1]), ry, vo, 320 * 4096);
  } else if (b < K2_NFFT + K2_NM) {
    const int mb = b - K2_NFFT, ks = mb >> 2, cp = mb & 3;
    const int hf = (32 * ks) / 20, e0 = 20 * hf;
    const int L1 = (160 * cp) >> 7, L2 = (1120 - 160 * cp) >> 7;
    {
      const rsrc_t rwo = mk_rsrc(wo, 640 * 1280);
      const int line = (wave < 2 ? L1 : L2) + (wave & 1);
      const int row = lane >> 3;
#pragma unroll
      for (int kb = 0; kb < 8; ++kb) {
        const int pair = ((lane & 7) >> 1) ^ (((row >> 1) & 1) | ((kb & 1) << 1));
        DMA16(rwo, sm + (wave * 8 + kb) * 1024, min(e0 + 8 * kb + row, 639) * 1280 + line * 128 + pair * 32 + (lane & 1) * 16, 0);
      }
    }
    const int kl = min(tid, 159) / 5, qd = min(tid, 159) - kl * 5;
    const int k = 32 * ks + kl, h = k / 20, d = k - 20 * h;
    const rsrc_t rk = mk_rsrc(ws + WS_KVP, 983040);
    f32x4 v[16];
#pragma unroll
    for (int i = 0; i < 16; ++i) v[i] = __builtin_bit_cast(f32x4, __builtin_amdgcn_raw_buffer_load_b128(rk, h * 1920 + d * 96 + qd * 16, i * 61440, 0));
    lptr ab = sm + 32768;
    *(LDS_AS u32x4*)(ab + tid * 16) = (u32x4){0u, 0u, 0u, 0u};
    if (tid < 32) *(LDS_AS u32x4*)(ab + 4096 + tid * 16) = (u32x4){0u, 0u, 0u, 0u};
    SCHED();
    f32x4 sum = z4;
#pragma unroll
    for (int i = 0; i < 16; ++i) sum += v[i];
    WAIT_V(0);
    __syncthreads();
    if (tid < 160) *(LDS_AS u32x2*)(ab + kl * 144 + (20 * (h - hf) + 4 * qd) * 2) = (u32x2){pk2(sum[0], sum[1]), pk2(sum[2], sum[3])};
    __syncthreads();
    bf16x8 a[2][2];
#pragma unroll
    for (int t = 0; t < 2; ++t)
#pragma unroll
      for (int s = 0; s < 2; ++s) a[t][s] = *(LDS_AS bf16x8*)(ab + (8 * (r >> 2) + (r & 3) + 4 * t) * 144 + s * 64 + g * 16);
    const int q = r >> 2, p = lane & 3;
    const int hsw = ((q >> 1) & 1) | ((g & 1) << 1);
    const rsrc_t rm = mk_rsrc(ws + WS_M, 819200);
#pragma unroll
    for (int n = 0; n < 3; ++n) {
      const int tl = min(wave + 4 * n, 9);
      const int ctl = tl >= 5 ? 1 : 0, nt = tl - 5 * ctl, ctg = 2 * cp + ctl;
      const int i = 16 * nt + 4 * p;
      const int byte = 2 * (i < 40 ? 40 * ctg + i : 560 - 40 * ctg + i);
      const int pl = i < 40 ? (byte >> 7) - L1 : 2 + (byte >> 7) - L2;
      lptr bp = sm + (pl * 8 + g) * 1024 + q * 128 + ((((byte >> 5) & 3) ^ hsw) * 32) + (byte & 31);
      bf16x8 bw[2];
#pragma unroll
      for (int s = 0; s < 2; ++s) {
        const s16x4 lo = TRREAD(bp + s * 4096), hi = TRREAD(bp + s * 4096 + 512);
        bw[s] = __builtin_shufflevector(lo, hi, 0, 1, 2, 3, 4, 5, 6, 7);
      }
      f32x4 d1 = MFMA16(a[0][0], bw[0], z4), d2 = MFMA16(a[1][0], bw[0], z4);
      d1 = MFMA16(a[0][1], bw[1], d1); d2 = MFMA16(a[1][1], bw[1], d2);
      if (wave + 4 * n < 10) ST16(__builtin_bit_cast(u32x4, pack8(d1, d2)), rm, lane * 16, (10 * cp + tl) * 20480 + ks * 1024);
    }
  } else {
    const int pb = b - K2_NFFT - K2_NM, rb = pb >> 4, hp = pb & 15;
    const rsrc_t rp = mk_rsrc(ws + WS_P + (size_t)(16 * rb) * 20480 + hp * 1280, 16 * 20480);
    const unsigned vo = (tid >> 4) * 20480 + (tid & 15) * 16;
    u32x4 q[5];
#pragma unroll
    for (int i = 0; i < 5; ++i) q[i] = __builtin_amdgcn_raw_buffer_load_b128(rp, vo + i * 256, 0, 0);
    LDS_AS float* zs = (LDS_AS float*)sm;
    {
      const int zi = tid % 40, pg = min(tid / 40, 3);
      const rsrc_t rk = mk_rsrc(ws + WS_KVP + (size_t)hp * 3840 + 80, 983040);
      const unsigned zo = (zi >= 20 ? 1920 + (zi - 20) * 96 : zi * 96) + pg * 4 * 61440;
      float zv[4];
#pragma unroll
      for (int i = 0; i < 4; ++i) zv[i] = __uint_as_float(__builtin_amdgcn_raw_buffer_load_b32(rk, zo, i * 61440, 0));
      if (tid < 160) zs[tid] = (zv[0] + zv[1]) + (zv[2] + zv[3]);
    }
    __syncthreads();
    float den0 = 0.f, den1 = 0.f;
    float qf[40];
#pragma unroll
    for (int i = 0; i < 10; ++i) {
      const f32x4 z = (*(LDS_AS f32x4*)(zs + 4 * i) + *(LDS_AS f32x4*)(zs + 40 + 4 * i)) + (*(LDS_AS f32x4*)(zs + 80 + 4 * i) + *(LDS_AS f32x4*)(zs + 120 + 4 * i));
      const unsigned u0 = q[i >> 1][2 * (i & 1)], u1 = q[i >> 1][2 * (i & 1) + 1];
      qf[4 * i] = bf_lo(u0); qf[4 * i + 1] = bf_hi(u0); qf[4 * i + 2] = bf_lo(u1); qf[4 * i + 3] = bf_hi(u1);
      const float s = qf[4 * i] * z[0] + qf[4 * i + 1] * z[1] + qf[4 * i + 2] * z[2] + qf[4 * i + 3] * z[3];
      if (i < 5) den0 += s; else den1 += s;
    }
    const float inv0 = 1.f / (den0 + kEps), inv1 = 1.f / (den1 + kEps);
#pragma unroll
    for (int i = 0; i < 5; ++i) {
      u32x4 o;
#pragma unroll
      for (int e = 0; e < 4; ++e) { const float iv = (8 * i + 2 * e < 20) ? inv0 : inv1; o[e] = pk2(qf[8 * i + 2 * e] * iv, qf[8 * i + 2 * e + 1] * iv); }
      ST16(o, rp, vo + i * 256, 0);
    }
  }
  TS(4);
  TS_FLUSH(1);
}

constexpr int K3_LDS = 2 * 71680;

__global__ __launch_bounds__(256, 1) void k3_kernel(const char* __restrict__ ws, u16* __restrict__ out) {
  extern __shared__ __attribute__((aligned(16))) char smem[];
  lptr sm = (lptr)smem;
  const int tid = threadIdx.x, lane = tid & 63;
  const int wave = __builtin_amdgcn_readfirstlane(tid >> 6);
  const int b = blockIdx.x, xcd = b & 7, idx = b >> 3;
  TS_DECL;
  TS(0);
  const int rt = (xcd & 3) * 8 + (idx & 7);
  const int ct = (xcd >> 2) * 4 + (idx >> 3);
  const int r = lane & 15, g = lane >> 4;
  LDS_AS float* red = (LDS_AS float*)sm;
  constexpr int RA = 64 * 84, RE = 64 * 52;
  if (wave < 2) {
    const rsrc_t ra = mk_rsrc(ws + WS_P + (size_t)(4 * rt) * 20480 + wave * 10240, 4 * 20480);
    const rsrc_t rb = mk_rsrc(ws + WS_M + (size_t)(5 * ct) * 20480 + wave * 10240, 5 * 20480);
    const unsigned vo = lane * 16;
    f32x4 acc[4][5];
#pragma unroll
    for (int mt = 0; mt < 4; ++mt)
#pragma unroll
      for (int nt = 0; nt < 5; ++nt) acc[mt][nt] = (f32x4){0.f, 0.f, 0.f, 0.f};
    u32x4 fa[10][4], fb[10][5];
    constexpr int PF = 6;
    auto ld = [&](int ks) {
#pragma unroll
      for (int nt = 0; nt < 5; ++nt) fb[ks][nt] = __builtin_amdgcn_raw_buffer_load_b128(rb, vo + (ks & 3) * 1024, nt * 20480 + (ks >> 2) * 4096, 0);
#pragma unroll
      for (int mt = 0; mt < 4; ++mt) fa[ks][mt] = __builtin_amdgcn_raw_buffer_load_b128(ra, vo + (ks & 3) * 1024, mt * 20480 + (ks >> 2) * 4096, 0);
    };
#pragma unroll
    for (int ks = 0; ks < PF; ++ks) { ld(ks); SCHED(); }
    const float gq0 = ((const float*)(ws + WS_G))[64 * rt + lane];
    SCHED();
    TS(1);
#pragma unroll
    for (int ks = 0; ks < 10; ++ks) {
#pragma unroll
      for (int mt = 0; mt < 4; ++mt)
#pragma unroll
        for (int nt = 0; nt < 5; ++nt) acc[mt][nt] = MFMA16(BC(fb[ks][nt]), BC(fa[ks][mt]), acc[mt][nt]);
      SCHED();
      if (ks == 0) TS(2);
      if (ks + PF < 10) ld(ks + PF);
      SCHED();
    }
    TS(3);
    BAR();
#pragma unroll
    for (int mt = 0; mt < 4; ++mt)
#pragma unroll
      for (int nt = 0; nt < 5; ++nt) *(LDS_AS f32x4*)(red + wave * RA + (16 * mt + r) * 84 + 16 * nt + 4 * g) = acc[mt][nt];
    if (wave == 0) red[2 * RA + 2 * RE + lane] = gq0;
  } else {
    const int od = wave - 2;
    lptr yb = sm + od * 71680;
    const rsrc_t ry = mk_rsrc(ws + WS_Y + (size_t)od * 320 * 4096 + rt * 128, 320 * 4096);
    const rsrc_t rg = mk_rsrc((const char*)&GT + (size_t)(2 * ct + od) * 30720, 30720);
    const int row = lane >> 3;
    const unsigned vy0 = row * 4096 + ((((lane & 7) >> 1) ^ ((row >> 1) & 1)) * 32) + (lane & 1) * 16;
    const unsigned vy1 = vy0 ^ 64;
#pragma unroll
    for (int ks = 0; ks < 10; ++ks) {
#pragma unroll
      for (int i = 0; i < 4; ++i) DMA16(ry, yb + (ks * 4 + i) * 1024, (i & 1) ? vy1 : vy0, (ks * 4 + i) * 32768);
#pragma unroll
      for (int i = 0; i < 3; ++i) DMA16(rg, yb + 40960 + (ks * 3 + i) * 1024, lane * 16, (ks * 3 + i) * 1024);
    }
    TS(1);
    const int q = r >> 2, p = lane & 3;
    const int hsw = ((q >> 1) & 1) | ((g & 1) << 1);
    unsigned yo[4];
#pragma unroll
    for (int mt = 0; mt < 4; ++mt) yo[mt] = g * 1024 + q * 128 + ((mt ^ hsw) * 32) + p * 8;
    f32x4 acc[4][3];
#pragma unroll
    for (int mt = 0; mt < 4; ++mt)
#pragma unroll
      for (int nt = 0; nt < 3; ++nt) acc[mt][nt] = (f32x4){0.f, 0.f, 0.f, 0.f};
#define K3_STEP(ks, n)                                                                                         \
    {                                                                                                          \
      WAIT_V(n);                                                                                               \
      SCHED();                                                                                                 \
      bf16x8 fy[4], fg[3];                                                                                     \
      _Pragma("unroll") for (int nt = 0; nt < 3; ++nt) fg[nt] = *(LDS_AS bf16x8*)(yb + 40960 + (ks * 3 + nt) * 1024 + lane * 16); \
      _Pragma("unroll") for (int mt = 0; mt < 4; ++mt) {                                                       \
        const s16x4 lo = TRREAD(yb + yo[mt] + ks * 4096), hi = TRREAD(yb + yo[mt] + ks * 4096 + 512);          \
        fy[mt] = __builtin_shufflevector(lo, hi, 0, 1, 2, 3, 4, 5, 6, 7);                                      \
      }                                                                                                        \
      _Pragma("unroll") for (int mt = 0; mt < 4; ++mt)                                                         \
        _Pragma("unroll") for (int nt = 0; nt < 3; ++nt) acc[mt][nt] = MFMA16(fg[nt], fy[mt], acc[mt][nt]);   \
      SCHED();                                                                                                 \
    }
    K3_STEP(0, 63) TS(2); K3_STEP(1, 56) K3_STEP(2, 49) K3_STEP(3, 42) K3_STEP(4, 35)
    K3_STEP(5, 28) K3_STEP(6, 21) K3_STEP(7, 14) K3_STEP(8, 7) K3_STEP(9, 0)
    TS(3);
    BAR();
#pragma unroll
    for (int mt = 0; mt < 4; ++mt)
#pragma unroll
      for (int nt = 0; nt < 3; ++nt) *(LDS_AS f32x4*)(red + 2 * RA + od * RE + (16 * mt + r) * 52 + 16 * nt + 4 * g) = acc[mt][nt];
  }
  WAIT_L0();
  BAR();
  const rsrc_t ro = mk_rsrc(out + (size_t)(64 * rt) * 640, 64 * 1280);
#pragma unroll
  for (int i = 0; i < 5; ++i) {
    const int it = i * 256 + tid, row = it / 20, c4 = it - row * 20;
    LDS_AS float* pa = red + row * 84 + 4 * c4;
    const f32x4 a = *(LDS_AS f32x4*)pa + *(LDS_AS f32x4*)(pa + RA);
    LDS_AS float* pe = red + 2 * RA + row * 52;
    const float gq = red[2 * RA + 2 * RE + row];
    const float yn = pe[RE + 41];
    const bool lo = c4 < 10;
    f32x4 bb;
#pragma unroll
    for (int j = 0; j < 4; ++j) {
      const int ci = lo ? 4 * c4 + j : 80 - 4 * c4 - j;
      const float e = pe[ci], o = pe[RE + ci];
      bb[j] = (lo ? e - o : e + o) + ((j & 1) ? -yn : yn);
    }
    const f32x4 o = bb + gq * (a - bb);
    const u32x2 v = {pk2(o[0], o[1]), pk2(o[2], o[3])};
    __builtin_amdgcn_raw_buffer_store_b64(v, ro, row * 1280 + (lo ? 80 * ct + 8 * c4 : 1120 - 80 * ct + 8 * c4), 0, OUT_AUX);
  }
  TS(4);
  TS_FLUSH(2);
}

extern "C" void kernel_launch(void* const* d_in, const int* in_sizes, int n_in, void* d_out, int out_size, void* d_ws,
                              size_t ws_size, hipStream_t stream) {
  const u16* x = (const u16*)d_in[0];
  const u16* wqkv = (const u16*)d_in[1];
  const u16* wo = (const u16*)d_in[2];
  const u16* wr = (const u16*)d_in[4];
  const u16* wi = (const u16*)d_in[5];
  const u16* gw = (const u16*)d_in[6];
  char* ws = (char*)d_ws;
  (void)hipFuncSetAttribute((const void*)k1_kernel, hipFuncAttributeMaxDynamicSharedMemorySize, K1_LDS);
  (void)hipFuncSetAttribute((const void*)k3_kernel, hipFuncAttributeMaxDynamicSharedMemorySize, K3_LDS);
  k1_kernel<<<256, 512, K1_LDS, stream>>>(x, wqkv, wr, wi, gw, ws);
  k2_kernel<<<K2_NFFT + K2_NM + K2_NP, 256, 0, stream>>>(wo, ws);
  k3_kernel<<<256, 256, K3_LDS, stream>>>(ws, (u16*)d_out);
}
```

```cpp
#include <hip/hip_runtime.h>
#include <hip/hip_bf16.h>
#include <utility>

typedef unsigned short u16;
typedef __attribute__((ext_vector_type(8))) short bf16x8;
typedef __attribute__((ext_vector_type(4))) short s16x4;
typedef __attribute__((ext_vector_type(4))) float f32x4;
typedef __attribute__((ext_vector_type(2))) float f32x2;
typedef __attribute__((ext_vector_type(2))) __bf16 bf16x2_t;
typedef __attribute__((ext_vector_type(4))) unsigned u32x4;
typedef __attribute__((ext_vector_type(2))) unsigned u32x2;
#define LDS_AS __attribute__((address_space(3)))
#define DI __device__ __forceinline__
#define MFMA16(a, b, c) __builtin_amdgcn_mfma_f32_16x16x32_bf16((a), (b), (c), 0, 0, 0)
typedef LDS_AS char* lptr;
typedef __amdgpu_buffer_rsrc_t rsrc_t;

#define ST_AUX 16
#define OUT_AUX 0
#define K1_ROT 0

constexpr double kPi = 3.14159265358979323846;
constexpr double ccos_s(double x) { double x2 = x * x, t = 1, s = 1; for (int i = 1; i < 12; ++i) { t *= -x2 / ((2 * i - 1) * (2 * i)); s += t; } return s; }
constexpr double csin_s(double x) { double x2 = x * x, t = x, s = x; for (int i = 1; i < 12; ++i) { t *= -x2 / ((2 * i) * (2 * i + 1)); s += t; } return s; }
constexpr double cos640(int m) {
  m %= 640; if (m > 320) m = 640 - m;
  bool neg = false; if (m > 160) { m = 320 - m; neg = true; }
  double v = (m <= 80) ? ccos_s(2 * kPi * m / 640) : csin_s(2 * kPi * (160 - m) / 640);
  return neg ? -v : v;
}
constexpr u16 to_bf16c(double v) { float f = (float)v; unsigned u = __builtin_bit_cast(unsigned, f); u += 0x7fffu + ((u >> 16) & 1u); return (u16)(u >> 16); }
struct C640 { u16 v[640]; };
constexpr C640 make_c640() { C640 c{}; for (int i = 0; i < 640; ++i) c.v[i] = to_bf16c(cos640(i)); return c; }
constexpr C640 COS640 = make_c640();

struct alignas(128) FRow { u16 v[16 * 640]; };
constexpr FRow make_frow(int dt) {
  FRow t{};
  for (int r = 0; r < 16; ++r) {
    const int d = dt * 16 + r;
    for (int kk = 0; kk < 640; ++kk) {
      const int m = (kk == 1) ? (d * 320) % 640 : (d * (kk >> 1) + 160 * (kk & 1)) % 640;
      t.v[r * 640 + kk] = COS640.v[m];
    }
  }
  return t;
}
template <int I> constexpr FRow FROW = make_frow(I);
struct alignas(128) FTab { FRow r[40]; };
template <int... I> constexpr FTab make_ftab(std::integer_sequence<int, I...>) { return FTab{{FROW<I>...}}; }
__device__ const FTab FT = make_ftab(std::make_integer_sequence<int, 40>{});

struct alignas(128) GRow { u16 v[10 * 3 * 512]; };
constexpr GRow make_grow(int id) {
  GRow t{};
  const int ct = id >> 1, od = id & 1;
  for (int ks = 0; ks < 10; ++ks) for (int nt = 0; nt < 3; ++nt) for (int l = 0; l < 64; ++l) {
    const int ci = 16 * nt + (l & 15), d = 40 * ct + ci;
    for (int j = 0; j < 8; ++j) {
      const int f = 32 * ks + 8 * (l >> 4) + j;
      t.v[((ks * 3 + nt) * 64 + l) * 8 + j] = (ci <= 40) ? COS640.v[(f * d + (od ? 480 : 0)) % 640] : (u16)((od && ci == 41 && f == 0) ? 0x3f80 : 0);
    }
  }
  return t;
}
template <int I> constexpr GRow GROW = make_grow(I);
struct alignas(128) GTab { GRow r[16]; };
template <int... I> constexpr GTab make_gtab(std::integer_sequence<int, I...>) { return GTab{{GROW<I>...}}; }
__device__ const GTab GT = make_gtab(std::make_integer_sequence<int, 16>{});

constexpr size_t WS_P   = 0;
constexpr size_t WS_Y   = WS_P + 2621440;
constexpr size_t WS_X1  = WS_Y + 2621440;
constexpr size_t WS_WT  = WS_X1 + 2621440;
constexpr size_t WS_KVP = WS_WT + 352 * 8192;
constexpr size_t WS_M   = WS_KVP + 983040;
constexpr size_t WS_G   = WS_M + 819200;
constexpr size_t WS_END = WS_G + 8192;

DI rsrc_t mk_rsrc(const void* p, unsigned bytes) { return __builtin_amdgcn_make_buffer_rsrc((void*)p, 0, bytes, 0x00020000); }
DI unsigned pk2(float a, float b) { f32x2 v = {a, b}; return __builtin_bit_cast(unsigned, __builtin_convertvector(v, bf16x2_t)); }
DI float bf_lo(unsigned u) { return __uint_as_float(u << 16); }
DI float bf_hi(unsigned u) { return __uint_as_float(u & 0xffff0000u); }
DI float dot2(unsigned a, unsigned b, float c) { return __builtin_amdgcn_fdot2_f32_bf16(__builtin_bit_cast(bf16x2_t, a), __builtin_bit_cast(bf16x2_t, b), c, false); }
DI float rbf(float a) { return bf_lo(pk2(a, a)); }
constexpr float kEps = 1e-6f, kL2E = 1.4426950408889634f;
DI float phi_f(float a) {
  float e;
  const float u = __builtin_fmaf(a, kL2E, -kEps * kL2E);
  asm("v_exp_f32_e64 %0, %1 clamp" : "=v"(e) : "v"(u));
  return __builtin_fmaxf(a, kEps) + e;
}
DI bf16x8 pack8(const f32x4& a, const f32x4& b) {
  u32x4 p = {pk2(a[0], a[1]), pk2(a[2], a[3]), pk2(b[0], b[1]), pk2(b[2], b[3])};
  return __builtin_bit_cast(bf16x8, p);
}
DI u32x4 widen(u32x2 a, u32x2 b) {
  auto x = __builtin_amdgcn_permlane16_swap(a[0], b[0], false, false);
  auto y = __builtin_amdgcn_permlane16_swap(a[1], b[1], false, false);
  return (u32x4){x[0], y[0], x[1], y[1]};
}
#define WAIT_V(n) asm volatile("s_waitcnt vmcnt(" #n ")" ::: "memory")
#define BC(v) __builtin_bit_cast(bf16x8, (v))
#define WAIT_L0() __builtin_amdgcn_s_waitcnt(0xC07F)
#define BAR() __builtin_amdgcn_s_barrier()
#define SCHED() __builtin_amdgcn_sched_barrier(0)
#define DMA16(rs, dst, vo, so) __builtin_amdgcn_raw_ptr_buffer_load_lds((rs), (LDS_AS void*)(dst), 16, (vo), (so), 0, 0)
#define TRREAD(p) __builtin_amdgcn_ds_read_tr16_b64_v4i16((LDS_AS s16x4*)(p))
#define ST16(v, rs, vo, so) __builtin_amdgcn_raw_buffer_store_b128((v), (rs), (vo), (so), ST_AUX)

#define TS_DECL
#define TS(i)
#define TC(i)
#define TS_FLUSH(kern)
#define TS_ARG
#define TS_PASS

constexpr int K1_STAGE = 40960;
constexpr int K1_WF = 3 * K1_STAGE;
constexpr int K1_JUNK = K1_WF + 32768;
constexpr int K1_LDS = K1_JUNK + 2048;
#define K1_PF 1

struct Frags { bf16x8 a[2][2]; bf16x8 b[2][5]; };
struct K1Ctx { rsrc_t ra, rb; unsigned va[2], vb[3], ao[2], bo[5], ldsa, ldsb; int st, rs, wave; };

template <int BUF> DI void k1_read(Frags& F, lptr sm, const K1Ctx& c) {
#pragma unroll
  for (int k = 0; k < 2; ++k) {
#pragma unroll
    for (int mt = 0; mt < 2; ++mt) F.a[k][mt] = *(LDS_AS bf16x8*)(sm + c.ao[k] + BUF * K1_STAGE + mt * 2048);
#pragma unroll
    for (int nt = 0; nt < 5; ++nt) {
      s16x4 lo = TRREAD(sm + c.bo[nt] + BUF * K1_STAGE + k * 12288);
      s16x4 hi = TRREAD(sm + c.bo[nt] + BUF * K1_STAGE + k * 12288 + 512);
      F.b[k][nt] = __builtin_shufflevector(lo, hi, 0, 1, 2, 3, 4, 5, 6, 7);
    }
  }
}
DI void dma16_asm(rsrc_t rs, unsigned lds, unsigned vo, int so) {
  asm volatile("s_mov_b32 m0, %2\n\ts_nop 0\n\tbuffer_load_dwordx4 %0, %1, %3 offen lds" :: "v"(vo), "s"(rs), "s"(lds), "s"(so));
}
DI void pf_asm(rsrc_t rs, unsigned lds, unsigned vo, int so) {
  asm volatile("s_mov_b32 m0, %2\n\ts_nop 0\n\tbuffer_load_dword %0, %1, %3 offen lds" :: "v"(vo), "s"(rs), "s"(lds), "s"(so));
}
template <int BUF, int I> DI void k1_issue1(const K1Ctx& c) {
  if (I < 2) dma16_asm(c.ra, c.ldsa + BUF * K1_STAGE + I * 1024, c.va[I < 2 ? I : 0], c.st * 128);
  else dma16_asm(c.rb, c.ldsb + BUF * K1_STAGE + (I - 2) * 1024, c.vb[I < 2 ? 0 : I - 2], c.st * 64 * c.rs);
}
template <bool SWAP, int I0, int I1> DI void k1_mma(f32x4 (&acc)[2][5], const Frags& F) {
#pragma unroll
  for (int i = I0; i < I1; ++i) {
    const int k = i / 10, mt = (i / 5) & 1, nt = i % 5;
    acc[mt][nt] = SWAP ? MFMA16(F.b[k][nt], F.a[k][mt], acc[mt][nt]) : MFMA16(F.a[k][mt], F.b[k][nt], acc[mt][nt]);
  }
}
template <bool SWAP, int N> DI void k1_step(f32x4 (&acc)[2][5], Frags& Fn, const Frags& Fp, lptr sm, K1Ctx& c) {
  WAIT_L0();
  BAR();
  SCHED();
  constexpr int NB = (N + 2) % 3;
  constexpr bool D = N + 2 <= 9;
  if (N > 0) k1_mma<SWAP, 0, 1>(acc, Fp);
  if (D) k1_issue1<NB, 0>(c);
  SCHED();
  if (N > 0) k1_mma<SWAP, 1, 2>(acc, Fp);
  if (D) k1_issue1<NB, 1>(c);
  SCHED();
  if (N > 0) k1_mma<SWAP, 2, 3>(acc, Fp);
  if (D) k1_issue1<NB, 2>(c);
  SCHED();
  if (N > 0) k1_mma<SWAP, 3, 4>(acc, Fp);
  if (D) k1_issue1<NB, 3>(c);
  SCHED();
  if (N > 0) k1_mma<SWAP, 4, 5>(acc, Fp);
  if (D) { k1_issue1<NB, 4>(c); c.st += 1; }
  SCHED();
  if (N % 3 != 0) {
    K1Ctx d = c; d.ao[0] += (N % 3) * K1_STAGE; d.ao[1] += (N % 3) * K1_STAGE;
#pragma unroll
    for (int nt = 0; nt < 5; ++nt) d.bo[nt] += (N % 3) * K1_STAGE;
    k1_read<0>(Fn, sm, d);
  } else k1_read<0>(Fn, sm, c);
  if (N > 0) {
    k1_mma<SWAP, 5, 20>(acc, Fp);
#pragma unroll
    for (int i = 0; i < 12; ++i) {
      __builtin_amdgcn_sched_group_barrier(0x100, 2, 0);
      __builtin_amdgcn_sched_group_barrier(0x008, 1, 0);
    }
    __builtin_amdgcn_sched_group_barrier(0x008, 3, 0);
  }
  SCHED();
}

__global__ __launch_bounds__(512, 2) void k1_kernel(const u16* __restrict__ x, const u16* __restrict__ wqkv,
                                                    const u16* __restrict__ wr, const u16* __restrict__ wi, const u16* __restrict__ wo,
                                                    char* __restrict__ ws) {
  extern __shared__ __attribute__((aligned(16))) char smem[];
  lptr sm = (lptr)smem;
  TS_DECL;
  TS(0);
  const int tid = threadIdx.x, lane = tid & 63;
  const int wave = __builtin_amdgcn_readfirstlane(tid >> 6);
  const int b = blockIdx.x;
  const int xcd = b & 7, idx = b >> 3;
  const int rt = (xcd & 1) * 8 + (idx & 7);
  const int ct = (xcd >> 1) * 4 + (idx >> 3);
  const bool dft = ct >= 12, iskv = ct >= 4 && ct < 12;
  const int c0 = iskv ? 80 * (ct - 4) : 160 * (ct & 3);
  const int L0 = c0 >> 6, p0 = (c0 & 63) >> 4;
  const int xs = (p0 == 0 || p0 == 3) ? 1 : 2;
  const int bigB = p0 >= 2 ? 1 : 0;

  K1Ctx c;
  c.wave = wave;
  c.ra = mk_rsrc(x, 2048 * 1280);
  c.rb = dft ? mk_rsrc(&FT, 819200) : mk_rsrc(wqkv, 640 * 3840);
  c.rs = dft ? 1280 : 3840;
  auto grow = [&](int row) { return dft ? 32 * (16 * (rt >> 2) + 4 * (row >> 5) + 2 * ((row >> 3) & 1) + ((row >> 4) & 1)) + 8 * (rt & 3) + (row & 7) : 128 * rt + row; };
#pragma unroll
  for (int i = 0; i < 2; ++i) {
    const int row = 16 * wave + 8 * i + (lane >> 3);
    c.va[i] = grow(row) * 1280 + ((lane & 7) ^ ((row >> 1) & 7)) * 16;
  }
  {
    const int row = lane >> 3, hf = lane & 1;
    const int pair = ((lane & 7) >> 1) ^ (((row >> 1) & 1) | ((wave & 1) << 1));
    const unsigned rowb = (8 * wave + row) * c.rs;
    const bool kown = bigB ? (pair >= p0) : (pair <= p0);
    c.vb[0] = rowb + (iskv ? 1280 + (L0 + bigB) * 128 : L0 * 128) + pair * 32 + hf * 16;
    c.vb[1] = rowb + (iskv ? 2560 + (L0 + bigB) * 128 + (hf ^ 1) * 16 : (L0 + 1) * 128 + hf * 16) + pair * 32;
    c.vb[2] = rowb + (iskv ? (L0 + 1 - bigB) * 128 + (kown ? 1280 + pair * 32 + hf * 16 : 2560 + (pair ^ xs) * 32 + (hf ^ 1) * 16)
                           : (L0 + 2) * 128 + pair * 32 + hf * 16);
  }
  c.ldsa = (unsigned)(size_t)sm + 2 * wave * 1024;
  c.ldsb = (unsigned)(size_t)sm + 16384 + 3 * wave * 1024;
  if (wave < 4) {
    const int a4 = dft ? 8 * 32 * 1280 : 64 * 1280, b4 = 32 * c.rs;
#pragma unroll
    for (int stg = 0; stg < 2; ++stg) {
#pragma unroll
      for (int h = 0; h < 2; ++h) {
#pragma unroll
        for (int i = 0; i < 2; ++i) dma16_asm(c.ra, c.ldsa + stg * K1_STAGE + h * 8192 + i * 1024, c.va[i], stg * 128 + h * a4);
#pragma unroll
        for (int i = 0; i < 3; ++i) dma16_asm(c.rb, c.ldsb + stg * K1_STAGE + h * 12288 + i * 1024, c.vb[i], stg * 64 * c.rs + h * b4);
      }
    }
  } else {
    const int l32 = lane & 31, sl = lane & 3, sg = 2 + 2 * (wave & 1) + (lane >> 5);
    const unsigned px = grow(32 * (idx >> 3) + l32) * 1280 + sg * 128;
    const unsigned pw = (64 * sg + 8 * (idx & 7) + (l32 >> 2)) * c.rs + (iskv ? 1280 * (1 + (sl >> 1)) + (L0 + (sl & 1)) * 128 : (L0 + min(sl, 2)) * 128);
    const unsigned junk = (unsigned)(size_t)sm + K1_JUNK + (wave & 3) * 256;
    const rsrc_t rp = (wave & 2) ? c.rb : c.ra;
    const unsigned vp = K1_PF ? ((wave & 2) ? pw : px) : 0x7fffffffu;
    pf_asm(rp, junk, vp, 0);
    pf_asm(rp, junk, vp, (wave & 2) ? 256 * c.rs : 512);
    const rsrc_t rx = (wave & 1) ? mk_rsrc(&GT, 491520) : mk_rsrc(wo, 819200);
    const int nl = (wave & 1) ? 15 : 25;
    pf_asm(rx, junk, (K1_PF && wave < 6 && lane < nl) ? (b * nl + lane) * 128 : 0x7fffffffu, 0);
  }
  c.st = 2;

  const int wm = wave >> 1, wn = wave & 1;
  const int r = lane & 15, g = lane >> 4, q = (lane & 15) >> 2, p = lane & 3;
  const int hsw = ((q >> 1) & 1) | ((g & 1) << 1);
  const unsigned bbase = 16384 + g * 3072 + q * 128;
  c.ao[0] = (32 * wm + r) * 128 + ((g ^ (r >> 1)) * 16);
  c.ao[1] = (32 * wm + r) * 128 + (((4 + g) ^ (r >> 1)) * 16);
  f32x4 acc[2][5];
  Frags F0, F1;

#define K1_HEAD(SWAP)                                                                          \
  _Pragma("unroll") for (int mt = 0; mt < 2; ++mt)                                             \
    _Pragma("unroll") for (int nt = 0; nt < 5; ++nt) asm volatile("" : "+v"(acc[mt][nt]));     \
  TS(1);                                                                                       \
  WAIT_V(10); k1_step<SWAP, 0>(acc, F0, F1, sm, c);                                            \
  TS(2); TC(5);                                                                                \
  if (wave < 4) WAIT_V(5);                                                                     \
  k1_step<SWAP, 1>(acc, F1, F0, sm, c);                                                        \
  WAIT_V(5); k1_step<SWAP, 2>(acc, F0, F1, sm, c);                                             \
  WAIT_V(5); k1_step<SWAP, 3>(acc, F1, F0, sm, c);                                             \
  WAIT_V(5); k1_step<SWAP, 4>(acc, F0, F1, sm, c);                                             \
  WAIT_V(5); k1_step<SWAP, 5>(acc, F1, F0, sm, c);                                             \
  WAIT_V(5); k1_step<SWAP, 6>(acc, F0, F1, sm, c);
#define K1_TC()

  if (iskv) {
#pragma unroll
    for (int nt = 0; nt < 5; ++nt) {
      const int u = 4 * nt + p;
      const int isv = u >= 10 ? 1 : 0;
      const int pc = 4 * p0 + 10 * wn + u - 10 * isv;
      const int gp = pc >> 2, big = ((gp >> 2) == bigB);
      const int slot = (gp & 3) ^ ((!big && isv) ? xs : 0);
      c.bo[nt] = bbase + (big ? isv : 2) * 1024 + ((slot ^ hsw) * 32) + (((pc & 3) ^ (isv << 1)) * 8);
      const float i0 = ((nt < 2) || (nt == 2 && r < 8)) ? kEps : 0.f;
#pragma unroll
      for (int mt = 0; mt < 2; ++mt) acc[mt][nt] = (f32x4){i0, i0, i0, i0};
    }
    unsigned ko[10];
    {
      constexpr int TI[10] = {0, 0, 1, 1, 1, 2, 2, 0, 1, 2}, TJ[10] = {2, 3, 2, 3, 4, 3, 4, -1, -1, -1};
#pragma unroll
      for (int i = 0; i < 10; ++i) {
        const int ti = TI[i], tj = TJ[i];
        const int ur = 16 * ti + r, hk = ur >= 20 ? 1 : 0;
        const int uc = 16 * tj + 4 * g - 40, hv = uc >= 20 ? 1 : 0;
        const bool valid = tj < 0 ? (ur < 40 && g == 0) : (ur < 40 && uc >= 0 && hk == hv);
        const int off = wave * 3840 + hk * 1920 + (ur - 20 * hk) * 96 + (tj < 0 ? 80 : (uc - 20 * hv) * 4);
        ko[i] = K1_STAGE + (valid ? off : 8 * 3840 + lane * 16);
        asm volatile("" : "+v"(ko[i]));
      }
    }
    K1_HEAD(false)
    WAIT_V(5); k1_step<false, 7>(acc, F1, F0, sm, c);
    WAIT_V(5); k1_step<false, 8>(acc, F0, F1, sm, c);
    WAIT_V(0); k1_step<false, 9>(acc, F1, F0, sm, c);
    k1_mma<false, 0, 20>(acc, F1);
    K1_TC();
    TS(3);
    bf16x8 fr[5];
#pragma unroll
    for (int nt = 0; nt < 5; ++nt) {
#pragma unroll
      for (int m = 0; m < 2; ++m)
#pragma unroll
        for (int j = 0; j < 4; ++j) {
          if (nt < 2) acc[m][nt][j] = phi_f(acc[m][nt][j]);
          if (nt == 2) { const float ph = phi_f(acc[m][nt][j]); acc[m][nt][j] = (r < 8) ? ph : acc[m][nt][j]; }
        }
      fr[nt] = pack8(acc[0][nt], acc[1][nt]);
    }
    const bf16x8 ones = {0x3f80, 0x3f80, 0x3f80, 0x3f80, 0x3f80, 0x3f80, 0x3f80, 0x3f80};
    {
      constexpr int TI[10] = {0, 0, 1, 1, 1, 2, 2, 0, 1, 2}, TJ[10] = {2, 3, 2, 3, 4, 3, 4, -1, -1, -1};
      const f32x4 zz = {0.f, 0.f, 0.f, 0.f};
      f32x4 d[10];
#pragma unroll
      for (int i = 0; i < 10; ++i) d[i] = MFMA16(TJ[i] < 0 ? ones : fr[TJ[i] < 0 ? 0 : TJ[i]], fr[TI[i]], zz);
#pragma unroll
      for (int i = 0; i < 10; ++i) *(LDS_AS f32x4*)(sm + ko[i]) = d[i];
    }
    WAIT_L0();
    BAR();
    const rsrc_t rk = mk_rsrc(ws + WS_KVP + ((size_t)rt * 32 + 4 * (ct - 4)) * 1920, 7680);
    LDS_AS f32x4* src = (LDS_AS f32x4*)(sm + K1_STAGE);
    const f32x4 s0 = (src[tid] + src[tid + 480]) + (src[tid + 960] + src[tid + 1440]);
    ST16(__builtin_bit_cast(u32x4, s0), rk, tid * 16, 0);
  } else {
#pragma unroll
    for (int nt = 0; nt < 5; ++nt) {
      const int gp = p0 + 5 * wn + nt;
      c.bo[nt] = bbase + (gp >> 2) * 1024 + (((gp & 3) ^ hsw) * 32) + p * 8;
    }
    const float i0 = (ct < 4) ? kEps : 0.f;
#pragma unroll
    for (int mt = 0; mt < 2; ++mt)
#pragma unroll
      for (int nt = 0; nt < 5; ++nt) acc[mt][nt] = (f32x4){i0, i0, i0, i0};
    K1_HEAD(true)
    const int un = (dft ? 64 : 0) + rt * 4 + (ct & 3), mo = un >> 1, fh = un & 1;
    {
      const rsrc_t rf = mk_rsrc((wave >> 2) ? wi : wr, 2048 * 642);
      const int kb = 16 * (mo >> 4) + 8 * (mo & 1) + 64 * (16 * ((mo >> 3) & 1) + 4 * ((mo >> 1) & 3) + (wave & 3));
      const unsigned v0 = (lane & 31) < 21 ? (lane >> 5) * 640 + (lane & 31) * 16 : 0x7fffffffu;
#pragma unroll
      for (int i = 0; i < 4; ++i)
        dma16_asm(rf, (unsigned)(size_t)sm + K1_WF + (wave * 4 + i) * 1024, v0, kb * 642 + 320 * fh + i * 1280);
    }
    WAIT_V(9); k1_step<true, 7>(acc, F1, F0, sm, c);
    WAIT_V(9); k1_step<true, 8>(acc, F0, F1, sm, c);
    WAIT_V(0); k1_step<true, 9>(acc, F1, F0, sm, c);
    k1_mma<true, 0, 20>(acc, F1);
    K1_TC();
    TS(3);
    if (ct < 4) {
      const rsrc_t rq = mk_rsrc(ws + WS_P + (size_t)(8 * rt + 2 * wm) * 20480 + (20 * ct + 10 * wn) * 256, 2 * 20480);
      const unsigned vo = (g & 1) * 20480 + (g >> 1) * 256 + r * 16;
#pragma unroll
      for (int nt = 0; nt < 5; ++nt) {
        const f32x4 v = acc[0][nt], w = acc[1][nt];
        const u32x2 a = {pk2(phi_f(v[0]), phi_f(v[1])), pk2(phi_f(v[2]), phi_f(v[3]))};
        const u32x2 d = {pk2(phi_f(w[0]), phi_f(w[1])), pk2(phi_f(w[2]), phi_f(w[3]))};
        ST16(widen(a, d), rq, vo + nt * 512, 0);
      }
    } else {
      const rsrc_t rx = mk_rsrc(ws + WS_X1 + (size_t)(80 * (ct - 12) + 40 * wn) * 8192 + ((rt >> 1) & 1) * 4096 + (rt >> 2) * 1024 + wm * 256 + (rt & 1) * 128, 40 * 8192);
      const unsigned vo = (2 * g + (r >> 3)) * 8192 + (r & 7) * 16;
      const bool up = r >= 8;
#pragma unroll
      for (int nt = 0; nt < 5; ++nt) {
        const unsigned a0 = pk2(acc[0][nt][0], acc[0][nt][1]), a1 = pk2(acc[1][nt][0], acc[1][nt][1]);
        const unsigned b0 = pk2(acc[0][nt][2], acc[0][nt][3]), b1 = pk2(acc[1][nt][2], acc[1][nt][3]);
        const unsigned t0 = __builtin_amdgcn_update_dpp(0u, up ? a0 : b0, 0x128, 0xf, 0xf, false);
        const unsigned t1 = __builtin_amdgcn_update_dpp(0u, up ? a1 : b1, 0x128, 0xf, 0xf, false);
        const u32x4 o = {up ? t0 : a0, up ? t1 : a1, up ? b0 : t0, up ? b1 : t1};
        ST16(o, rx, vo, nt * 8 * 8192);
      }
    }
    TS(4);
    {
      const rsrc_t rw = mk_rsrc(ws + WS_WT + (size_t)fh * 160 * 8192 + mo * 128, (160 + fh) * 8192 - mo * 128);
      const int i8 = lane & 7;
#pragma unroll
      for (int rd = 0; rd < 3; ++rd) {
        const int fl = (rd * 8 + wave) * 8 + (lane >> 3);
        lptr src = sm + K1_WF + i8 * 514 + min(fl, 160) * 2;
        u32x4 o;
#pragma unroll
        for (int j = 0; j < 4; ++j) o[j] = (unsigned)*(LDS_AS u16*)(src + j * 4096) | ((unsigned)*(LDS_AS u16*)(src + 16384 + j * 4096) << 16);
        ST16(o, rw, fl * 8192 + i8 * 16, 0);
      }
    }
  }
  TS(5);
  TS_FLUSH(0);
}

constexpr int K2_NFFT = 321, K2_NM = 80, K2_NP = 128;
constexpr int K2_LDS = 32768 + 4608;

__global__ __launch_bounds__(256) void k2_kernel(const u16* __restrict__ wo, const u16* __restrict__ x, const u16* __restrict__ gw, char* __restrict__ ws) {
  __shared__ __attribute__((aligned(16))) char smem[K2_LDS];
  lptr sm = (lptr)smem;
  TS_DECL;
  TS(0);
  const int tid = threadIdx.x, lane = tid & 63;
  const int wave = __builtin_amdgcn_readfirstlane(tid >> 6);
  const int b = blockIdx.x;
  const int r = lane & 15, g = lane >> 4;
  const f32x4 z4 = {0.f, 0.f, 0.f, 0.f};
  if (b < K2_NFFT) {
    const int f = b;
    const bool edge = (f == 0 || f == 320);
    {
      const rsrc_t rx = mk_rsrc(ws + WS_X1 + (size_t)(f == 320 ? 0 : f) * 8192, 8192);
      DMA16(rx, sm + wave * 2048, lane * 16, wave * 2048);
      DMA16(rx, sm + wave * 2048 + 1024, lane * 16, wave * 2048 + 1024);
    }
    const rsrc_t rw = mk_rsrc(ws + WS_WT + (size_t)f * 8192, 8192);
    u32x4 wv[2];
    wv[0] = __builtin_amdgcn_raw_buffer_load_b128(rw, lane * 16, wave * 2048, 0);
    wv[1] = __builtin_amdgcn_raw_buffer_load_b128(rw, lane * 16 + 1024, wave * 2048, 0);
    SCHED();
    u32x4 fre[4], fim[4], fc[2], fs[2], fn[2];
    float tc[2][4], tsn[2][4];
    {
      const float a64 = (float)(16 * wave + r) * (1.f / 64.f);
#pragma unroll
      for (int ks = 0; ks < 4; ++ks)
#pragma unroll
        for (int e = 0; e < 4; ++e) {
          const float ang = a64 * (float)(16 * ks + 4 * g + e);
          const float c = __builtin_amdgcn_cosf(ang), s = __builtin_amdgcn_sinf(ang);
          fre[ks][e] = pk2(c, s);
          fim[ks][e] = pk2(-s, c);
        }
#pragma unroll
      for (int t = 0; t < 2; ++t) {
        const float a32 = (float)(16 * t + r) * (1.f / 32.f);
#pragma unroll
        for (int e = 0; e < 4; ++e) {
          const int b0 = 16 * (e >> 1) + 4 * g + 2 * (e & 1);
          const float a0 = a32 * (float)b0, a1 = a0 + a32;
          const float s0 = __builtin_amdgcn_sinf(a0), s1 = __builtin_amdgcn_sinf(a1);
          fc[t][e] = pk2(__builtin_amdgcn_cosf(a0), __builtin_amdgcn_cosf(a1));
          fs[t][e] = pk2(s0, s1);
          fn[t][e] = fs[t][e] ^ 0x80008000u;
        }
#pragma unroll
        for (int j = 0; j < 4; ++j) {
          const float a = a64 * (1.f / 32.f) * (float)(16 * t + 4 * g + j);
          tc[t][j] = __builtin_amdgcn_cosf(a);
          tsn[t][j] = __builtin_amdgcn_sinf(a);
        }
      }
    }
#pragma unroll
    for (int ks = 0; ks < 4; ++ks) { asm volatile("" : "+v"(fre[ks]), "+v"(fim[ks])); }
#pragma unroll
    for (int t = 0; t < 2; ++t) {
      asm volatile("" : "+v"(fc[t]), "+v"(fs[t]), "+v"(fn[t]));
#pragma unroll
      for (int j = 0; j < 4; ++j) asm volatile("" : "+v"(tc[t][j]), "+v"(tsn[t][j]));
    }
    SCHED();
    TS(5);
    WAIT_V(0);
    BAR();
    SCHED();
    TS(1);
    f32x4 re[2], im[2];
#pragma unroll
    for (int t = 0; t < 2; ++t) {
      re[t] = z4; im[t] = z4;
#pragma unroll
      for (int ks = 0; ks < 4; ++ks) {
        u32x4 xa = *(LDS_AS u32x4*)(sm + (t * 4 + ks) * 1024 + lane * 16);
        if (edge) {
#pragma unroll
          for (int e = 0; e < 4; ++e) xa[e] = (f == 0) ? (xa[e] & 0xffffu) : (xa[e] >> 16);
        }
        re[t] = MFMA16(BC(xa), BC(fre[ks]), re[t]);
        im[t] = MFMA16(BC(xa), BC(fim[ks]), im[t]);
      }
    }
    TS(2);
#pragma unroll
    for (int t = 0; t < 2; ++t)
#pragma unroll
      for (int j = 0; j < 4; ++j) {
        const float a = re[t][j], c = im[t][j];
        re[t][j] = a * tc[t][j] + c * tsn[t][j];
        im[t][j] = c * tc[t][j] - a * tsn[t][j];
      }
    bf16x8 bre = pack8(re[0], re[1]), bim = pack8(im[0], im[1]);
#pragma unroll
    for (int t = 0; t < 2; ++t) {
      re[t] = MFMA16(BC(fc[t]), bre, z4); im[t] = MFMA16(BC(fc[t]), bim, z4);
      re[t] = MFMA16(BC(fs[t]), bim, re[t]); im[t] = MFMA16(BC(fn[t]), bre, im[t]);
    }
    const float sc = (edge ? 1.f : 2.f) / (2048.f * 640.f);
#pragma unroll
    for (int t = 0; t < 2; ++t)
#pragma unroll
      for (int j = 0; j < 4; ++j) {
        const float a = re[t][j], c = im[t][j], wre = bf_lo(wv[t][j]) * sc, wim = bf_hi(wv[t][j]) * sc;
        re[t][j] = a * wre - c * wim;
        im[t][j] = a * wim + c * wre;
      }
    bre = pack8(re[0], re[1]); bim = pack8(im[0], im[1]);
#pragma unroll
    for (int t = 0; t < 2; ++t) {
      re[t] = MFMA16(BC(fc[t]), bre, z4); im[t] = MFMA16(BC(fc[t]), bim, z4);
      re[t] = MFMA16(BC(fn[t]), bim, re[t]); im[t] = MFMA16(BC(fs[t]), bre, im[t]);
    }
#pragma unroll
    for (int t = 0; t < 2; ++t)
#pragma unroll
      for (int j = 0; j < 4; ++j) {
        const float a = re[t][j], c = im[t][j];
        const float ur = a * tc[t][j] - c * tsn[t][j];
        const float ui = -(c * tc[t][j]) - a * tsn[t][j];
        const int s2 = 16 * t + 4 * g + j;
        *(LDS_AS unsigned*)(sm + 8192 + s2 * 256 + (((4 * wave + (r >> 2)) ^ (s2 & 15)) * 16) + (r & 3) * 4) = pk2(ur, ui);
      }
    WAIT_L0();
    BAR();
    TS(3);
    u32x2 pr[2], pi[2];
#pragma unroll
    for (int t = 0; t < 2; ++t) {
      f32x4 yr = z4, yi = z4;
#pragma unroll
      for (int ks = 0; ks < 4; ++ks) {
        const bf16x8 u = *(LDS_AS bf16x8*)(sm + 8192 + (16 * t + r) * 256 + (((4 * ks + g) ^ r) * 16));
        yr = MFMA16(u, BC(fre[ks]), yr);
        yi = MFMA16(u, BC(fim[ks]), yi);
      }
      pr[t] = (u32x2){pk2(yr[0], yr[1]), pk2(yr[2], yr[3])};
      pi[t] = (u32x2){pk2(-yi[0], -yi[1]), pk2(-yi[2], -yi[3])};
    }
    const unsigned vo = wave * 1024 + r * 64 + (g & 1) * 32 + (g >> 1) * 16;
    const rsrc_t ry = mk_rsrc(ws + WS_Y + (f == 320 ? (size_t)320 * 4096 : (size_t)f * 4096), 321 * 4096);
    ST16(widen(pr[0], pr[1]), ry, vo, 0);
    if (!edge) ST16(widen(pi[0], pi[1]), ry, vo, 320 * 4096);
  } else if (b < K2_NFFT + K2_NM) {
    const int mb = b - K2_NFFT, ks = mb >> 2, cp = mb & 3;
    const int hf = (32 * ks) / 20, e0 = 20 * hf;
    const int L1 = (160 * cp) >> 7, L2 = (1120 - 160 * cp) >> 7;
    {
      const rsrc_t rwo = mk_rsrc(wo, 640 * 1280);
      const int line = (wave < 2 ? L1 : L2) + (wave & 1);
      const int row = lane >> 3;
#pragma unroll
      for (int kb = 0; kb < 8; ++kb) {
        const int pair = ((lane & 7) >> 1) ^ (((row >> 1) & 1) | ((kb & 1) << 1));
        DMA16(rwo, sm + (wave * 8 + kb) * 1024, min(e0 + 8 * kb + row, 639) * 1280 + line * 128 + pair * 32 + (lane & 1) * 16, 0);
      }
    }
    const int kl = min(tid, 159) / 5, qd = min(tid, 159) - kl * 5;
    const int k = 32 * ks + kl, h = k / 20, d = k - 20 * h;
    const rsrc_t rk = mk_rsrc(ws + WS_KVP, 983040);
    f32x4 v[16];
#pragma unroll
    for (int i = 0; i < 16; ++i) v[i] = __builtin_bit_cast(f32x4, __builtin_amdgcn_raw_buffer_load_b128(rk, h * 1920 + d * 96 + qd * 16, i * 61440, 0));
    lptr ab = sm + 32768;
    *(LDS_AS u32x4*)(ab + tid * 16) = (u32x4){0u, 0u, 0u, 0u};
    if (tid < 32) *(LDS_AS u32x4*)(ab + 4096 + tid * 16) = (u32x4){0u, 0u, 0u, 0u};
    SCHED();
    f32x4 sum = z4;
#pragma unroll
    for (int i = 0; i < 16; ++i) sum += v[i];
    WAIT_V(0);
    __syncthreads();
    if (tid < 160) *(LDS_AS u32x2*)(ab + kl * 144 + (20 * (h - hf) + 4 * qd) * 2) = (u32x2){pk2(sum[0], sum[1]), pk2(sum[2], sum[3])};
    __syncthreads();
    bf16x8 a[2][2];
#pragma unroll
    for (int t = 0; t < 2; ++t)
#pragma unroll
      for (int s = 0; s < 2; ++s) a[t][s] = *(LDS_AS bf16x8*)(ab + (8 * (r >> 2) + (r & 3) + 4 * t) * 144 + s * 64 + g * 16);
    const int q = r >> 2, p = lane & 3;
    const int hsw = ((q >> 1) & 1) | ((g & 1) << 1);
    const rsrc_t rm = mk_rsrc(ws + WS_M, 819200);
#pragma unroll
    for (int n = 0; n < 3; ++n) {
      const int tl = min(wave + 4 * n, 9);
      const int ctl = tl >= 5 ? 1 : 0, nt = tl - 5 * ctl, ctg = 2 * cp + ctl;
      const int i = 16 * nt + 4 * p;
      const int byte = 2 * (i < 40 ? 40 * ctg + i : 560 - 40 * ctg + i);
      const int pl = i < 40 ? (byte >> 7) - L1 : 2 + (byte >> 7) - L2;
      lptr bp = sm + (pl * 8 + g) * 1024 + q * 128 + ((((byte >> 5) & 3) ^ hsw) * 32) + (byte & 31);
      bf16x8 bw[2];
#pragma unroll
      for (int s = 0; s < 2; ++s) {
        const s16x4 lo = TRREAD(bp + s * 4096), hi = TRREAD(bp + s * 4096 + 512);
        bw[s] = __builtin_shufflevector(lo, hi, 0, 1, 2, 3, 4, 5, 6, 7);
      }
      f32x4 d1 = MFMA16(a[0][0], bw[0], z4), d2 = MFMA16(a[1][0], bw[0], z4);
      d1 = MFMA16(a[0][1], bw[1], d1); d2 = MFMA16(a[1][1], bw[1], d2);
      if (wave + 4 * n < 10) ST16(__builtin_bit_cast(u32x4, pack8(d1, d2)), rm, lane * 16, (10 * cp + tl) * 20480 + ks * 1024);
    }
  } else {
    const int pb = b - K2_NFFT - K2_NM, rb = pb >> 4, hp = pb & 15;
    float zv[4];
    {
      const int zi = min(tid >> 2, 39);
      const rsrc_t rk = mk_rsrc(ws + WS_KVP + (size_t)hp * 3840 + 80, 983040);
      const unsigned zo = (zi >= 20 ? 1920 + (zi - 20) * 96 : zi * 96) + (tid & 3) * 4 * 61440;
#pragma unroll
      for (int i = 0; i < 4; ++i) zv[i] = __uint_as_float(__builtin_amdgcn_raw_buffer_load_b32(rk, zo, i * 61440, 0));
    }
    const rsrc_t rp = mk_rsrc(ws + WS_P + (size_t)(16 * rb) * 20480 + hp * 1280, 16 * 20480);
    const unsigned vo = (tid >> 4) * 20480 + (tid & 15) * 16;
    u32x4 q[5], gx[5], gg[5];
#pragma unroll
    for (int i = 0; i < 5; ++i) q[i] = __builtin_amdgcn_raw_buffer_load_b128(rp, vo + i * 256, 0, 0);
    {
      const rsrc_t rx = mk_rsrc(x + (size_t)pb * 16 * 640, 16 * 1280), rg = mk_rsrc(gw, 1280);
#pragma unroll
      for (int i = 0; i < 5; ++i) {
        gx[i] = __builtin_amdgcn_raw_buffer_load_b128(rx, tid * 80 + i * 16, 0, 0);
        gg[i] = __builtin_amdgcn_raw_buffer_load_b128(rg, (tid & 15) * 80 + i * 16, 0, 0);
      }
    }
    SCHED();
    float zsum = (zv[0] + zv[1]) + (zv[2] + zv[3]);
    zsum += __shfl_xor(zsum, 1);
    zsum += __shfl_xor(zsum, 2);
    if (tid < 160 && (tid & 3) == 0) *(LDS_AS u16*)(sm + (tid >> 2) * 2) = (u16)pk2(zsum, zsum);
    __syncthreads();
    float den0 = kEps, den1 = kEps;
#pragma unroll
    for (int i = 0; i < 5; ++i) {
      const u32x4 z = *(LDS_AS u32x4*)(sm + i * 16);
#pragma unroll
      for (int e = 0; e < 4; ++e) {
        if (4 * i + e < 10) den0 = dot2(q[i][e], z[e], den0); else den1 = dot2(q[i][e], z[e], den1);
      }
    }
    const float inv0 = __builtin_amdgcn_rcpf(den0), inv1 = __builtin_amdgcn_rcpf(den1);
#pragma unroll
    for (int i = 0; i < 5; ++i) {
      u32x4 o;
#pragma unroll
      for (int e = 0; e < 4; ++e) {
        const float iv = (4 * i + e < 10) ? inv0 : inv1;
        const f32x2 v = (f32x2){bf_lo(q[i][e]), bf_hi(q[i][e])} * (f32x2){iv, iv};
        o[e] = pk2(v[0], v[1]);
      }
      ST16(o, rp, vo + i * 256, 0);
    }
    float s0 = 0.f, s1 = 0.f;
#pragma unroll
    for (int i = 0; i < 5; ++i) { s0 = dot2(gx[i][0], gg[i][0], s0); s1 = dot2(gx[i][1], gg[i][1], s1); s0 = dot2(gx[i][2], gg[i][2], s0); s1 = dot2(gx[i][3], gg[i][3], s1); }
    float s = s0 + s1;
#pragma unroll
    for (int o = 8; o > 0; o >>= 1) s += __shfl_xor(s, o);
    if ((tid & 15) == 0) __builtin_amdgcn_raw_buffer_store_b32(__float_as_uint(1.f / (1.f + __expf(-s))), mk_rsrc(ws + WS_G, 8192), (pb * 16 + (tid >> 4)) * 4, 0, ST_AUX);
  }
  TS(4);
  TS_FLUSH(1);
}

constexpr int K3_LDS = 2 * 71680 + 256;

__global__ __launch_bounds__(512, 2) void k3_kernel(const char* __restrict__ ws, u16* __restrict__ out) {
  extern __shared__ __attribute__((aligned(16))) char smem[];
  lptr sm = (lptr)smem;
  const int tid = threadIdx.x, lane = tid & 63;
  const int wave = __builtin_amdgcn_readfirstlane(tid >> 6);
  const int b = blockIdx.x, xcd = b & 7, idx = b >> 3;
  TS_DECL;
  TS(0);
  const int rt = (xcd & 3) * 8 + (idx & 7);
  const int ct = (xcd >> 2) * 4 + (idx >> 3);
  const int r = lane & 15, g = lane >> 4;
  LDS_AS float* red = (LDS_AS float*)sm;
  constexpr int RA = 64 * 84, RE = 64 * 52;
  if (wave < 2) {
    const rsrc_t ra = mk_rsrc(ws + WS_P + (size_t)(4 * rt) * 20480 + wave * 10240, 4 * 20480);
    const rsrc_t rb = mk_rsrc(ws + WS_M + (size_t)(5 * ct) * 20480 + wave * 10240, 5 * 20480);
    const unsigned vo = lane * 16;
    f32x4 acc[4][5];
#pragma unroll
    for (int mt = 0; mt < 4; ++mt)
#pragma unroll
      for (int nt = 0; nt < 5; ++nt) acc[mt][nt] = (f32x4){0.f, 0.f, 0.f, 0.f};
    u32x4 fa[10][4], fb[10][5];
    constexpr int PF = 4;
    auto ld = [&](int ks) {
#pragma unroll
      for (int nt = 0; nt < 5; ++nt) fb[ks][nt] = __builtin_amdgcn_raw_buffer_load_b128(rb, vo + (ks & 3) * 1024, nt * 20480 + (ks >> 2) * 4096, 0);
#pragma unroll
      for (int mt = 0; mt < 4; ++mt) fa[ks][mt] = __builtin_amdgcn_raw_buffer_load_b128(ra, vo + (ks & 3) * 1024, mt * 20480 + (ks >> 2) * 4096, 0);
    };
#pragma unroll
    for (int ks = 0; ks < PF; ++ks) { ld(ks); SCHED(); }
    TS(1);
#pragma unroll
    for (int ks = 0; ks < 10; ++ks) {
#pragma unroll
      for (int mt = 0; mt < 4; ++mt)
#pragma unroll
        for (int nt = 0; nt < 5; ++nt) acc[mt][nt] = MFMA16(BC(fb[ks][nt]), BC(fa[ks][mt]), acc[mt][nt]);
      SCHED();
      if (ks == 0) TS(2);
      if (ks + PF < 10) ld(ks + PF);
      SCHED();
    }
    TS(3);
    BAR();
#pragma unroll
    for (int mt = 0; mt < 4; ++mt)
#pragma unroll
      for (int nt = 0; nt < 5; ++nt) *(LDS_AS f32x4*)(red + wave * RA + (16 * mt + r) * 84 + 16 * nt + 4 * g) = acc[mt][nt];
  } else if (wave < 4) {
    const int od = wave - 2;
    lptr yb = sm + od * 71680;
    const unsigned ybu = (unsigned)(size_t)yb;
    const rsrc_t ry = mk_rsrc(ws + WS_Y + (size_t)od * 320 * 4096 + rt * 128, 320 * 4096);
    const rsrc_t rg = mk_rsrc((const char*)&GT + (size_t)(2 * ct + od) * 30720, 30720);
    const int row = lane >> 3;
    const unsigned vy0 = row * 4096 + ((((lane & 7) >> 1) ^ ((row >> 1) & 1)) * 32) + (lane & 1) * 16;
    const unsigned vy1 = vy0 ^ 64, vg = lane * 16;
    auto issue = [&](int ks) {
#pragma unroll
      for (int i = 0; i < 4; ++i) dma16_asm(ry, ybu + (ks * 4 + i) * 1024, (i & 1) ? vy1 : vy0, (ks * 4 + i) * 32768);
#pragma unroll
      for (int i = 0; i < 3; ++i) dma16_asm(rg, ybu + 40960 + (ks * 3 + i) * 1024, vg, (ks * 3 + i) * 1024);
    };
    issue(0); issue(1); issue(2);
    TS(1);
    const int q = r >> 2, p = lane & 3;
    const int hsw = ((q >> 1) & 1) | ((g & 1) << 1);
    unsigned yo[4];
#pragma unroll
    for (int mt = 0; mt < 4; ++mt) yo[mt] = g * 1024 + q * 128 + ((mt ^ hsw) * 32) + p * 8;
    lptr gb = yb + 40960 + lane * 16;
    f32x4 acc[4][3];
#pragma unroll
    for (int mt = 0; mt < 4; ++mt)
#pragma unroll
      for (int nt = 0; nt < 3; ++nt) acc[mt][nt] = (f32x4){0.f, 0.f, 0.f, 0.f};
#define K3_STEP(ks, n)                                                                                         \
    {                                                                                                          \
      WAIT_V(n);                                                                                               \
      SCHED();                                                                                                 \
      bf16x8 fy[4], fg[3];                                                                                     \
      _Pragma("unroll") for (int nt = 0; nt < 3; ++nt) fg[nt] = *(LDS_AS bf16x8*)(gb + (ks * 3 + nt) * 1024);  \
      _Pragma("unroll") for (int mt = 0; mt < 4; ++mt) {                                                       \
        const s16x4 lo = TRREAD(yb + yo[mt] + ks * 4096), hi = TRREAD(yb + yo[mt] + ks * 4096 + 512);          \
        fy[mt] = __builtin_shufflevector(lo, hi, 0, 1, 2, 3, 4, 5, 6, 7);                                      \
      }                                                                                                        \
      SCHED();                                                                                                 \
      if (ks + 3 < 10) issue(ks + 3);                                                                          \
      SCHED();                                                                                                 \
      _Pragma("unroll") for (int mt = 0; mt < 4; ++mt)                                                         \
        _Pragma("unroll") for (int nt = 0; nt < 3; ++nt) acc[mt][nt] = MFMA16(fg[nt], fy[mt], acc[mt][nt]);   \
      SCHED();                                                                                                 \
    }
    K3_STEP(0, 14) TS(2); K3_STEP(1, 14) K3_STEP(2, 14) K3_STEP(3, 14) K3_STEP(4, 14)
    K3_STEP(5, 14) K3_STEP(6, 14) K3_STEP(7, 14) K3_STEP(8, 7) K3_STEP(9, 0)
    TS(3);
    BAR();
#pragma unroll
    for (int mt = 0; mt < 4; ++mt)
#pragma unroll
      for (int nt = 0; nt < 3; ++nt) *(LDS_AS f32x4*)(red + 2 * RA + od * RE + (16 * mt + r) * 52 + 16 * nt + 4 * g) = acc[mt][nt];
  } else {
    if (wave == 4) red[2 * 17920 + lane] = ((const float*)(ws + WS_G))[64 * rt + lane];
    WAIT_L0();
    BAR();
  }
  const rsrc_t ro = mk_rsrc(out + (size_t)(64 * rt) * 640, 64 * 1280);
  const int nit = (wave == 4 || wave == 5) ? 2 : 1;
  int row[2], gr[2];
  float gq[2];
#pragma unroll
  for (int i = 0; i < 2; ++i) {
    const int it = min(i ? 256 + tid : tid, 639);
    row[i] = it / 10; gr[i] = it - row[i] * 10;
    gq[i] = red[2 * 17920 + row[i]];
  }
  WAIT_L0();
  BAR();
  TS(5);
#pragma unroll
  for (int i = 0; i < 2; ++i) {
    if (i < nit) {
      LDS_AS float* pa = red + row[i] * 84 + 8 * gr[i];
      const f32x4 a0 = *(LDS_AS f32x4*)pa + *(LDS_AS f32x4*)(pa + RA);
      const f32x4 a1 = *(LDS_AS f32x4*)(pa + 4) + *(LDS_AS f32x4*)(pa + RA + 4);
      const bool lo = gr[i] < 5;
      LDS_AS float* pe = red + 2 * RA + row[i] * 52;
      const float yn = pe[RE + 41];
      LDS_AS float* pq = pe + (lo ? 8 * gr[i] : 72 - 8 * gr[i]);
      const f32x4 e0 = *(LDS_AS f32x4*)pq, e1 = *(LDS_AS f32x4*)(pq + 4), o0 = *(LDS_AS f32x4*)(pq + RE), o1 = *(LDS_AS f32x4*)(pq + RE + 4);
      const float e8 = pq[8], o8 = pq[RE + 8];
      const f32x4 m0 = e0 - o0, m1 = e1 - o1, p0 = e0 + o0, p1 = e1 + o1;
      const float p8 = e8 + o8;
      const f32x4 yy = {yn, -yn, yn, -yn};
      const f32x4 b0 = (lo ? m0 : (f32x4){p8, p1[3], p1[2], p1[1]}) + yy;
      const f32x4 b1 = (lo ? m1 : (f32x4){p1[0], p0[3], p0[2], p0[1]}) + yy;
      const f32x4 r0 = b0 + gq[i] * (a0 - b0), r1 = b1 + gq[i] * (a1 - b1);
      __builtin_amdgcn_raw_buffer_store_b128(__builtin_bit_cast(u32x4, pack8(r0, r1)), ro, row[i] * 1280 + (lo ? 80 * ct : 1120 - 80 * ct) + 16 * gr[i], 0, OUT_AUX);
    }
  }
  TS(4);
  TS_FLUSH(2);
}

extern "C" void kernel_launch(void* const* d_in, const int* in_sizes, int n_in, void* d_out, int out_size, void* d_ws,
                              size_t ws_size, hipStream_t stream) {
  const u16* x = (const u16*)d_in[0];
  const u16* wqkv = (const u16*)d_in[1];
  const u16* wo = (const u16*)d_in[2];
  const u16* wr = (const u16*)d_in[4];
  const u16* wi = (const u16*)d_in[5];
  const u16* gw = (const u16*)d_in[6];
  char* ws = (char*)d_ws;
  (void)hipFuncSetAttribute((const void*)k1_kernel, hipFuncAttributeMaxDynamicSharedMemorySize, K1_LDS);
  (void)hipFuncSetAttribute((const void*)k3_kernel, hipFuncAttributeMaxDynamicSharedMemorySize, K3_LDS);
  k1_kernel<<<256, 512, K1_LDS, stream>>>(x, wqkv, wr, wi, wo, ws);
  k2_kernel<<<K2_NFFT + K2_NM + K2_NP, 256, 0, stream>>>(wo, x, gw, ws);
  k3_kernel<<<256, 512, K3_LDS, stream>>>(ws, (u16*)d_out);
}
```

```cpp
#include <hip/hip_runtime.h>
#include <hip/hip_bf16.h>
#include <utility>

typedef unsigned short u16;
typedef __attribute__((ext_vector_type(8))) short bf16x8;
typedef __attribute__((ext_vector_type(4))) short s16x4;
typedef __attribute__((ext_vector_type(4))) float f32x4;
typedef __attribute__((ext_vector_type(2))) float f32x2;
typedef __attribute__((ext_vector_type(2))) __bf16 bf16x2_t;
typedef __attribute__((ext_vector_type(4))) unsigned u32x4;
typedef __attribute__((ext_vector_type(2))) unsigned u32x2;
#define LDS_AS __attribute__((address_space(3)))
#define DI __device__ __forceinline__
#define MFMA16(a, b, c) __builtin_amdgcn_mfma_f32_16x16x32_bf16((a), (b), (c), 0, 0, 0)
typedef LDS_AS char* lptr;
typedef __amdgpu_buffer_rsrc_t rsrc_t;

#define ST_AUX 16
#define OUT_AUX 0
#define K1_ROT 5
#define FUSED 1
#define LD_AUX 16

constexpr double kPi = 3.14159265358979323846;
constexpr double ccos_s(double x) { double x2 = x * x, t = 1, s = 1; for (int i = 1; i < 12; ++i) { t *= -x2 / ((2 * i - 1) * (2 * i)); s += t; } return s; }
constexpr double csin_s(double x) { double x2 = x * x, t = x, s = x; for (int i = 1; i < 12; ++i) { t *= -x2 / ((2 * i) * (2 * i + 1)); s += t; } return s; }
constexpr double cos640(int m) {
  m %= 640; if (m > 320) m = 640 - m;
  bool neg = false; if (m > 160) { m = 320 - m; neg = true; }
  double v = (m <= 80) ? ccos_s(2 * kPi * m / 640) : csin_s(2 * kPi * (160 - m) / 640);
  return neg ? -v : v;
}
constexpr u16 to_bf16c(double v) { float f = (float)v; unsigned u = __builtin_bit_cast(unsigned, f); u += 0x7fffu + ((u >> 16) & 1u); return (u16)(u >> 16); }
struct C640 { u16 v[640]; };
constexpr C640 make_c640() { C640 c{}; for (int i = 0; i < 640; ++i) c.v[i] = to_bf16c(cos640(i)); return c; }
constexpr C640 COS640 = make_c640();

struct alignas(128) FRow { u16 v[16 * 640]; };
constexpr FRow make_frow(int dt) {
  FRow t{};
  for (int r = 0; r < 16; ++r) {
    const int d = dt * 16 + r;
    for (int kk = 0; kk < 640; ++kk) {
      const int m = (kk == 1) ? (d * 320) % 640 : (d * (kk >> 1) + 160 * (kk & 1)) % 640;
      t.v[r * 640 + kk] = COS640.v[m];
    }
  }
  return t;
}
template <int I> constexpr FRow FROW = make_frow(I);
struct alignas(128) FTab { FRow r[40]; };
template <int... I> constexpr FTab make_ftab(std::integer_sequence<int, I...>) { return FTab{{FROW<I>...}}; }
__device__ const FTab FT = make_ftab(std::make_integer_sequence<int, 40>{});

struct alignas(128) GRow { u16 v[10 * 3 * 512]; };
constexpr GRow make_grow(int id) {
  GRow t{};
  const int ct = id >> 1, od = id & 1;
  for (int ks = 0; ks < 10; ++ks) for (int nt = 0; nt < 3; ++nt) for (int l = 0; l < 64; ++l) {
    const int ci = 16 * nt + (l & 15), d = 40 * ct + ci;
    for (int j = 0; j < 8; ++j) {
      const int f = 32 * ks + 8 * (l >> 4) + j;
      t.v[((ks * 3 + nt) * 64 + l) * 8 + j] = (ci <= 40) ? COS640.v[(f * d + (od ? 480 : 0)) % 640] : (u16)((od && ci == 41 && f == 0) ? 0x3f80 : 0);
    }
  }
  return t;
}
template <int I> constexpr GRow GROW = make_grow(I);
struct alignas(128) GTab { GRow r[16]; };
template <int... I> constexpr GTab make_gtab(std::integer_sequence<int, I...>) { return GTab{{GROW<I>...}}; }
__device__ const GTab GT = make_gtab(std::make_integer_sequence<int, 16>{});

constexpr size_t WS_P   = 0;
constexpr size_t WS_Y   = WS_P + 2621440;
constexpr size_t WS_X1  = WS_Y + 2621440;
constexpr size_t WS_WT  = WS_X1 + 2621440;
constexpr size_t WS_KVP = WS_WT + 352 * 8192;
constexpr size_t WS_M   = WS_KVP + 983040;
constexpr size_t WS_G   = WS_M + 819200;
constexpr size_t WS_ZP  = WS_G + 8192;
constexpr size_t WS_SYN = WS_ZP + 40960;

DI rsrc_t mk_rsrc(const void* p, unsigned bytes) { return __builtin_amdgcn_make_buffer_rsrc((void*)p, 0, bytes, 0x00020000); }
DI unsigned pk2(float a, float b) { f32x2 v = {a, b}; return __builtin_bit_cast(unsigned, __builtin_convertvector(v, bf16x2_t)); }
DI float bf_lo(unsigned u) { return __uint_as_float(u << 16); }
DI float bf_hi(unsigned u) { return __uint_as_float(u & 0xffff0000u); }
DI float dot2(unsigned a, unsigned b, float c) { return __builtin_amdgcn_fdot2_f32_bf16(__builtin_bit_cast(bf16x2_t, a), __builtin_bit_cast(bf16x2_t, b), c, false); }
DI float rbf(float a) { return bf_lo(pk2(a, a)); }
constexpr float kEps = 1e-6f, kL2E = 1.4426950408889634f;
DI float phi_f(float a) {
  float e;
  const float u = __builtin_fmaf(a, kL2E, -kEps * kL2E);
  asm("v_exp_f32_e64 %0, %1 clamp" : "=v"(e) : "v"(u));
  return __builtin_fmaxf(a, kEps) + e;
}
DI bf16x8 pack8(const f32x4& a, const f32x4& b) {
  u32x4 p = {pk2(a[0], a[1]), pk2(a[2], a[3]), pk2(b[0], b[1]), pk2(b[2], b[3])};
  return __builtin_bit_cast(bf16x8, p);
}
DI u32x4 widen(u32x2 a, u32x2 b) {
  auto x = __builtin_amdgcn_permlane16_swap(a[0], b[0], false, false);
  auto y = __builtin_amdgcn_permlane16_swap(a[1], b[1], false, false);
  return (u32x4){x[0], y[0], x[1], y[1]};
}
#define WAIT_V(n) asm volatile("s_waitcnt vmcnt(" #n ")" ::: "memory")
#define BC(v) __builtin_bit_cast(bf16x8, (v))
#define WAIT_L0() __builtin_amdgcn_s_waitcnt(0xC07F)
#define BAR() __builtin_amdgcn_s_barrier()
#define SCHED() __builtin_amdgcn_sched_barrier(0)
#define DMA16(rs, dst, vo, so) __builtin_amdgcn_raw_ptr_buffer_load_lds((rs), (LDS_AS void*)(dst), 16, (vo), (so), 0, 0)
#define TRREAD(p) __builtin_amdgcn_ds_read_tr16_b64_v4i16((LDS_AS s16x4*)(p))
#define ST16(v, rs, vo, so) __builtin_amdgcn_raw_buffer_store_b128((v), (rs), (vo), (so), ST_AUX)

#define TS_DECL
#define TS(i)
#define TC(i)
#define TS_FLUSH(kern)
#define TS_FLUSH2()
#define TS_ARG
#define TS_PASS

constexpr int K1_STAGE = 40960;
constexpr int K1_WF = 3 * K1_STAGE;
constexpr int K1_JUNK = K1_WF + 32768;
constexpr int K1_LDS = K1_JUNK + 2048;
#define K1_PF 1

struct Frags { bf16x8 a[2][2]; bf16x8 b[2][5]; };
struct K1Ctx { rsrc_t ra, rb; unsigned va[2], vb[3], ao[3][2], bo[3][5], ldsa, ldsb; int st, rs, wave; };

struct Half { s16x4 lo, hi; };
template <int I0, int I1> DI void k1_read(Frags& F, lptr sm, const unsigned (&ao)[2], const unsigned (&bo)[5]) {
#pragma unroll
  for (int i = I0; i < I1; ++i) {
    const int k = i / 7, j = i % 7;
    if (j < 2) F.a[k][j] = *(LDS_AS bf16x8*)(sm + ao[k] + j * 2048);
    else {
      const s16x4 lo = TRREAD(sm + bo[j - 2] + k * 12288), hi = TRREAD(sm + bo[j - 2] + k * 12288 + 512);
      F.b[k][j - 2] = __builtin_shufflevector(lo, hi, 0, 1, 2, 3, 4, 5, 6, 7);
    }
  }
}
DI void dma16_asm(rsrc_t rs, unsigned lds, unsigned vo, int so) {
  asm volatile("s_mov_b32 m0, %2\n\ts_nop 0\n\tbuffer_load_dwordx4 %0, %1, %3 offen lds" :: "v"(vo), "s"(rs), "s"(lds), "s"(so));
}
DI void pf_asm(rsrc_t rs, unsigned lds, unsigned vo, int so) {
  asm volatile("s_mov_b32 m0, %2\n\ts_nop 0\n\tbuffer_load_dword %0, %1, %3 offen lds" :: "v"(vo), "s"(rs), "s"(lds), "s"(so));
}
template <int BUF, int I> DI void k1_issue1(const K1Ctx& c) {
  if (I < 2) dma16_asm(c.ra, c.ldsa + BUF * K1_STAGE + I * 1024, c.va[I < 2 ? I : 0], c.st * 128);
  else dma16_asm(c.rb, c.ldsb + BUF * K1_STAGE + (I - 2) * 1024, c.vb[I < 2 ? 0 : I - 2], c.st * 64 * c.rs);
}
template <int BUF> DI void k1_issue(K1Ctx& c) {
  k1_issue1<BUF, 0>(c); k1_issue1<BUF, 1>(c); k1_issue1<BUF, 2>(c); k1_issue1<BUF, 3>(c); k1_issue1<BUF, 4>(c);
  c.st = (c.st == 9) ? 0 : c.st + 1;
}
template <bool SWAP, int I0, int I1> DI void k1_mma(f32x4 (&acc)[2][5], const Frags& F) {
#pragma unroll
  for (int i = I0; i < I1; ++i) {
    const int k = i / 10, mt = (i / 5) & 1, nt = i % 5;
    acc[mt][nt] = SWAP ? MFMA16(F.b[k][nt], F.a[k][mt], acc[mt][nt]) : MFMA16(F.a[k][mt], F.b[k][nt], acc[mt][nt]);
  }
}
template <bool SWAP, int N, int SEG> DI void k1_seg(f32x4 (&acc)[2][5], Frags& Fn, const Frags& Fp, lptr sm, K1Ctx& c) {
  constexpr int R0[6] = {0, 4, 7, 10, 12, 14};
  k1_read<R0[SEG], R0[SEG + 1]>(Fn, sm, c.ao[N % 3], c.bo[N % 3]);
  if (N > 0) {
    k1_mma<SWAP, 4 * SEG, 4 * SEG + 4>(acc, Fp);
    if (SEG < 2) {
      __builtin_amdgcn_sched_group_barrier(0x008, 1, 0); __builtin_amdgcn_sched_group_barrier(0x100, 2, 0);
      __builtin_amdgcn_sched_group_barrier(0x008, 1, 0); __builtin_amdgcn_sched_group_barrier(0x100, 2, 0);
      __builtin_amdgcn_sched_group_barrier(0x008, 1, 0); __builtin_amdgcn_sched_group_barrier(0x100, 2, 0);
      __builtin_amdgcn_sched_group_barrier(0x008, 1, 0);
    } else {
#pragma unroll
      for (int i = 0; i < 4; ++i) { __builtin_amdgcn_sched_group_barrier(0x008, 1, 0); __builtin_amdgcn_sched_group_barrier(0x100, 2, 0); }
    }
  }
  SCHED();
  if (N + 2 <= 9) k1_issue1<(N + 2) % 3, SEG>(c);
  SCHED();
}
template <bool SWAP, int N> DI void k1_step(f32x4 (&acc)[2][5], Frags& Fn, const Frags& Fp, lptr sm, K1Ctx& c) {
  WAIT_L0();
  BAR();
  SCHED();
  k1_seg<SWAP, N, 0>(acc, Fn, Fp, sm, c);
  k1_seg<SWAP, N, 1>(acc, Fn, Fp, sm, c);
  k1_seg<SWAP, N, 2>(acc, Fn, Fp, sm, c);
  k1_seg<SWAP, N, 3>(acc, Fn, Fp, sm, c);
  k1_seg<SWAP, N, 4>(acc, Fn, Fp, sm, c);
  c.st = (c.st == 9) ? 0 : c.st + 1;
}

struct Args { const u16 *x, *wqkv, *wo, *wr, *wi, *gw; char* ws; u16* out; };

DI void phase1(const Args& A, lptr sm, f32x4 (&pq)[2][5] TS_ARG) {
  const u16* x = A.x; const u16* wqkv = A.wqkv; const u16* wr = A.wr; const u16* wi = A.wi; char* ws = A.ws;
  const int tid = threadIdx.x, lane = tid & 63;
  const int wave = __builtin_amdgcn_readfirstlane(tid >> 6);
  const int b = blockIdx.x;
  const int xcd = b & 7, idx = b >> 3;
  const int rt = (xcd & 1) * 8 + (idx & 7);
  const int ct = (xcd >> 1) * 4 + (idx >> 3);
  const bool dft = ct >= 12, iskv = ct >= 4 && ct < 12;
  const int c0 = iskv ? 80 * (ct - 4) : 160 * (ct & 3);
  const int L0 = c0 >> 6, p0 = (c0 & 63) >> 4;
  const int xs = (p0 == 0 || p0 == 3) ? 1 : 2;
  const int bigB = p0 >= 2 ? 1 : 0;

  K1Ctx c;
  c.wave = wave;
  c.ra = mk_rsrc(x, 2048 * 1280);
  c.rb = dft ? mk_rsrc(&FT, 819200) : mk_rsrc(wqkv, 640 * 3840);
  c.rs = dft ? 1280 : 3840;
  auto grow = [&](int row) { return dft ? 32 * (16 * (rt >> 2) + 4 * (row >> 5) + 2 * ((row >> 3) & 1) + ((row >> 4) & 1)) + 8 * (rt & 3) + (row & 7) : 128 * rt + row; };
#pragma unroll
  for (int i = 0; i < 2; ++i) {
    const int row = 16 * wave + 8 * i + (lane >> 3);
    c.va[i] = grow(row) * 1280 + ((lane & 7) ^ ((row >> 1) & 7)) * 16;
  }
  {
    const int row = lane >> 3, hf = lane & 1;
    const int pair = ((lane & 7) >> 1) ^ (((row >> 1) & 1) | ((wave & 1) << 1));
    const unsigned rowb = (8 * wave + row) * c.rs;
    const bool kown = bigB ? (pair >= p0) : (pair <= p0);
    c.vb[0] = rowb + (iskv ? 1280 + (L0 + bigB) * 128 : L0 * 128) + pair * 32 + hf * 16;
    c.vb[1] = rowb + (iskv ? 2560 + (L0 + bigB) * 128 + (hf ^ 1) * 16 : (L0 + 1) * 128 + hf * 16) + pair * 32;
    c.vb[2] = rowb + (iskv ? (L0 + 1 - bigB) * 128 + (kown ? 1280 + pair * 32 + hf * 16 : 2560 + (pair ^ xs) * 32 + (hf ^ 1) * 16)
                           : (L0 + 2) * 128 + pair * 32 + hf * 16);
  }
  c.ldsa = (unsigned)(size_t)sm + 2 * wave * 1024;
  c.ldsb = (unsigned)(size_t)sm + 16384 + 3 * wave * 1024;
  c.st = (((((idx & 7) * 5 + (idx >> 3) * 10) >> 2) % 10) * K1_ROT / 10) * (10 / K1_ROT);
  k1_issue<0>(c);
  BAR();
  k1_issue<1>(c);

  const int wm = wave >> 1, wn = wave & 1;
  const int r = lane & 15, g = lane >> 4, q = (lane & 15) >> 2, p = lane & 3;
  const int hsw = ((q >> 1) & 1) | ((g & 1) << 1);
  const unsigned bbase = 16384 + g * 3072 + q * 128;
  c.ao[0][0] = (32 * wm + r) * 128 + ((g ^ (r >> 1)) * 16);
  c.ao[0][1] = (32 * wm + r) * 128 + (((4 + g) ^ (r >> 1)) * 16);
  f32x4 acc[2][5];
  Frags F0, F1;
  const rsrc_t rpf = (wave & 1) ? mk_rsrc(&GT, 491520) : mk_rsrc(A.wo, 819200);
  const unsigned vpf = (wave < 2 && lane < ((wave & 1) ? 15 : 25)) ? (b * ((wave & 1) ? 15 : 25) + lane) * 128 : 0x7fffffffu;

#define K1_HEAD(SWAP)                                                                          \
  _Pragma("unroll") for (int u = 1; u < 3; ++u) {                                              \
    _Pragma("unroll") for (int k = 0; k < 2; ++k) { c.ao[u][k] = c.ao[0][k] + u * K1_STAGE; asm volatile("" : "+v"(c.ao[u][k])); }     \
    _Pragma("unroll") for (int k = 0; k < 5; ++k) { c.bo[u][k] = c.bo[0][k] + u * K1_STAGE; asm volatile("" : "+v"(c.bo[u][k])); }     \
  }                                                                                            \
  _Pragma("unroll") for (int mt = 0; mt < 2; ++mt)                                             \
    _Pragma("unroll") for (int nt = 0; nt < 5; ++nt) asm volatile("" : "+v"(acc[mt][nt]));     \
  TS(1);                                                                                       \
  WAIT_V(5); k1_step<SWAP, 0>(acc, F0, F1, sm, c);                                             \
  TS(2);                                                                                       \
  WAIT_V(5); k1_step<SWAP, 1>(acc, F1, F0, sm, c);                                             \
  WAIT_V(5); k1_step<SWAP, 2>(acc, F0, F1, sm, c);                                             \
  pf_asm(rpf, (unsigned)(size_t)sm + K1_JUNK + wave * 256, vpf, 0);

  if (iskv) {
#pragma unroll
    for (int nt = 0; nt < 5; ++nt) {
      const int u = 4 * nt + p;
      const int isv = u >= 10 ? 1 : 0;
      const int pc = 4 * p0 + 10 * wn + u - 10 * isv;
      const int gp = pc >> 2, big = ((gp >> 2) == bigB);
      const int slot = (gp & 3) ^ ((!big && isv) ? xs : 0);
      c.bo[0][nt] = bbase + (big ? isv : 2) * 1024 + ((slot ^ hsw) * 32) + (((pc & 3) ^ (isv << 1)) * 8);
      const float i0 = ((nt < 2) || (nt == 2 && r < 8)) ? kEps : 0.f;
#pragma unroll
      for (int mt = 0; mt < 2; ++mt) acc[mt][nt] = (f32x4){i0, i0, i0, i0};
    }
    unsigned ko[10];
    {
      constexpr int TI[10] = {0, 0, 1, 1, 1, 2, 2, 0, 1, 2}, TJ[10] = {2, 3, 2, 3, 4, 3, 4, -1, -1, -1};
#pragma unroll
      for (int i = 0; i < 10; ++i) {
        const int ti = TI[i], tj = TJ[i];
        const int ur = 16 * ti + r, hk = ur >= 20 ? 1 : 0;
        const int uc = 16 * tj + 4 * g - 40, hv = uc >= 20 ? 1 : 0;
        const bool valid = tj < 0 ? (ur < 40 && g == 0) : (ur < 40 && uc >= 0 && hk == hv);
        const int off = wave * 3840 + hk * 1920 + (ur - 20 * hk) * 96 + (tj < 0 ? 80 : (uc - 20 * hv) * 4);
        ko[i] = K1_STAGE + (valid ? off : 8 * 3840 + lane * 16);
        asm volatile("" : "+v"(ko[i]));
      }
    }
    K1_HEAD(false)
    WAIT_V(6); k1_step<false, 3>(acc, F1, F0, sm, c);
    WAIT_V(6); k1_step<false, 4>(acc, F0, F1, sm, c);
    WAIT_V(5); k1_step<false, 5>(acc, F1, F0, sm, c);
    WAIT_V(5); k1_step<false, 6>(acc, F0, F1, sm, c);
    WAIT_V(5); k1_step<false, 7>(acc, F1, F0, sm, c);
    WAIT_V(5); k1_step<false, 8>(acc, F0, F1, sm, c);
    WAIT_V(0); k1_step<false, 9>(acc, F1, F0, sm, c);
    k1_mma<false, 0, 20>(acc, F1);
    TS(3);
    bf16x8 fr[5];
#pragma unroll
    for (int nt = 0; nt < 5; ++nt) {
#pragma unroll
      for (int m = 0; m < 2; ++m)
#pragma unroll
        for (int j = 0; j < 4; ++j) {
          if (nt < 2) acc[m][nt][j] = phi_f(acc[m][nt][j]);
          if (nt == 2) { const float ph = phi_f(acc[m][nt][j]); acc[m][nt][j] = (r < 8) ? ph : acc[m][nt][j]; }
        }
      fr[nt] = pack8(acc[0][nt], acc[1][nt]);
    }
    const bf16x8 ones = {0x3f80, 0x3f80, 0x3f80, 0x3f80, 0x3f80, 0x3f80, 0x3f80, 0x3f80};
    {
      constexpr int TI[10] = {0, 0, 1, 1, 1, 2, 2, 0, 1, 2}, TJ[10] = {2, 3, 2, 3, 4, 3, 4, -1, -1, -1};
      const f32x4 zz = {0.f, 0.f, 0.f, 0.f};
      f32x4 d[10];
#pragma unroll
      for (int i = 0; i < 10; ++i) d[i] = MFMA16(TJ[i] < 0 ? ones : fr[TJ[i] < 0 ? 0 : TJ[i]], fr[TI[i]], zz);
#pragma unroll
      for (int i = 0; i < 10; ++i) *(LDS_AS f32x4*)(sm + ko[i]) = d[i];
    }
    WAIT_L0();
    BAR();
    const rsrc_t rk = mk_rsrc(ws + WS_KVP + ((size_t)rt * 32 + 4 * (ct - 4)) * 1920, 7680);
    LDS_AS f32x4* src = (LDS_AS f32x4*)(sm + K1_STAGE);
    const f32x4 s0 = (src[tid] + src[tid + 480]) + (src[tid + 960] + src[tid + 1440]);
    ST16(__builtin_bit_cast(u32x4, s0), rk, tid * 16, 0);
    if (wave >= 6) {
      LDS_AS float* zs = (LDS_AS float*)(sm + K1_STAGE) + min(tid - 384, 79) * 24 + 20;
      const float z = (zs[0] + zs[1920]) + (zs[3840] + zs[5760]);
      __builtin_amdgcn_raw_buffer_store_b32(__float_as_uint(z), mk_rsrc(ws + WS_ZP + rt * 2560 + (ct - 4) * 320, 320), (tid - 384) * 4, 0, ST_AUX);
    }
  } else {
#pragma unroll
    for (int nt = 0; nt < 5; ++nt) {
      const int gp = p0 + 5 * wn + nt;
      c.bo[0][nt] = bbase + (gp >> 2) * 1024 + (((gp & 3) ^ hsw) * 32) + p * 8;
    }
    const float i0 = (ct < 4) ? kEps : 0.f;
#pragma unroll
    for (int mt = 0; mt < 2; ++mt)
#pragma unroll
      for (int nt = 0; nt < 5; ++nt) acc[mt][nt] = (f32x4){i0, i0, i0, i0};
    K1_HEAD(true)
    const int un = (dft ? 64 : 0) + rt * 4 + (ct & 3), mo = un >> 1, fh = un & 1;
    {
      const rsrc_t rf = mk_rsrc((wave >> 2) ? wi : wr, 2048 * 642);
      const int kb = 16 * (mo >> 4) + 8 * (mo & 1) + 64 * (16 * ((mo >> 3) & 1) + 4 * ((mo >> 1) & 3) + (wave & 3));
      const unsigned v0 = (lane & 31) < 21 ? (lane >> 5) * 640 + (lane & 31) * 16 : 0x7fffffffu;
#pragma unroll
      for (int i = 0; i < 4; ++i)
        dma16_asm(rf, (unsigned)(size_t)sm + K1_WF + (wave * 4 + i) * 1024, v0, kb * 642 + 320 * fh + i * 1280);
    }
    WAIT_V(10); k1_step<true, 3>(acc, F1, F0, sm, c);
    WAIT_V(10); k1_step<true, 4>(acc, F0, F1, sm, c);
    WAIT_V(5); k1_step<true, 5>(acc, F1, F0, sm, c);
    WAIT_V(5); k1_step<true, 6>(acc, F0, F1, sm, c);
    {
      const rsrc_t rw = mk_rsrc(ws + WS_WT + (size_t)fh * 160 * 8192 + mo * 128, (160 + fh) * 8192 - mo * 128);
      const int i8 = lane & 7;
#pragma unroll
      for (int rd = 0; rd < 3; ++rd) {
        const int fl = (rd * 8 + wave) * 8 + (lane >> 3);
        lptr src = sm + K1_WF + i8 * 514 + min(fl, 160) * 2;
        u32x4 o;
#pragma unroll
        for (int j = 0; j < 4; ++j) o[j] = (unsigned)*(LDS_AS u16*)(src + j * 4096) | ((unsigned)*(LDS_AS u16*)(src + 16384 + j * 4096) << 16);
        ST16(o, rw, fl * 8192 + i8 * 16, 0);
      }
      SCHED();
    }
    WAIT_V(8); k1_step<true, 7>(acc, F1, F0, sm, c);
    WAIT_V(8); k1_step<true, 8>(acc, F0, F1, sm, c);
    WAIT_V(0); k1_step<true, 9>(acc, F1, F0, sm, c);
    k1_mma<true, 0, 20>(acc, F1);
    TS(3);
    if (ct < 4) {
#pragma unroll
      for (int mt = 0; mt < 2; ++mt)
#pragma unroll
        for (int nt = 0; nt < 5; ++nt)
#pragma unroll
          for (int j = 0; j < 4; ++j) pq[mt][nt][j] = phi_f(acc[mt][nt][j]);
    } else {
      const rsrc_t rx = mk_rsrc(ws + WS_X1 + (size_t)(80 * (ct - 12) + 40 * wn) * 8192 + ((rt >> 1) & 1) * 4096 + (rt >> 2) * 1024 + wm * 256 + (rt & 1) * 128, 40 * 8192);
      const unsigned vo = (2 * g + (r >> 3)) * 8192 + (r & 7) * 16;
      const bool up = r >= 8;
#pragma unroll
      for (int nt = 0; nt < 5; ++nt) {
        const unsigned a0 = pk2(acc[0][nt][0], acc[0][nt][1]), a1 = pk2(acc[1][nt][0], acc[1][nt][1]);
        const unsigned b0 = pk2(acc[0][nt][2], acc[0][nt][3]), b1 = pk2(acc[1][nt][2], acc[1][nt][3]);
        const unsigned t0 = __builtin_amdgcn_update_dpp(0u, up ? a0 : b0, 0x128, 0xf, 0xf, false);
        const unsigned t1 = __builtin_amdgcn_update_dpp(0u, up ? a1 : b1, 0x128, 0xf, 0xf, false);
        const u32x4 o = {up ? t0 : a0, up ? t1 : a1, up ? b0 : t0, up ? b1 : t1};
        ST16(o, rx, vo, nt * 8 * 8192);
      }
    }
  }
  TS(4);
}

DI u32x2 make_tag(unsigned e) { return (u32x2){e + 1u, (e + 1u) * 0x9E3779B9u ^ 0x5bd1e995u}; }
DI int flag_slot(int b) { return ((b & 7) >> 1) * 64 + (b & 1) * 32 + (b >> 3); }
template <bool F> DI void publish(char* ws, int which, unsigned e) {
  if (!F) return;
  WAIT_V(0);
  BAR();
  if (threadIdx.x == 0) __builtin_amdgcn_raw_buffer_store_b64(make_tag(e), mk_rsrc(ws + WS_SYN + which * 2048, 2048), flag_slot(blockIdx.x) * 8, 0, 16);
}
template <bool F> DI void await(char* ws, int which, unsigned e, int need, int wave, int lane) {
  if (!F) return;
  if (wave == 0) {
    const rsrc_t rf = mk_rsrc(ws + WS_SYN + which * 2048, 2048);
    const u32x2 t = make_tag(e);
    const bool n0 = (need >> (lane < 32 ? 0 : 1)) & 1, n1 = (need >> (lane < 32 ? 1 : 2)) & 1;
    for (unsigned spin = 0; spin < (1u << 17); ++spin) {
      const u32x4 f0 = __builtin_amdgcn_raw_buffer_load_b128(rf, lane * 16, 0, 16);
      const u32x4 f1 = __builtin_amdgcn_raw_buffer_load_b128(rf, lane * 16, 1024, 16);
      const bool ok0 = f0[0] == t[0] && f0[1] == t[1] && f0[2] == t[0] && f0[3] == t[1];
      const bool ok1 = f1[0] == t[0] && f1[1] == t[1] && f1[2] == t[0] && f1[3] == t[1];
      if (__all((ok0 || !n0) && (ok1 || !n1))) break;
      __builtin_amdgcn_s_sleep(2);
    }
  }
  asm volatile("" ::: "memory");
  BAR();
  asm volatile("" ::: "memory");
}
DI void dma16_sc1(rsrc_t rs, unsigned lds, unsigned vo, int so) {
  asm volatile("s_mov_b32 m0, %2\n\ts_nop 0\n\tbuffer_load_dwordx4 %0, %1, %3 offen sc1 lds" :: "v"(vo), "s"(rs), "s"(lds), "s"(so));
}
#define LD16(rs, vo, so) __builtin_amdgcn_raw_buffer_load_b128((rs), (vo), (so), LD_AUX)

constexpr int P3_HALF = 71680;

DI void p3_load_g(lptr sm, int wave, int lane) {
  const int b = blockIdx.x, ct = ((b & 7) >> 2) * 4 + (b >> 6);
  const rsrc_t rg = mk_rsrc((const char*)&GT + (size_t)(2 * ct) * 30720, 61440);
#pragma unroll
  for (int i = 0; i < 8; ++i) {
    const int n = min(wave * 8 + i, 59);
    dma16_asm(rg, (unsigned)(size_t)sm + (n >= 30 ? P3_HALF + 40960 - 30720 : 40960) + n * 1024, lane * 16, n * 1024);
  }
}

template <bool F> DI void phase2(const Args& A, lptr sm0, unsigned e, const f32x4 (&pq)[2][5] TS_ARG) {
  char* ws = A.ws;
  const int lane = threadIdx.x & 63, tid = threadIdx.x & 255;
  const int wave8 = __builtin_amdgcn_readfirstlane(threadIdx.x >> 6);
  const int wave = wave8 & 3, team = wave8 >> 2;
  lptr sm = sm0 + team * P3_HALF;
  const unsigned smu = (unsigned)(size_t)sm;
  const int b = blockIdx.x, xcd = b & 7, idx = b >> 3;
  const int rt1 = (xcd & 1) * 8 + (idx & 7), ct1 = (xcd >> 1) * 4 + (idx >> 3);
  const bool iskv = ct1 >= 4 && ct1 < 12;
  const int v = (ct1 - 4) * 16 + rt1;
  const int r = lane & 15, g = lane >> 4;
  const f32x4 z4 = {0.f, 0.f, 0.f, 0.f};
  if (F) p3_load_g(sm0, wave8, lane);
  if (ct1 >= 12 || (iskv && v >= 20 && v < 117)) {
    const int fu = (iskv ? 128 + 2 * (v - 20) : 2 * (rt1 * 4 + (ct1 & 3))) + team;
    const int f = min(fu, 320);
    const bool edge = (f == 0 || f == 320);
    u32x4 fre[4], fim[4], fc[2], fs[2], fn[2];
    float tc[2][4], tsn[2][4];
    unsigned uo[2][4];
    {
      const float a64 = (float)(16 * wave + r) * (1.f / 64.f);
#pragma unroll
      for (int ks = 0; ks < 4; ++ks)
#pragma unroll
        for (int j = 0; j < 4; ++j) {
          const float ang = a64 * (float)(16 * ks + 4 * g + j);
          const float c = __builtin_amdgcn_cosf(ang), s = __builtin_amdgcn_sinf(ang);
          fre[ks][j] = pk2(c, s);
          fim[ks][j] = pk2(-s, c);
        }
#pragma unroll
      for (int t = 0; t < 2; ++t) {
        const float a32 = (float)(16 * t + r) * (1.f / 32.f);
#pragma unroll
        for (int j = 0; j < 4; ++j) {
          const int b0 = 16 * (j >> 1) + 4 * g + 2 * (j & 1);
          const float a0 = a32 * (float)b0, a1 = a0 + a32;
          const float s0 = __builtin_amdgcn_sinf(a0), s1 = __builtin_amdgcn_sinf(a1);
          fc[t][j] = pk2(__builtin_amdgcn_cosf(a0), __builtin_amdgcn_cosf(a1));
          fs[t][j] = pk2(s0, s1);
          fn[t][j] = fs[t][j] ^ 0x80008000u;
        }
#pragma unroll
        for (int j = 0; j < 4; ++j) {
          const float a = a64 * (1.f / 32.f) * (float)(16 * t + 4 * g + j);
          tc[t][j] = __builtin_amdgcn_cosf(a);
          tsn[t][j] = __builtin_amdgcn_sinf(a);
          const int s2 = 16 * t + 4 * g + j;
          uo[t][j] = 8192 + s2 * 256 + (((4 * wave + (r >> 2)) ^ (s2 & 15)) * 16) + (r & 3) * 4;
        }
      }
    }
#pragma unroll
    for (int ks = 0; ks < 4; ++ks) { asm volatile("" : "+v"(fre[ks]), "+v"(fim[ks])); }
#pragma unroll
    for (int t = 0; t < 2; ++t) {
      asm volatile("" : "+v"(fc[t]), "+v"(fs[t]), "+v"(fn[t]));
#pragma unroll
      for (int j = 0; j < 4; ++j) asm volatile("" : "+v"(tc[t][j]), "+v"(tsn[t][j]), "+v"(uo[t][j]));
    }
    SCHED();
    TS(6);
    await<F>(ws, 0, e, 5, wave8, lane);
    TS(7);
    {
      const rsrc_t rx = mk_rsrc(ws + WS_X1 + (size_t)(f == 320 ? 0 : f) * 8192, 8192);
      dma16_sc1(rx, smu + wave * 2048, lane * 16, wave * 2048);
      dma16_sc1(rx, smu + wave * 2048 + 1024, lane * 16, wave * 2048 + 1024);
    }
    const rsrc_t rw = mk_rsrc(ws + WS_WT + (size_t)f * 8192, 8192);
    u32x4 wv[2];
    wv[0] = LD16(rw, lane * 16, wave * 2048);
    wv[1] = LD16(rw, lane * 16 + 1024, wave * 2048);
    SCHED();
    WAIT_V(2);
    BAR();
    SCHED();
    TS(8);
    f32x4 re[2], im[2];
    const unsigned emask = edge ? 0xffffu : 0xffffffffu, esh = (f == 320) ? 16u : 0u;
#pragma unroll
    for (int t = 0; t < 2; ++t) {
      re[t] = z4; im[t] = z4;
#pragma unroll
      for (int ks = 0; ks < 4; ++ks) {
        u32x4 xa = *(LDS_AS u32x4*)(sm + (t * 4 + ks) * 1024 + lane * 16);
        if (edge) {
#pragma unroll
          for (int j = 0; j < 4; ++j) xa[j] = (xa[j] >> esh) & emask;
        }
        re[t] = MFMA16(BC(xa), BC(fre[ks]), re[t]);
        im[t] = MFMA16(BC(xa), BC(fim[ks]), im[t]);
      }
    }
#pragma unroll
    for (int t = 0; t < 2; ++t)
#pragma unroll
      for (int j = 0; j < 4; ++j) {
        const float a = re[t][j], c = im[t][j];
        re[t][j] = a * tc[t][j] + c * tsn[t][j];
        im[t][j] = c * tc[t][j] - a * tsn[t][j];
      }
    bf16x8 bre = pack8(re[0], re[1]), bim = pack8(im[0], im[1]);
#pragma unroll
    for (int t = 0; t < 2; ++t) {
      re[t] = MFMA16(BC(fc[t]), bre, z4); im[t] = MFMA16(BC(fc[t]), bim, z4);
      re[t] = MFMA16(BC(fs[t]), bim, re[t]); im[t] = MFMA16(BC(fn[t]), bre, im[t]);
    }
    const float sc = (edge ? 1.f : 2.f) / (2048.f * 640.f);
#pragma unroll
    for (int t = 0; t < 2; ++t)
#pragma unroll
      for (int j = 0; j < 4; ++j) {
        const float a = re[t][j], c = im[t][j], wre = bf_lo(wv[t][j]) * sc, wim = bf_hi(wv[t][j]) * sc;
        re[t][j] = a * wre - c * wim;
        im[t][j] = a * wim + c * wre;
      }
    bre = pack8(re[0], re[1]); bim = pack8(im[0], im[1]);
#pragma unroll
    for (int t = 0; t < 2; ++t) {
      re[t] = MFMA16(BC(fc[t]), bre, z4); im[t] = MFMA16(BC(fc[t]), bim, z4);
      re[t] = MFMA16(BC(fn[t]), bim, re[t]); im[t] = MFMA16(BC(fs[t]), bre, im[t]);
    }
#pragma unroll
    for (int t = 0; t < 2; ++t)
#pragma unroll
      for (int j = 0; j < 4; ++j) {
        const float a = re[t][j], c = im[t][j];
        const float ur = a * tc[t][j] - c * tsn[t][j];
        const float ui = -(c * tc[t][j]) - a * tsn[t][j];
        *(LDS_AS unsigned*)(sm + uo[t][j]) = pk2(ur, ui);
      }
    WAIT_L0();
    BAR();
    u32x2 pr[2], pi[2];
#pragma unroll
    for (int t = 0; t < 2; ++t) {
      f32x4 yr = z4, yi = z4;
#pragma unroll
      for (int ks = 0; ks < 4; ++ks) {
        const bf16x8 u = *(LDS_AS bf16x8*)(sm + 8192 + (16 * t + r) * 256 + (((4 * ks + g) ^ r) * 16));
        yr = MFMA16(u, BC(fre[ks]), yr);
        yi = MFMA16(u, BC(fim[ks]), yi);
      }
      pr[t] = (u32x2){pk2(yr[0], yr[1]), pk2(yr[2], yr[3])};
      pi[t] = (u32x2){pk2(-yi[0], -yi[1]), pk2(-yi[2], -yi[3])};
    }
    const unsigned vo = wave * 1024 + r * 64 + (g & 1) * 32 + (g >> 1) * 16;
    const rsrc_t ry = mk_rsrc(ws + WS_Y + (size_t)f * 4096, fu > 320 ? 0 : (edge ? 4096 : 321 * 4096));
    ST16(widen(pr[0], pr[1]), ry, vo, 0);
    ST16(widen(pi[0], pi[1]), ry, vo, 320 * 4096);
  } else if (iskv && v < 20) {
    const int ks = v;
    const int hf = (32 * ks) / 20, e0 = 20 * hf;
    const int q = r >> 2, p = lane & 3;
    const int hsw = ((q >> 1) & 1) | ((g & 1) << 1);
    lptr ab = sm0 + 32768;
    if (team == 0) {
      *(LDS_AS u32x4*)(ab + tid * 16) = (u32x4){0u, 0u, 0u, 0u};
      if (tid < 32) *(LDS_AS u32x4*)(ab + 4096 + tid * 16) = (u32x4){0u, 0u, 0u, 0u};
    }
    bf16x8 bw[2][3][2];
#pragma unroll
    for (int rr = 0; rr < 2; ++rr) {
      const int cp = 2 * team + rr;
      const int L1 = (160 * cp) >> 7, L2 = (1120 - 160 * cp) >> 7;
      const rsrc_t rwo = mk_rsrc(A.wo, 640 * 1280);
      const int line = (wave < 2 ? L1 : L2) + (wave & 1);
      const int row = lane >> 3;
#pragma unroll
      for (int kb = 0; kb < 8; ++kb) {
        const int pair = ((lane & 7) >> 1) ^ (((row >> 1) & 1) | ((kb & 1) << 1));
        dma16_asm(rwo, smu + (wave * 8 + kb) * 1024, min(e0 + 8 * kb + row, 639) * 1280 + line * 128 + pair * 32 + (lane & 1) * 16, 0);
      }
      WAIT_V(0);
      BAR();
      SCHED();
#pragma unroll
      for (int n = 0; n < 3; ++n) {
        const int tl = min(wave + 4 * n, 9);
        const int ctl = tl >= 5 ? 1 : 0, nt = tl - 5 * ctl, ctg = 2 * cp + ctl;
        const int i = 16 * nt + 4 * p;
        const int byte = 2 * (i < 40 ? 40 * ctg + i : 560 - 40 * ctg + i);
        const int pl = i < 40 ? (byte >> 7) - L1 : 2 + (byte >> 7) - L2;
        lptr bp = sm + (pl * 8 + g) * 1024 + q * 128 + ((((byte >> 5) & 3) ^ hsw) * 32) + (byte & 31);
#pragma unroll
        for (int s = 0; s < 2; ++s) {
          const s16x4 lo = TRREAD(bp + s * 4096), hi = TRREAD(bp + s * 4096 + 512);
          bw[rr][n][s] = __builtin_shufflevector(lo, hi, 0, 1, 2, 3, 4, 5, 6, 7);
          asm volatile("" : "+v"(bw[rr][n][s]));
        }
      }
      WAIT_L0();
      if (rr == 0) BAR();
      SCHED();
    }
    const int t5 = min((int)threadIdx.x >> 1, 159), kl = t5 / 5, qd = t5 - kl * 5;
    const int k = 32 * ks + kl, h = k / 20, d = k - 20 * h;
    const rsrc_t rk = mk_rsrc(ws + WS_KVP, threadIdx.x < 320 ? 983040 : 0);
    unsigned ko = h * 1920 + d * 96 + qd * 16 + (threadIdx.x & 1) * 8 * 61440, ao = kl * 144 + (20 * (h - hf) + 4 * qd) * 2;
    asm volatile("" : "+v"(ko), "+v"(ao));
    TS(6);
    await<F>(ws, 0, e, 2, wave8, lane);
    TS(7);
    if (wave8 < 5) {
      const rsrc_t rk2 = mk_rsrc(ws + WS_KVP, 983040);
      f32x4 pv[8];
#pragma unroll
      for (int i = 0; i < 8; ++i) pv[i] = __builtin_bit_cast(f32x4, LD16(rk2, ko, i * 61440));
      f32x4 sum = ((pv[0] + pv[1]) + (pv[2] + pv[3])) + ((pv[4] + pv[5]) + (pv[6] + pv[7]));
#pragma unroll
      for (int j = 0; j < 4; ++j) sum[j] += __uint_as_float(__builtin_amdgcn_update_dpp(0u, __float_as_uint(sum[j]), 0xB1, 0xf, 0xf, false));
      if (!(threadIdx.x & 1)) *(LDS_AS u32x2*)(ab + ao) = (u32x2){pk2(sum[0], sum[1]), pk2(sum[2], sum[3])};
    }
    (void)rk;
    WAIT_L0();
    BAR();
    TS(8);
    bf16x8 a[2][2];
#pragma unroll
    for (int t = 0; t < 2; ++t)
#pragma unroll
      for (int s = 0; s < 2; ++s) a[t][s] = *(LDS_AS bf16x8*)(ab + (8 * (r >> 2) + (r & 3) + 4 * t) * 144 + s * 64 + g * 16);
#pragma unroll
    for (int rr = 0; rr < 2; ++rr) {
      const rsrc_t rm = mk_rsrc(ws + WS_M + (size_t)(10 * (2 * team + rr)) * 20480 + ks * 1024, 9 * 20480 + 1024);
#pragma unroll
      for (int n = 0; n < 3; ++n) {
        f32x4 d1 = MFMA16(a[0][0], bw[rr][n][0], z4), d2 = MFMA16(a[1][0], bw[rr][n][0], z4);
        d1 = MFMA16(a[0][1], bw[rr][n][1], d1); d2 = MFMA16(a[1][1], bw[rr][n][1], d2);
        ST16(__builtin_bit_cast(u32x4, pack8(d1, d2)), rm, lane * 16, (wave + 4 * n) * 20480);
      }
    }
  } else if (ct1 < 4) {
    const int t8 = threadIdx.x, wm = wave8 >> 1, wn = wave8 & 1;
    {
      const int gb = rt1 * 4 + ct1;
      const rsrc_t rx = mk_rsrc(A.x + (size_t)gb * 32 * 640, 32 * 1280), rg = mk_rsrc(A.gw, 1280);
      u32x4 gx[5], gg[5];
#pragma unroll
      for (int i = 0; i < 5; ++i) {
        gx[i] = __builtin_amdgcn_raw_buffer_load_b128(rx, t8 * 80 + i * 16, 0, 0);
        gg[i] = __builtin_amdgcn_raw_buffer_load_b128(rg, (t8 & 15) * 80 + i * 16, 0, 0);
      }
      float s0 = 0.f, s1 = 0.f;
#pragma unroll
      for (int i = 0; i < 5; ++i) { s0 = dot2(gx[i][0], gg[i][0], s0); s1 = dot2(gx[i][1], gg[i][1], s1); s0 = dot2(gx[i][2], gg[i][2], s0); s1 = dot2(gx[i][3], gg[i][3], s1); }
      float s = s0 + s1;
#pragma unroll
      for (int o = 8; o > 0; o >>= 1) s += __shfl_xor(s, o);
      if ((t8 & 15) == 0) __builtin_amdgcn_raw_buffer_store_b32(__float_as_uint(1.f / (1.f + __expf(-s))), mk_rsrc(ws + WS_G, 8192), (gb * 32 + (t8 >> 4)) * 4, 0, 16);
    }
    u32x2 pk[2][5];
    unsigned zm[5];
#pragma unroll
    for (int nt = 0; nt < 5; ++nt) {
#pragma unroll
      for (int mt = 0; mt < 2; ++mt) {
        pk[mt][nt] = (u32x2){pk2(pq[mt][nt][0], pq[mt][nt][1]), pk2(pq[mt][nt][2], pq[mt][nt][3])};
        asm volatile("" : "+v"(pk[mt][nt]));
      }
      zm[nt] = ((4 * nt + g) / 5 == (r & 3)) ? 0xffffffffu : 0u;
      asm volatile("" : "+v"(zm[nt]));
    }
    const rsrc_t rz = mk_rsrc(ws + WS_ZP + ct1 * 640, 40960);
    const rsrc_t rp = mk_rsrc(ws + WS_P + (size_t)(8 * rt1 + 2 * wm) * 20480 + (20 * ct1 + 10 * wn) * 256, 2 * 20480);
    const unsigned vo = (g & 1) * 20480 + (g >> 1) * 256 + r * 16;
    SCHED();
    TS(6);
    await<F>(ws, 0, e, 2, wave8, lane);
    TS(7);
    if (wave8 < 3) {
      float zv[16];
#pragma unroll
      for (int i = 0; i < 16; ++i) zv[i] = __uint_as_float(__builtin_amdgcn_raw_buffer_load_b32(rz, min(t8, 159) * 4, i * 2560, LD_AUX));
#pragma unroll
      for (int i = 0; i < 8; ++i) zv[i] += zv[i + 8];
      const float zs = ((zv[0] + zv[1]) + (zv[2] + zv[3])) + ((zv[4] + zv[5]) + (zv[6] + zv[7]));
      *(LDS_AS u16*)(sm0 + t8 * 2) = (u16)pk2(zs, zs);
    }
    WAIT_L0();
    BAR();
    TS(8);
    u32x2 zq[6];
#pragma unroll
    for (int nt = 0; nt < 5; ++nt) { zq[nt] = *(LDS_AS u32x2*)(sm0 + wn * 160 + nt * 32 + g * 8); zq[nt][0] &= zm[nt]; zq[nt][1] &= zm[nt]; }
    zq[5] = (u32x2){0u, 0u};
    u32x2 po[2][5];
#pragma unroll
    for (int mt = 0; mt < 2; ++mt) {
      f32x4 den = {kEps, kEps, kEps, kEps};
#pragma unroll
      for (int m = 0; m < 3; ++m) {
        const u32x4 za = {zq[2 * m][0], zq[2 * m][1], zq[2 * m + 1][0], zq[2 * m + 1][1]};
        const u32x2 p1 = m < 2 ? pk[mt][2 * m + 1] : (u32x2){0u, 0u};
        const u32x4 pb = {pk[mt][2 * m][0], pk[mt][2 * m][1], p1[0], p1[1]};
        den = MFMA16(BC(za), BC(pb), den);
      }
      f32x4 inv;
#pragma unroll
      for (int j = 0; j < 4; ++j) inv[j] = __builtin_amdgcn_rcpf(den[j]);
#pragma unroll
      for (int nt = 0; nt < 5; ++nt) {
        const float iv = nt == 0 ? inv[0] : nt == 4 ? inv[3] : (g < nt ? inv[nt - 1] : inv[nt]);
        const f32x4 w = pq[mt][nt] * iv;
        po[mt][nt] = (u32x2){pk2(w[0], w[1]), pk2(w[2], w[3])};
      }
    }
#pragma unroll
    for (int nt = 0; nt < 5; ++nt) ST16(widen(po[0][nt], po[1][nt]), rp, vo + nt * 512, 0);
  }
  TS(9);
}

constexpr int LDS_ALL = K1_LDS;
static_assert(2 * P3_HALF + 256 <= LDS_ALL, "lds");

template <bool F> DI void phase3(const Args& A, lptr sm, unsigned e TS_ARG) {
  char* ws = A.ws;
  const int tid = threadIdx.x, lane = tid & 63;
  const int wave = __builtin_amdgcn_readfirstlane(tid >> 6);
  const int b = blockIdx.x, xcd = b & 7, idx = b >> 3;
  const int rt = (xcd & 3) * 8 + (idx & 7);
  const int ct = (xcd >> 2) * 4 + (idx >> 3);
  const int r = lane & 15, g = lane >> 4;
  LDS_AS float* red = (LDS_AS float*)sm;
  constexpr int RA = 64 * 84, RE = 64 * 52;
  if (!F) { p3_load_g(sm, wave, lane); if (wave >= 4) WAIT_V(0); }
  await<F>(ws, 1, e, 7, wave, lane);
  TS(11);
  if (wave < 2) {
    const rsrc_t ra = mk_rsrc(ws + WS_P + (size_t)(4 * rt) * 20480 + wave * 10240, 4 * 20480);
    const rsrc_t rb = mk_rsrc(ws + WS_M + (size_t)(5 * ct) * 20480 + wave * 10240, 5 * 20480);
    const unsigned vo = lane * 16;
    f32x4 acc[4][5];
#pragma unroll
    for (int mt = 0; mt < 4; ++mt)
#pragma unroll
      for (int nt = 0; nt < 5; ++nt) acc[mt][nt] = (f32x4){0.f, 0.f, 0.f, 0.f};
    u32x4 fa[10][4], fb[10][5];
    constexpr int PF = 3;
    auto ld = [&](int ks) {
#pragma unroll
      for (int nt = 0; nt < 5; ++nt) fb[ks][nt] = LD16(rb, vo + (ks & 3) * 1024, nt * 20480 + (ks >> 2) * 4096);
#pragma unroll
      for (int mt = 0; mt < 4; ++mt) fa[ks][mt] = LD16(ra, vo + (ks & 3) * 1024, mt * 20480 + (ks >> 2) * 4096);
    };
#pragma unroll
    for (int ks = 0; ks < PF; ++ks) { ld(ks); SCHED(); }
    if (!F) { WAIT_V(27); BAR(); }
#pragma unroll
    for (int ks = 0; ks < 10; ++ks) {
#pragma unroll
      for (int mt = 0; mt < 4; ++mt)
#pragma unroll
        for (int nt = 0; nt < 5; ++nt) acc[mt][nt] = MFMA16(BC(fb[ks][nt]), BC(fa[ks][mt]), acc[mt][nt]);
      SCHED();
      if (ks + PF < 10) ld(ks + PF);
      SCHED();
    }
    TS(12);
    BAR();
#pragma unroll
    for (int mt = 0; mt < 4; ++mt)
#pragma unroll
      for (int nt = 0; nt < 5; ++nt) *(LDS_AS f32x4*)(red + wave * RA + (16 * mt + r) * 84 + 16 * nt + 4 * g) = acc[mt][nt];
  } else if (wave < 4) {
    const int od = wave - 2;
    lptr yb = sm + od * P3_HALF;
    const unsigned ybu = (unsigned)(size_t)yb;
    const rsrc_t ry = mk_rsrc(ws + WS_Y + (size_t)od * 320 * 4096 + rt * 128, 320 * 4096);
    const int row = lane >> 3;
    const unsigned vy0 = row * 4096 + ((((lane & 7) >> 1) ^ ((row >> 1) & 1)) * 32) + (lane & 1) * 16;
    const unsigned vy1 = vy0 ^ 64;
    auto issue = [&](int ks) {
#pragma unroll
      for (int i = 0; i < 4; ++i) dma16_sc1(ry, ybu + (ks * 4 + i) * 1024, (i & 1) ? vy1 : vy0, (ks * 4 + i) * 32768);
    };
    issue(0); issue(1); issue(2);
    if (!F) { WAIT_V(12); BAR(); }
    const int q = r >> 2, p = lane & 3;
    const int hsw = ((q >> 1) & 1) | ((g & 1) << 1);
    unsigned yo[4];
#pragma unroll
    for (int mt = 0; mt < 4; ++mt) yo[mt] = g * 1024 + q * 128 + ((mt ^ hsw) * 32) + p * 8;
    lptr gb = yb + 40960 + lane * 16;
    f32x4 acc[4][3];
#pragma unroll
    for (int mt = 0; mt < 4; ++mt)
#pragma unroll
      for (int nt = 0; nt < 3; ++nt) acc[mt][nt] = (f32x4){0.f, 0.f, 0.f, 0.f};
#define K3_STEP(ks, n)                                                                                         \
    {                                                                                                          \
      WAIT_V(n);                                                                                               \
      SCHED();                                                                                                 \
      bf16x8 fy[4], fg[3];                                                                                     \
      _Pragma("unroll") for (int nt = 0; nt < 3; ++nt) fg[nt] = *(LDS_AS bf16x8*)(gb + (ks * 3 + nt) * 1024);  \
      _Pragma("unroll") for (int mt = 0; mt < 4; ++mt) {                                                       \
        const s16x4 lo = TRREAD(yb + yo[mt] + ks * 4096), hi = TRREAD(yb + yo[mt] + ks * 4096 + 512);          \
        fy[mt] = __builtin_shufflevector(lo, hi, 0, 1, 2, 3, 4, 5, 6, 7);                                      \
      }                                                                                                        \
      SCHED();                                                                                                 \
      if (ks + 3 < 10) issue(ks + 3);                                                                          \
      SCHED();                                                                                                 \
      _Pragma("unroll") for (int mt = 0; mt < 4; ++mt)                                                         \
        _Pragma("unroll") for (int nt = 0; nt < 3; ++nt) acc[mt][nt] = MFMA16(fg[nt], fy[mt], acc[mt][nt]);   \
      SCHED();                                                                                                 \
    }
    K3_STEP(0, 8) K3_STEP(1, 8) K3_STEP(2, 8) K3_STEP(3, 8) K3_STEP(4, 8)
    K3_STEP(5, 8) K3_STEP(6, 8) K3_STEP(7, 8) K3_STEP(8, 4) K3_STEP(9, 0)
    TS(12);
    BAR();
#pragma unroll
    for (int mt = 0; mt < 4; ++mt)
#pragma unroll
      for (int nt = 0; nt < 3; ++nt) *(LDS_AS f32x4*)(red + 2 * RA + od * RE + (16 * mt + r) * 52 + 16 * nt + 4 * g) = acc[mt][nt];
  } else {
    if (!F) BAR();
    if (wave == 4) red[2 * P3_HALF / 4 + lane] = __uint_as_float(__builtin_amdgcn_raw_buffer_load_b32(mk_rsrc(ws + WS_G + rt * 256, 256), lane * 4, 0, LD_AUX));
    WAIT_L0();
    BAR();
  }
  const rsrc_t ro = mk_rsrc(A.out + (size_t)(64 * rt) * 640, 64 * 1280);
  const int nit = (wave == 4 || wave == 5) ? 2 : 1;
  int row[2], gr[2];
  float gq[2];
#pragma unroll
  for (int i = 0; i < 2; ++i) {
    const int it = min(i ? 256 + tid : tid, 639);
    row[i] = it / 10; gr[i] = it - row[i] * 10;
    gq[i] = red[2 * P3_HALF / 4 + row[i]];
  }
  WAIT_L0();
  BAR();
  TS(13);
#pragma unroll
  for (int i = 0; i < 2; ++i) {
    if (i < nit) {
      LDS_AS float* pa = red + row[i] * 84 + 8 * gr[i];
      const f32x4 a0 = *(LDS_AS f32x4*)pa + *(LDS_AS f32x4*)(pa + RA);
      const f32x4 a1 = *(LDS_AS f32x4*)(pa + 4) + *(LDS_AS f32x4*)(pa + RA + 4);
      const bool lo = gr[i] < 5;
      LDS_AS float* pe = red + 2 * RA + row[i] * 52;
      const float yn = pe[RE + 41];
      LDS_AS float* pq = pe + (lo ? 8 * gr[i] : 72 - 8 * gr[i]);
      const f32x4 e0 = *(LDS_AS f32x4*)pq, e1 = *(LDS_AS f32x4*)(pq + 4), o0 = *(LDS_AS f32x4*)(pq + RE), o1 = *(LDS_AS f32x4*)(pq + RE + 4);
      const float e8 = pq[8], o8 = pq[RE + 8];
      const f32x4 m0 = e0 - o0, m1 = e1 - o1, p0 = e0 + o0, p1 = e1 + o1;
      const float p8 = e8 + o8;
      const f32x4 yy = {yn, -yn, yn, -yn};
      const f32x4 b0 = (lo ? m0 : (f32x4){p8, p1[3], p1[2], p1[1]}) + yy;
      const f32x4 b1 = (lo ? m1 : (f32x4){p1[0], p0[3], p0[2], p0[1]}) + yy;
      const f32x4 r0 = b0 + gq[i] * (a0 - b0), r1 = b1 + gq[i] * (a1 - b1);
      __builtin_amdgcn_raw_buffer_store_b128(__builtin_bit_cast(u32x4, pack8(r0, r1)), ro, row[i] * 1280 + (lo ? 80 * ct : 1120 - 80 * ct) + 16 * gr[i], 0, OUT_AUX);
    }
  }
  TS(14);
}

#define TS_END()

__global__ __launch_bounds__(512, 2) void fused_kernel(Args A) {
  extern __shared__ __attribute__((aligned(16))) char smem[];
  lptr sm = (lptr)smem;
  TS_DECL;
  TS(0);
  const rsrc_t re = mk_rsrc(A.ws + WS_SYN + 4096, 4);
  const unsigned ev = __builtin_amdgcn_raw_buffer_load_b32(re, 0, 0, 16);
  f32x4 pq[2][5];
  phase1(A, sm, pq TS_PASS);
  WAIT_V(0);
  const unsigned e = __builtin_amdgcn_readfirstlane(ev);
  publish<true>(A.ws, 0, e);
  TS(5);
  phase2<true>(A, sm, e, pq TS_PASS);
  publish<true>(A.ws, 1, e);
  TS(10);
  phase3<true>(A, sm, e TS_PASS);
  if (blockIdx.x == 0 && threadIdx.x == 0) __builtin_amdgcn_raw_buffer_store_b32(e + 1u, re, 0, 0, 16);
  TS_END();
}

extern "C" void kernel_launch(void* const* d_in, const int* in_sizes, int n_in, void* d_out, int out_size, void* d_ws,
                              size_t ws_size, hipStream_t stream) {
  Args A;
  A.x = (const u16*)d_in[0]; A.wqkv = (const u16*)d_in[1]; A.wo = (const u16*)d_in[2];
  A.wr = (const u16*)d_in[4]; A.wi = (const u16*)d_in[5]; A.gw = (const u16*)d_in[6];
  A.ws = (char*)d_ws; A.out = (u16*)d_out;
  (void)hipFuncSetAttribute((const void*)fused_kernel, hipFuncAttributeMaxDynamicSharedMemorySize, LDS_ALL);
  fused_kernel<<<256, 512, LDS_ALL, stream>>>(A);
}
```

```cpp
#include <hip/hip_runtime.h>
#include <hip/hip_bf16.h>
#include <utility>

typedef unsigned short u16;
typedef __attribute__((ext_vector_type(8))) short bf16x8;
typedef __attribute__((ext_vector_type(4))) short s16x4;
typedef __attribute__((ext_vector_type(4))) float f32x4;
typedef __attribute__((ext_vector_type(2))) float f32x2;
typedef __attribute__((ext_vector_type(2))) __bf16 bf16x2_t;
typedef __attribute__((ext_vector_type(4))) unsigned u32x4;
typedef __attribute__((ext_vector_type(2))) unsigned u32x2;
#define LDS_AS __attribute__((address_space(3)))
#define DI __device__ __forceinline__
#define MFMA16(a, b, c) __builtin_amdgcn_mfma_f32_16x16x32_bf16((a), (b), (c), 0, 0, 0)
typedef LDS_AS char* lptr;
typedef __amdgpu_buffer_rsrc_t rsrc_t;

#define ST_AUX 16
#define OUT_AUX 0
#define K1_ROT 5
#define FUSED 1
#define POLL_SLEEP 2
#define G_EARLY 0
#define LD_AUX 16

constexpr double kPi = 3.14159265358979323846;
constexpr double ccos_s(double x) { double x2 = x * x, t = 1, s = 1; for (int i = 1; i < 12; ++i) { t *= -x2 / ((2 * i - 1) * (2 * i)); s += t; } return s; }
constexpr double csin_s(double x) { double x2 = x * x, t = x, s = x; for (int i = 1; i < 12; ++i) { t *= -x2 / ((2 * i) * (2 * i + 1)); s += t; } return s; }
constexpr double cos640(int m) {
  m %= 640; if (m > 320) m = 640 - m;
  bool neg = false; if (m > 160) { m = 320 - m; neg = true; }
  double v = (m <= 80) ? ccos_s(2 * kPi * m / 640) : csin_s(2 * kPi * (160 - m) / 640);
  return neg ? -v : v;
}
constexpr u16 to_bf16c(double v) { float f = (float)v; unsigned u = __builtin_bit_cast(unsigned, f); u += 0x7fffu + ((u >> 16) & 1u); return (u16)(u >> 16); }
struct C640 { u16 v[640]; };
constexpr C640 make_c640() { C640 c{}; for (int i = 0; i < 640; ++i) c.v[i] = to_bf16c(cos640(i)); return c; }
constexpr C640 COS640 = make_c640();

struct alignas(128) FRow { u16 v[10 * 512]; };
constexpr FRow make_frow(int id) {
  FRow t{};
  const int cq = id / 10, st = id % 10;
  for (int par = 0; par < 2; ++par) for (int nt = 0; nt < 5; ++nt) for (int l = 0; l < 64; ++l) {
    const int kk = 80 * cq + 16 * nt + (l & 15);
    for (int j = 0; j < 8; ++j) {
      const int d = 64 * st + 32 * (j >> 2) + 8 * (l >> 4) + 2 * (j & 3) + par;
      const int m = (kk == 1) ? (d * 160 + 160 * par) % 640 : (d * (kk >> 1) + 160 * (kk & 1)) % 640;
      t.v[((par * 5 + nt) * 64 + l) * 8 + j] = COS640.v[m];
    }
  }
  return t;
}
template <int I> constexpr FRow FROW = make_frow(I);
struct alignas(128) FTab { FRow r[40]; };
template <int... I> constexpr FTab make_ftab(std::integer_sequence<int, I...>) { return FTab{{FROW<I>...}}; }
__device__ const FTab FT = make_ftab(std::make_integer_sequence<int, 40>{});

struct alignas(128) GRow { u16 v[10 * 3 * 512]; };
constexpr GRow make_grow(int id) {
  GRow t{};
  const int ct = id >> 1, od = id & 1;
  for (int ks = 0; ks < 10; ++ks) for (int nt = 0; nt < 3; ++nt) for (int l = 0; l < 64; ++l) {
    const int ci = 16 * nt + (l & 15), d = 40 * ct + ci;
    for (int j = 0; j < 8; ++j) {
      const int f = 32 * ks + 8 * (l >> 4) + j;
      t.v[((ks * 3 + nt) * 64 + l) * 8 + j] = (ci <= 40) ? COS640.v[(f * d + (od ? 160 : 0)) % 640] : (u16)((od && ci == 41 && f == 0) ? 0x3f80 : 0);
    }
  }
  return t;
}
template <int I> constexpr GRow GROW = make_grow(I);
struct alignas(128) GTab { GRow r[16]; };
template <int... I> constexpr GTab make_gtab(std::integer_sequence<int, I...>) { return GTab{{GROW<I>...}}; }
__device__ const GTab GT = make_gtab(std::make_integer_sequence<int, 16>{});

constexpr size_t WS_P   = 0;
constexpr size_t WS_Y   = WS_P + 2621440;
constexpr size_t WS_X1  = WS_Y + 2621440;
constexpr size_t WS_WT  = WS_X1 + 321 * 8192;
constexpr size_t WS_KVP = WS_WT + 352 * 8192;
constexpr size_t WS_M   = WS_KVP + 983040;
constexpr size_t WS_G   = WS_M + 819200;
constexpr size_t WS_ZP  = WS_G + 8192;
constexpr size_t WS_SYN = WS_ZP + 40960;

DI rsrc_t mk_rsrc(const void* p, unsigned bytes) { return __builtin_amdgcn_make_buffer_rsrc((void*)p, 0, bytes, 0x00020000); }
DI unsigned pk2(float a, float b) { f32x2 v = {a, b}; return __builtin_bit_cast(unsigned, __builtin_convertvector(v, bf16x2_t)); }
DI float bf_lo(unsigned u) { return __uint_as_float(u << 16); }
DI float bf_hi(unsigned u) { return __uint_as_float(u & 0xffff0000u); }
DI float dot2(unsigned a, unsigned b, float c) { return __builtin_amdgcn_fdot2_f32_bf16(__builtin_bit_cast(bf16x2_t, a), __builtin_bit_cast(bf16x2_t, b), c, false); }
DI float rbf(float a) { return bf_lo(pk2(a, a)); }
constexpr float kEps = 1e-6f, kL2E = 1.4426950408889634f;
DI float phi_f(float a) {
  float e;
  const float u = __builtin_fmaf(a, kL2E, -kEps * kL2E);
  asm("v_exp_f32_e64 %0, %1 clamp" : "=v"(e) : "v"(u));
  return __builtin_amdgcn_fmed3f(a, kEps, __builtin_inff()) + e;
}
DI bf16x8 pack8(const f32x4& a, const f32x4& b) {
  u32x4 p = {pk2(a[0], a[1]), pk2(a[2], a[3]), pk2(b[0], b[1]), pk2(b[2], b[3])};
  return __builtin_bit_cast(bf16x8, p);
}
DI u32x4 widen(u32x2 a, u32x2 b) {
  auto x = __builtin_amdgcn_permlane16_swap(a[0], b[0], false, false);
  auto y = __builtin_amdgcn_permlane16_swap(a[1], b[1], false, false);
  return (u32x4){x[0], y[0], x[1], y[1]};
}
#define WAIT_V_(n) asm volatile("s_waitcnt vmcnt(" #n ")" ::: "memory")
#define WAIT_V(n) WAIT_V_(n)
#define BC(v) __builtin_bit_cast(bf16x8, (v))
#define WAIT_L0() __builtin_amdgcn_s_waitcnt(0xC07F)
#define BAR() __builtin_amdgcn_s_barrier()
#define SCHED() __builtin_amdgcn_sched_barrier(0)
#define DMA16(rs, dst, vo, so) __builtin_amdgcn_raw_ptr_buffer_load_lds((rs), (LDS_AS void*)(dst), 16, (vo), (so), 0, 0)
#define TRREAD(p) __builtin_amdgcn_ds_read_tr16_b64_v4i16((LDS_AS s16x4*)(p))
#define ST16(v, rs, vo, so) __builtin_amdgcn_raw_buffer_store_b128((v), (rs), (vo), (so), ST_AUX)

#define TS_DECL
#define TS(i)
#define TC(i)
#define TS_FLUSH(kern)
#define TS_FLUSH2()
#define TS_ARG
#define TS_PASS

constexpr int K1_STAGE = 40960;
constexpr int K1_WF = 3 * K1_STAGE;
constexpr int K1_JUNK = K1_WF + 32768;
constexpr int K1_LDS = K1_JUNK + 2048;
#define K1_PF 1

struct Frags { bf16x8 a[2][2]; bf16x8 b[2][5]; };
struct K1Ctx { rsrc_t ra, rb; unsigned va[2], vb[3], ao[3][2], bo[3][5], ldsa, ldsb; int st, rs, wave; };

struct Half { s16x4 lo, hi; };
template <int I0, int I1> DI void k1_read(Frags& F, lptr sm, const unsigned (&ao)[2], const unsigned (&bo)[5]) {
#pragma unroll
  for (int i = I0; i < I1; ++i) {
    const int k = i / 7, j = i % 7;
    if (j < 2) F.a[k][j] = *(LDS_AS bf16x8*)(sm + ao[k] + j * 2048);
    else {
      const s16x4 lo = TRREAD(sm + bo[j - 2] + k * 12288), hi = TRREAD(sm + bo[j - 2] + k * 12288 + 512);
      F.b[k][j - 2] = __builtin_shufflevector(lo, hi, 0, 1, 2, 3, 4, 5, 6, 7);
    }
  }
}
DI void dma16_asm(rsrc_t rs, unsigned lds, unsigned vo, int so) {
  asm volatile("s_mov_b32 m0, %2\n\ts_nop 0\n\tbuffer_load_dwordx4 %0, %1, %3 offen lds" :: "v"(vo), "s"(rs), "s"(lds), "s"(so));
}
DI void pf_asm(rsrc_t rs, unsigned lds, unsigned vo, int so) {
  asm volatile("s_mov_b32 m0, %2\n\ts_nop 0\n\tbuffer_load_dword %0, %1, %3 offen lds" :: "v"(vo), "s"(rs), "s"(lds), "s"(so));
}
template <int BUF, int I> DI void k1_issue1(const K1Ctx& c) {
  if (I < 2) dma16_asm(c.ra, c.ldsa + BUF * K1_STAGE + I * 1024, c.va[I < 2 ? I : 0], c.st * 128);
  else dma16_asm(c.rb, c.ldsb + BUF * K1_STAGE + (I - 2) * 1024, c.vb[I < 2 ? 0 : I - 2], c.st * 64 * c.rs);
}
template <int BUF> DI void k1_issue(K1Ctx& c) {
  k1_issue1<BUF, 0>(c); k1_issue1<BUF, 1>(c); k1_issue1<BUF, 2>(c); k1_issue1<BUF, 3>(c); k1_issue1<BUF, 4>(c);
  c.st = (c.st == 9) ? 0 : c.st + 1;
}
template <bool SWAP, int I0, int I1> DI void k1_mma(f32x4 (&acc)[2][5], const Frags& F) {
#pragma unroll
  for (int i = I0; i < I1; ++i) {
    const int k = i / 10, mt = (i / 5) & 1, nt = i % 5;
    acc[mt][nt] = SWAP ? MFMA16(F.b[k][nt], F.a[k][mt], acc[mt][nt]) : MFMA16(F.a[k][mt], F.b[k][nt], acc[mt][nt]);
  }
}
template <bool SWAP, int N, int SEG> DI void k1_seg(f32x4 (&acc)[2][5], Frags& Fn, const Frags& Fp, lptr sm, K1Ctx& c) {
  constexpr int R0[6] = {0, 4, 7, 10, 12, 14};
  k1_read<R0[SEG], R0[SEG + 1]>(Fn, sm, c.ao[N % 3], c.bo[N % 3]);
  if (N > 0) {
    k1_mma<SWAP, 4 * SEG, 4 * SEG + 4>(acc, Fp);
    if (SEG < 2) {
      __builtin_amdgcn_sched_group_barrier(0x008, 1, 0); __builtin_amdgcn_sched_group_barrier(0x100, 2, 0);
      __builtin_amdgcn_sched_group_barrier(0x008, 1, 0); __builtin_amdgcn_sched_group_barrier(0x100, 2, 0);
      __builtin_amdgcn_sched_group_barrier(0x008, 1, 0); __builtin_amdgcn_sched_group_barrier(0x100, 2, 0);
      __builtin_amdgcn_sched_group_barrier(0x008, 1, 0);
    } else {
#pragma unroll
      for (int i = 0; i < 4; ++i) { __builtin_amdgcn_sched_group_barrier(0x008, 1, 0); __builtin_amdgcn_sched_group_barrier(0x100, 2, 0); }
    }
  }
  SCHED();
  if (N + 2 <= 9) k1_issue1<(N + 2) % 3, SEG>(c);
  SCHED();
}
template <bool SWAP, int N> DI void k1_step(f32x4 (&acc)[2][5], Frags& Fn, const Frags& Fp, lptr sm, K1Ctx& c) {
  WAIT_L0();
  BAR();
  SCHED();
  k1_seg<SWAP, N, 0>(acc, Fn, Fp, sm, c);
  k1_seg<SWAP, N, 1>(acc, Fn, Fp, sm, c);
  k1_seg<SWAP, N, 2>(acc, Fn, Fp, sm, c);
  k1_seg<SWAP, N, 3>(acc, Fn, Fp, sm, c);
  k1_seg<SWAP, N, 4>(acc, Fn, Fp, sm, c);
  c.st = (c.st == 9) ? 0 : c.st + 1;
}

struct DFrags { u32x4 a[2][2]; bf16x8 b[2][3]; };
constexpr int KD_STAGE = 26624;
template <int BUF, int I> DI void kd_issue1(const K1Ctx& c) {
  if (I < 2) dma16_asm(c.ra, c.ldsa + BUF * KD_STAGE + I * 1024, c.va[I & 1], c.st * 128);
  else if (I == 2) dma16_asm(c.rb, c.ldsb + BUF * KD_STAGE, c.vb[0], c.st * 10240);
  else dma16_asm(c.rb, c.wave < 2 ? c.ldsb + BUF * KD_STAGE + 8192 : c.ldsa - 2048 * c.wave + K1_JUNK, c.vb[1], c.st * 10240);
}
template <int BUF> DI void kd_issue(K1Ctx& c) {
  kd_issue1<BUF, 0>(c); kd_issue1<BUF, 2>(c); kd_issue1<BUF, 1>(c); kd_issue1<BUF, 3>(c);
  c.st = (c.st == 9) ? 0 : c.st + 1;
}
template <int NT> DI void kd_mma(f32x4 (&acc)[2][2][3], const DFrags& F) {
#pragma unroll
  for (int mt = 0; mt < 2; ++mt) {
    u32x4 ev, od;
#pragma unroll
    for (int k = 0; k < 2; ++k)
#pragma unroll
      for (int i = 0; i < 2; ++i) {
        ev[2 * k + i] = __builtin_amdgcn_perm(F.a[mt][k][2 * i + 1], F.a[mt][k][2 * i], 0x05040100u);
        od[2 * k + i] = __builtin_amdgcn_perm(F.a[mt][k][2 * i + 1], F.a[mt][k][2 * i], 0x07060302u);
      }
#pragma unroll
    for (int nt = 0; nt < NT; ++nt) {
      acc[0][mt][nt] = MFMA16(F.b[0][nt], BC(ev), acc[0][mt][nt]);
      acc[1][mt][nt] = MFMA16(F.b[1][nt], BC(od), acc[1][mt][nt]);
    }
  }
}
template <int NT, int N> DI void kd_step(f32x4 (&acc)[2][2][3], DFrags& Fn, const DFrags& Fp, lptr sm, K1Ctx& c) {
  WAIT_L0();
  BAR();
  SCHED();
  if (N + 3 <= 9) kd_issue<(N + 3) % 4>(c);
  SCHED();
  constexpr int H = (N % 4) >> 1, O = (N & 1) * KD_STAGE;
#pragma unroll
  for (int mt = 0; mt < 2; ++mt)
#pragma unroll
    for (int k = 0; k < 2; ++k) Fn.a[mt][k] = *(LDS_AS u32x4*)(sm + c.ao[H][k] + O + mt * 2048);
#pragma unroll
  for (int par = 0; par < 2; ++par)
#pragma unroll
    for (int nt = 0; nt < NT; ++nt) Fn.b[par][nt] = *(LDS_AS bf16x8*)(sm + c.bo[H][0] + O + (par * 5 + nt) * 1024);
  if (N > 0) {
    kd_mma<NT>(acc, Fp);
    __builtin_amdgcn_sched_group_barrier(0x100, 2, 0);
    __builtin_amdgcn_sched_group_barrier(0x002, 8, 0);
#pragma unroll
    for (int i = 0; i < 2 * NT; ++i) { __builtin_amdgcn_sched_group_barrier(0x008, 1, 0); __builtin_amdgcn_sched_group_barrier(0x100, 1, 0); __builtin_amdgcn_sched_group_barrier(0x002, 2, 0); }
  }
  SCHED();
}

DI u32x2 make_tag(unsigned e) { return (u32x2){e + 1u, (e + 1u) * 0x9E3779B9u ^ 0x5bd1e995u}; }
DI int flag_slot(int b) { return ((b & 7) >> 1) * 64 + (b & 1) * 32 + (b >> 3); }
constexpr size_t WS_EPOCH = WS_SYN + 3 * 65536;
DI void flag_store(char* ws, int which, unsigned e, int lane) {
  __builtin_amdgcn_raw_buffer_store_b64(make_tag(e), mk_rsrc(ws + WS_SYN + which * 65536, 65536), lane * 2048 + flag_slot(blockIdx.x) * 8, 0, 16);
}
template <int NT, class FL, class FS> DI void kd_main(char* ws, lptr sm, K1Ctx& c, int rt, int cq, unsigned e, int lane, FL filt_load, FS filt_store TS_ARG) {
  const int wm = c.wave & 3, r = lane & 15, g = lane >> 4;
  constexpr int T0 = NT == 3 ? 0 : 3;
  DFrags D0, D1;
  f32x4 acc[2][2][3];
#pragma unroll
  for (int u = 0; u < 2; ++u) {
    c.ao[u][0] = 2 * u * KD_STAGE + (32 * wm + r) * 128 + ((g ^ (r >> 1)) * 16);
    c.ao[u][1] = 2 * u * KD_STAGE + (32 * wm + r) * 128 + (((4 + g) ^ (r >> 1)) * 16);
    c.bo[u][0] = 2 * u * KD_STAGE + 16384 + T0 * 1024 + lane * 16;
    asm volatile("" : "+v"(c.ao[u][0]), "+v"(c.ao[u][1]), "+v"(c.bo[u][0]));
  }
#pragma unroll
  for (int i = 0; i < 4 * NT; ++i) { acc[i / (2 * NT)][(i / NT) & 1][i % NT] = (f32x4){0.f, 0.f, 0.f, 0.f}; asm volatile("" : "+v"(acc[i / (2 * NT)][(i / NT) & 1][i % NT])); }
  TS(1);
  WAIT_V(8); kd_step<NT, 0>(acc, D0, D1, sm, c);
  TS(2);
  WAIT_V(8); kd_step<NT, 1>(acc, D1, D0, sm, c);
  filt_load();
  WAIT_V(12); kd_step<NT, 2>(acc, D0, D1, sm, c);
  WAIT_V(12); kd_step<NT, 3>(acc, D1, D0, sm, c);
  WAIT_V(12); kd_step<NT, 4>(acc, D0, D1, sm, c);
  WAIT_V(8); kd_step<NT, 5>(acc, D1, D0, sm, c);
  WAIT_V(8); kd_step<NT, 6>(acc, D0, D1, sm, c);
  filt_store();
  WAIT_V(11); kd_step<NT, 7>(acc, D1, D0, sm, c);
  WAIT_V(7); kd_step<NT, 8>(acc, D0, D1, sm, c);
  WAIT_V(3); kd_step<NT, 9>(acc, D1, D0, sm, c);
  kd_mma<NT>(acc, D1);
  TS(3);
  const rsrc_t rx = mk_rsrc(ws + WS_X1 + (wm >> 1) * 4096 + (rt >> 2) * 1024 + (rt & 3) * 256 + (wm & 1) * 128, 321 * 8192);
  const bool up = r >= 8;
  const int fb = 40 * cq + 8 * T0 + 2 * g;
  const bool sp = (fb == 0);
#pragma unroll
  for (int nt = 0; nt < NT; ++nt) {
    unsigned it[4][2];
#pragma unroll
    for (int mt = 0; mt < 2; ++mt) {
      const f32x4 E = acc[0][mt][nt], O = acc[1][mt][nt], P = E + O, M = E - O;
      it[0][mt] = pk2(P[0], P[1]); it[1][mt] = pk2(P[2], P[3]); it[2][mt] = pk2(M[0], -M[1]); it[3][mt] = pk2(M[2], -M[3]);
      if (NT == 3 && nt == 0) {
        it[0][mt] = sp ? (it[0][mt] & 0xffffu) : it[0][mt]; it[2][mt] = sp ? (it[2][mt] & 0xffffu) : it[2][mt];
        __builtin_amdgcn_raw_buffer_store_b32(pk2(E[1], O[1]), rx, sp ? 160 * 8192 + (r & 7) * 16 + (r >> 3) * 8 + mt * 4 : 0x7fffffff, 0, ST_AUX);
      }
    }
#pragma unroll
    for (int h = 0; h < 2; ++h) {
      const unsigned t0 = __builtin_amdgcn_update_dpp(0u, up ? it[2 * h][0] : it[2 * h + 1][0], 0x128, 0xf, 0xf, false);
      const unsigned t1 = __builtin_amdgcn_update_dpp(0u, up ? it[2 * h][1] : it[2 * h + 1][1], 0x128, 0xf, 0xf, false);
      const u32x4 o = {up ? t0 : it[2 * h][0], up ? t1 : it[2 * h][1], up ? it[2 * h + 1][0] : t0, up ? it[2 * h + 1][1] : t1};
      const int f0 = fb + 8 * nt + (r >> 3);
      ST16(o, rx, (h ? 320 - f0 : f0) * 8192 + (r & 7) * 16, 0);
    }
  }
}

struct Args { const u16 *x, *wqkv, *wo, *wr, *wi, *gw; char* ws; u16* out; };

DI void phase1(const Args& A, lptr sm, f32x4 (&pq)[2][5], unsigned e TS_ARG) {
  const u16* x = A.x; const u16* wqkv = A.wqkv; const u16* wr = A.wr; const u16* wi = A.wi; char* ws = A.ws;
  const int tid = threadIdx.x, lane = tid & 63;
  const int wave = __builtin_amdgcn_readfirstlane(tid >> 6);
  const int b = blockIdx.x;
  const int xcd = b & 7, idx = b >> 3;
  const int rt = (xcd & 1) * 8 + (idx & 7);
  const int ct = (xcd >> 1) * 4 + (idx >> 3);
  const bool dft = ct >= 12, iskv = ct >= 4 && ct < 12;
  const int c0 = iskv ? 80 * (ct - 4) : 160 * (ct & 3);
  const int L0 = c0 >> 6, p0 = (c0 & 63) >> 4;
  const int xs = (p0 == 0 || p0 == 3) ? 1 : 2;
  const int bigB = p0 >= 2 ? 1 : 0;

  K1Ctx c;
  c.wave = wave;
  c.ra = mk_rsrc(x, 2048 * 1280);
  c.rb = dft ? mk_rsrc((const char*)&FT + (ct & 3) * 102400, 102400) : mk_rsrc(wqkv, 640 * 3840);
  c.rs = 3840;
  auto grow = [&](int row) { return dft ? 32 * (4 * rt + 2 * ((row >> 3) & 1) + ((row >> 4) & 1)) + 8 * (row >> 5) + (row & 7) : 128 * rt + row; };
#pragma unroll
  for (int i = 0; i < 2; ++i) {
    const int row = 16 * wave + 8 * i + (lane >> 3);
    c.va[i] = grow(row) * 1280 + ((lane & 7) ^ ((row >> 1) & 7)) * 16;
  }
  c.ldsa = (unsigned)(size_t)sm + 2 * wave * 1024;
  c.st = (((((idx & 7) * 5 + (idx >> 3) * 10) >> 2) % 10) * K1_ROT / 10) * (10 / K1_ROT);
  k1_issue1<0, 0>(c); k1_issue1<0, 1>(c);
  SCHED();
  {
    const int row = lane >> 3, hf = lane & 1;
    const int pair = ((lane & 7) >> 1) ^ (((row >> 1) & 1) | ((wave & 1) << 1));
    const unsigned rowb = (8 * wave + row) * c.rs;
    const bool kown = bigB ? (pair >= p0) : (pair <= p0);
    c.vb[0] = rowb + (iskv ? 1280 + (L0 + bigB) * 128 : L0 * 128) + pair * 32 + hf * 16;
    c.vb[1] = rowb + (iskv ? 2560 + (L0 + bigB) * 128 + (hf ^ 1) * 16 : (L0 + 1) * 128 + hf * 16) + pair * 32;
    c.vb[2] = rowb + (iskv ? (L0 + 1 - bigB) * 128 + (kown ? 1280 + pair * 32 + hf * 16 : 2560 + (pair ^ xs) * 32 + (hf ^ 1) * 16)
                           : (L0 + 2) * 128 + pair * 32 + hf * 16);
  }
  c.ldsb = (unsigned)(size_t)sm + 16384 + (dft ? 1 : 3) * wave * 1024;
  if (dft) { c.vb[0] = wave * 1024 + lane * 16; c.vb[1] = wave < 2 ? c.vb[0] + 8192 : 0x7fffffffu; }
  if (dft) { kd_issue1<0, 2>(c); kd_issue1<0, 3>(c); }
  else { k1_issue1<0, 2>(c); k1_issue1<0, 3>(c); k1_issue1<0, 4>(c); }
  c.st = (c.st == 9) ? 0 : c.st + 1;
  BAR();
  if (dft) { kd_issue<1>(c); kd_issue<2>(c); }
  else k1_issue<1>(c);

  const int wm = wave >> 1, wn = wave & 1;
  const int r = lane & 15, g = lane >> 4, q = (lane & 15) >> 2, p = lane & 3;
  const int hsw = ((q >> 1) & 1) | ((g & 1) << 1);
  const unsigned bbase = 16384 + g * 3072 + q * 128;
  c.ao[0][0] = (32 * wm + r) * 128 + ((g ^ (r >> 1)) * 16);
  c.ao[0][1] = (32 * wm + r) * 128 + (((4 + g) ^ (r >> 1)) * 16);
  f32x4 acc[2][5];
  Frags F0, F1;
  const rsrc_t rpf = (wave & 1) ? mk_rsrc(&GT, 491520) : mk_rsrc(A.wo, 819200);
  const unsigned vpf = (wave < 2 && lane < ((wave & 1) ? 20 : 34)) ? ((ct * 16 + rt) * ((wave & 1) ? 20 : 34) + lane) * 128 : 0x7fffffffu;

#define K1_HEAD(SWAP, HOOK, W2)                                                                          \
  _Pragma("unroll") for (int u = 1; u < 3; ++u) {                                              \
    _Pragma("unroll") for (int k = 0; k < 2; ++k) { c.ao[u][k] = c.ao[0][k] + u * K1_STAGE; asm volatile("" : "+v"(c.ao[u][k])); }     \
    _Pragma("unroll") for (int k = 0; k < 5; ++k) { c.bo[u][k] = c.bo[0][k] + u * K1_STAGE; asm volatile("" : "+v"(c.bo[u][k])); }     \
  }                                                                                            \
  _Pragma("unroll") for (int mt = 0; mt < 2; ++mt)                                             \
    _Pragma("unroll") for (int nt = 0; nt < 5; ++nt) asm volatile("" : "+v"(acc[mt][nt]));     \
  TS(1);                                                                                       \
  WAIT_V(5); k1_step<SWAP, 0>(acc, F0, F1, sm, c);                                           \
  TS(2);                                                                                       \
  WAIT_V(5); k1_step<SWAP, 1>(acc, F1, F0, sm, c); HOOK;                                             \
  WAIT_V(W2); k1_step<SWAP, 2>(acc, F0, F1, sm, c);                                             \
  pf_asm(rpf, (unsigned)(size_t)sm + K1_JUNK + wave * 256, vpf, 0);

  if (iskv) {
#pragma unroll
    for (int nt = 0; nt < 5; ++nt) {
      const int u = 4 * nt + p;
      const int isv = u >= 10 ? 1 : 0;
      const int pc = 4 * p0 + 10 * wn + u - 10 * isv;
      const int gp = pc >> 2, big = ((gp >> 2) == bigB);
      const int slot = (gp & 3) ^ ((!big && isv) ? xs : 0);
      c.bo[0][nt] = bbase + (big ? isv : 2) * 1024 + ((slot ^ hsw) * 32) + (((pc & 3) ^ (isv << 1)) * 8);
      const float i0 = ((nt < 2) || (nt == 2 && r < 8)) ? kEps : 0.f;
#pragma unroll
      for (int mt = 0; mt < 2; ++mt) acc[mt][nt] = (f32x4){i0, i0, i0, i0};
    }
    unsigned ko[10];
    {
      constexpr int TI[10] = {0, 0, 1, 1, 1, 2, 2, 0, 1, 2}, TJ[10] = {2, 3, 2, 3, 4, 3, 4, -1, -1, -1};
#pragma unroll
      for (int i = 0; i < 10; ++i) {
        const int ti = TI[i], tj = TJ[i];
        const int ur = 16 * ti + r, hk = ur >= 20 ? 1 : 0;
        const int uc = 16 * tj + 4 * g - 40, hv = uc >= 20 ? 1 : 0;
        const bool valid = tj < 0 ? (ur < 40 && g == 0) : (ur < 40 && uc >= 0 && hk == hv);
        const int off = wave * 3840 + hk * 1920 + (ur - 20 * hk) * 96 + (tj < 0 ? 80 : (uc - 20 * hv) * 4);
        ko[i] = K1_STAGE + (valid ? off : 8 * 3840 + lane * 16);
        asm volatile("" : "+v"(ko[i]));
      }
    }
    K1_HEAD(false, , 5)
    WAIT_V(6); k1_step<false, 3>(acc, F1, F0, sm, c);
    WAIT_V(6); k1_step<false, 4>(acc, F0, F1, sm, c);
    WAIT_V(5); k1_step<false, 5>(acc, F1, F0, sm, c);
    WAIT_V(5); k1_step<false, 6>(acc, F0, F1, sm, c);
    WAIT_V(5); k1_step<false, 7>(acc, F1, F0, sm, c);
    WAIT_V(5); k1_step<false, 8>(acc, F0, F1, sm, c);
    WAIT_V(0); k1_step<false, 9>(acc, F1, F0, sm, c);
    k1_mma<false, 0, 20>(acc, F1);
    TS(3);
    bf16x8 fr[5];
#pragma unroll
    for (int nt = 0; nt < 5; ++nt) {
#pragma unroll
      for (int m = 0; m < 2; ++m)
#pragma unroll
        for (int j = 0; j < 4; ++j) {
          if (nt < 2) acc[m][nt][j] = phi_f(acc[m][nt][j]);
          if (nt == 2) { const float ph = phi_f(acc[m][nt][j]); acc[m][nt][j] = (r < 8) ? ph : acc[m][nt][j]; }
        }
      fr[nt] = pack8(acc[0][nt], acc[1][nt]);
    }
    const bf16x8 ones = {0x3f80, 0x3f80, 0x3f80, 0x3f80, 0x3f80, 0x3f80, 0x3f80, 0x3f80};
    {
      constexpr int TI[10] = {0, 0, 1, 1, 1, 2, 2, 0, 1, 2}, TJ[10] = {2, 3, 2, 3, 4, 3, 4, -1, -1, -1};
      const f32x4 zz = {0.f, 0.f, 0.f, 0.f};
      f32x4 d[10];
#pragma unroll
      for (int i = 0; i < 10; ++i) d[i] = MFMA16(TJ[i] < 0 ? ones : fr[TJ[i] < 0 ? 0 : TJ[i]], fr[TI[i]], zz);
#pragma unroll
      for (int i = 0; i < 10; ++i) *(LDS_AS f32x4*)(sm + ko[i]) = d[i];
    }
    WAIT_L0();
    BAR();
    const rsrc_t rk = mk_rsrc(ws + WS_KVP + ((size_t)rt * 32 + 4 * (ct - 4)) * 1920, 7680);
    LDS_AS f32x4* src = (LDS_AS f32x4*)(sm + K1_STAGE);
    const f32x4 s0 = (src[tid] + src[tid + 480]) + (src[tid + 960] + src[tid + 1440]);
    ST16(__builtin_bit_cast(u32x4, s0), rk, tid * 16, 0);
    if (wave >= 6) {
      LDS_AS float* zs = (LDS_AS float*)(sm + K1_STAGE) + min(tid - 384, 79) * 24 + 20;
      const float z = (zs[0] + zs[1920]) + (zs[3840] + zs[5760]);
      __builtin_amdgcn_raw_buffer_store_b32(__float_as_uint(z), mk_rsrc(ws + WS_ZP + rt * 2560 + (ct - 4) * 320, 320), (tid - 384) * 4, 0, ST_AUX);
    }
  } else {
    const int un = (dft ? 64 : 0) + rt * 4 + (ct & 3), mo = un >> 1, fh = un & 1;
    auto filt_load = [&]() {
      const rsrc_t rf = mk_rsrc((wave >> 2) ? wi : wr, 2048 * 642);
      const int kb = 16 * (mo >> 4) + 8 * (mo & 1) + 64 * (16 * ((mo >> 3) & 1) + 4 * ((mo >> 1) & 3) + (wave & 3));
      const unsigned v0 = (lane & 31) < 21 ? (lane >> 5) * 640 + (lane & 31) * 16 : 0x7fffffffu;
#pragma unroll
      for (int i = 0; i < 4; ++i)
        dma16_asm(rf, (unsigned)(size_t)sm + K1_WF + (wave * 4 + i) * 1024, v0, kb * 642 + 320 * fh + i * 1280);
    };
    auto filt_store = [&]() {
      const rsrc_t rw = mk_rsrc(ws + WS_WT + (size_t)fh * 160 * 8192 + mo * 128, (160 + fh) * 8192 - mo * 128);
      const int i8 = lane & 7;
#pragma unroll
      for (int rd = 0; rd < 3; ++rd) {
        const int fl = (rd * 8 + wave) * 8 + (lane >> 3);
        lptr src = sm + K1_WF + i8 * 514 + min(fl, 160) * 2;
        u32x4 o;
#pragma unroll
        for (int j = 0; j < 4; ++j) o[j] = (unsigned)*(LDS_AS u16*)(src + j * 4096) | ((unsigned)*(LDS_AS u16*)(src + 16384 + j * 4096) << 16);
        ST16(o, rw, fl * 8192 + i8 * 16, 0);
      }
      SCHED();
    };
    if (!dft) {
#pragma unroll
      for (int nt = 0; nt < 5; ++nt) {
        const int gp = p0 + 5 * wn + nt;
        c.bo[0][nt] = bbase + (gp >> 2) * 1024 + (((gp & 3) ^ hsw) * 32) + p * 8;
      }
#pragma unroll
      for (int mt = 0; mt < 2; ++mt)
#pragma unroll
        for (int nt = 0; nt < 5; ++nt) acc[mt][nt] = (f32x4){kEps, kEps, kEps, kEps};
      K1_HEAD(true, filt_load(), 9)
      WAIT_V(10); k1_step<true, 3>(acc, F1, F0, sm, c);
      WAIT_V(6); k1_step<true, 4>(acc, F0, F1, sm, c);
      filt_store();
      WAIT_V(8); k1_step<true, 5>(acc, F1, F0, sm, c);
      WAIT_V(8); k1_step<true, 6>(acc, F0, F1, sm, c);
      WAIT_V(5); k1_step<true, 7>(acc, F1, F0, sm, c);
      if (wave == 0) flag_store(ws, 1, e, lane);
      WAIT_V(5); k1_step<true, 8>(acc, F0, F1, sm, c);
      WAIT_V(0); k1_step<true, 9>(acc, F1, F0, sm, c);
      k1_mma<true, 0, 20>(acc, F1);
      TS(3);
#pragma unroll
      for (int mt = 0; mt < 2; ++mt)
#pragma unroll
        for (int nt = 0; nt < 5; ++nt)
#pragma unroll
          for (int j = 0; j < 4; ++j) pq[mt][nt][j] = phi_f(acc[mt][nt][j]);
    } else {
      if (wave < 4) kd_main<3>(ws, sm, c, rt, ct & 3, e, lane, filt_load, filt_store TS_PASS);
      else kd_main<2>(ws, sm, c, rt, ct & 3, e, lane, filt_load, filt_store TS_PASS);
    }
  }
  TS(4);
}

DI void publish(char* ws, int which, unsigned e) {
  WAIT_V(0);
  BAR();
  if (threadIdx.x < 64) flag_store(ws, which, e, threadIdx.x);
}
DI bool poll(char* ws, int which, unsigned e, int need, int lane, unsigned tries) {
  {
    const rsrc_t rf = mk_rsrc(ws + WS_SYN + which * 65536 + (blockIdx.x & 31) * 2048, 2048);
    const u32x2 t = make_tag(e);
    bool n0 = (need >> (lane < 32 ? 0 : 1)) & 1, n1 = (need >> (lane < 32 ? 1 : 2)) & 1;
    for (unsigned spin = 0; spin < tries; ++spin) {
      const u32x4 f0 = __builtin_amdgcn_raw_buffer_load_b128(rf, n0 ? lane * 16 : 0x7fffffffu, 0, 16);
      const u32x4 f1 = __builtin_amdgcn_raw_buffer_load_b128(rf, n1 ? lane * 16 + 1024 : 0x7fffffffu, 0, 16);
      n0 = n0 && !(f0[0] == t[0] && f0[1] == t[1] && f0[2] == t[0] && f0[3] == t[1]);
      n1 = n1 && !(f1[0] == t[0] && f1[1] == t[1] && f1[2] == t[0] && f1[3] == t[1]);
      if (!__any(n0 || n1)) return true;
      __builtin_amdgcn_s_sleep(POLL_SLEEP);
    }
  }
  return false;
}
DI void await(char* ws, int which, unsigned e, int need, int wave, int lane) {
  if (wave == 7) poll(ws, which, e, need, lane, 1u << 17);
  asm volatile("" ::: "memory");
  BAR();
  asm volatile("" ::: "memory");
}
DI void dma16_sc1(rsrc_t rs, unsigned lds, unsigned vo, int so) {
  asm volatile("s_mov_b32 m0, %2\n\ts_nop 0\n\tbuffer_load_dwordx4 %0, %1, %3 offen sc1 lds" :: "v"(vo), "s"(rs), "s"(lds), "s"(so));
}
#define LD16(rs, vo, so) __builtin_amdgcn_raw_buffer_load_b128((rs), (vo), (so), LD_AUX)

constexpr int P3_HALF = 71680;

DI void p3_load_g(lptr sm, int wave, int lane) {
  const int b = blockIdx.x, ct = ((b & 7) >> 2) * 4 + (b >> 6);
  const rsrc_t rg = mk_rsrc((const char*)&GT + (size_t)(2 * ct) * 30720, 61440);
#pragma unroll
  for (int i = 0; i < 9; ++i) {
    const int n = min(wave * 9 + i, 59);
    if (wave < 7)
    dma16_asm(rg, (unsigned)(size_t)sm + (n >= 30 ? P3_HALF + 40960 - 30720 : 40960) + n * 1024, lane * 16, n * 1024);
  }
}

struct FftC {
  const u32x4 (&fre)[4]; const u32x4 (&fim)[4]; const u32x4 (&fc)[2]; const u32x4 (&fs)[2]; const u32x4 (&fn)[2];
  const f32x2 (&tc)[2][2]; const f32x2 (&tsn)[2][2]; const unsigned (&uo)[2][4]; const unsigned (&ro)[4]; unsigned vy;
};
DI f32x2 half2(const f32x4& v, int h) { return h ? (f32x2){v[2], v[3]} : (f32x2){v[0], v[1]}; }
constexpr int FSECOND = 304;
template <int NC> DI void fft_run(char* ws, unsigned e, lptr sm, const FftC& T, int fu0, int wave8, int lane TS_ARG) {
  const int wave = wave8 & 3;
  const unsigned smu = (unsigned)(size_t)sm;
  const f32x4 z4 = {0.f, 0.f, 0.f, 0.f};
  await(ws, 1, e, 5, wave8, lane);
  TS(7);
  u32x4 wv[NC][2];
#pragma unroll
  for (int c = 0; c < NC; ++c) {
    const rsrc_t rx = mk_rsrc(ws + WS_X1 + (size_t)min((c ? FSECOND + (fu0 >> 1) : fu0), 320) * 8192, 8192);
    dma16_sc1(rx, smu + c * 16384 + wave * 2048, lane * 16, wave * 2048);
    dma16_sc1(rx, smu + c * 16384 + wave * 2048 + 1024, lane * 16, wave * 2048 + 1024);
  }
#pragma unroll
  for (int c = 0; c < NC; ++c) {
    const rsrc_t rw = mk_rsrc(ws + WS_WT + (size_t)min((c ? FSECOND + (fu0 >> 1) : fu0), 320) * 8192, 8192);
    wv[c][0] = LD16(rw, lane * 16, wave * 2048);
    wv[c][1] = LD16(rw, lane * 16 + 1024, wave * 2048);
  }
  SCHED();
  WAIT_V(0);
#pragma unroll
  for (int c = 0; c < NC; ++c) asm volatile("" : "+v"(wv[c][0]), "+v"(wv[c][1]));
  BAR();
  SCHED();
  TS(8);
  f32x2 wre[NC][2][2], wim[NC][2][2];
#pragma unroll
  for (int c = 0; c < NC; ++c) {
    const int f = min((c ? FSECOND + (fu0 >> 1) : fu0), 320);
    const float sc = ((f == 0 || f == 320) ? 1.f : 2.f) / (2048.f * 640.f);
#pragma unroll
    for (int t = 0; t < 2; ++t)
#pragma unroll
      for (int h = 0; h < 2; ++h) {
        wre[c][t][h] = (f32x2){bf_lo(wv[c][t][2 * h]), bf_lo(wv[c][t][2 * h + 1])} * sc;
        wim[c][t][h] = (f32x2){bf_hi(wv[c][t][2 * h]), bf_hi(wv[c][t][2 * h + 1])} * sc;
      }
  }
#define FOR_C _Pragma("unroll") for (int c = 0; c < NC; ++c)
#define FOR_T _Pragma("unroll") for (int t = 0; t < 2; ++t)
#define FOR_H _Pragma("unroll") for (int h = 0; h < 2; ++h)
#define FOR_K _Pragma("unroll") for (int ks = 0; ks < 4; ++ks)
  f32x4 re[NC][2], im[NC][2];
  u32x4 bre[NC], bim[NC];
  {
    bf16x8 xa[NC][2][4];
    FOR_K FOR_C FOR_T xa[c][t][ks] = *(LDS_AS bf16x8*)(sm + c * 16384 + lane * 16 + (t * 4 + ks) * 1024);
    FOR_K FOR_C FOR_T {
      re[c][t] = MFMA16(xa[c][t][ks], BC(T.fre[ks]), ks ? re[c][t] : z4);
      im[c][t] = MFMA16(xa[c][t][ks], BC(T.fim[ks]), ks ? im[c][t] : z4);
    }
  }
  FOR_C FOR_T FOR_H {
    const f32x2 a = half2(re[c][t], h), d = half2(im[c][t], h);
    const f32x2 x = a * T.tc[t][h] + d * T.tsn[t][h], y = d * T.tc[t][h] - a * T.tsn[t][h];
    bre[c][2 * t + h] = pk2(x[0], x[1]); bim[c][2 * t + h] = pk2(y[0], y[1]);
  }
  FOR_C FOR_T { re[c][t] = MFMA16(BC(T.fc[t]), BC(bre[c]), z4); im[c][t] = MFMA16(BC(T.fc[t]), BC(bim[c]), z4); }
  FOR_C FOR_T { re[c][t] = MFMA16(BC(T.fs[t]), BC(bim[c]), re[c][t]); im[c][t] = MFMA16(BC(T.fn[t]), BC(bre[c]), im[c][t]); }
  FOR_C FOR_T FOR_H {
    const f32x2 a = half2(re[c][t], h), d = half2(im[c][t], h);
    const f32x2 x = a * wre[c][t][h] - d * wim[c][t][h], y = a * wim[c][t][h] + d * wre[c][t][h];
    bre[c][2 * t + h] = pk2(x[0], x[1]); bim[c][2 * t + h] = pk2(y[0], y[1]);
  }
  FOR_C FOR_T { re[c][t] = MFMA16(BC(T.fc[t]), BC(bre[c]), z4); im[c][t] = MFMA16(BC(T.fc[t]), BC(bim[c]), z4); }
  FOR_C FOR_T { re[c][t] = MFMA16(BC(T.fn[t]), BC(bim[c]), re[c][t]); im[c][t] = MFMA16(BC(T.fs[t]), BC(bre[c]), im[c][t]); }
  FOR_C FOR_T FOR_H {
    const f32x2 a = half2(re[c][t], h), d = half2(im[c][t], h);
    const f32x2 x = a * T.tc[t][h] - d * T.tsn[t][h], y = -(d * T.tc[t][h]) - a * T.tsn[t][h];
    *(LDS_AS unsigned*)(sm + c * 16384 + T.uo[t][2 * h]) = pk2(x[0], y[0]);
    *(LDS_AS unsigned*)(sm + c * 16384 + T.uo[t][2 * h + 1]) = pk2(x[1], y[1]);
  }
  WAIT_L0();
  BAR();
  SCHED();
  {
    bf16x8 ua[NC][2][4];
    FOR_K FOR_C FOR_T ua[c][t][ks] = *(LDS_AS bf16x8*)(sm + c * 16384 + T.ro[ks] + t * 4096);
    FOR_K FOR_C FOR_T {
      re[c][t] = MFMA16(ua[c][t][ks], BC(T.fre[ks]), ks ? re[c][t] : z4);
      im[c][t] = MFMA16(ua[c][t][ks], BC(T.fim[ks]), ks ? im[c][t] : z4);
    }
  }
  FOR_C {
    const int fu = (c ? FSECOND + (fu0 >> 1) : fu0), f = min(fu, 320);
    const rsrc_t ry = mk_rsrc(ws + WS_Y + (size_t)f * 4096, fu > 320 ? 0 : ((f == 0 || f == 320) ? 4096 : 321 * 4096));
    ST16(widen((u32x2){pk2(re[c][0][0], re[c][0][1]), pk2(re[c][0][2], re[c][0][3])}, (u32x2){pk2(re[c][1][0], re[c][1][1]), pk2(re[c][1][2], re[c][1][3])}), ry, T.vy, 0);
    ST16(widen((u32x2){pk2(im[c][0][0], im[c][0][1]), pk2(im[c][0][2], im[c][0][3])}, (u32x2){pk2(im[c][1][0], im[c][1][1]), pk2(im[c][1][2], im[c][1][3])}), ry, T.vy, 320 * 4096);
  }
}

template <bool F> DI void phase2(const Args& A, lptr sm0, unsigned e, const f32x4 (&pq)[2][5] TS_ARG) {
  char* ws = A.ws;
  const int lane = threadIdx.x & 63, tid = threadIdx.x & 255;
  const int wave8 = __builtin_amdgcn_readfirstlane(threadIdx.x >> 6);
  const int wave = wave8 & 3, team = wave8 >> 2;
  lptr sm = sm0 + team * P3_HALF;
  const unsigned smu = (unsigned)(size_t)sm;
  const int b = blockIdx.x, xcd = b & 7, idx = b >> 3;
  const int rt1 = (xcd & 1) * 8 + (idx & 7), ct1 = (xcd >> 1) * 4 + (idx >> 3);
  const bool iskv = ct1 >= 4 && ct1 < 12;
  const int v = (ct1 - 4) * 16 + rt1;
  const int r = lane & 15, g = lane >> 4;
  const f32x4 z4 = {0.f, 0.f, 0.f, 0.f};
  const bool isfft = ct1 >= 12 || (iskv && v < 88);
  publish(ws, 1, e);
  TS(5);
  if (isfft) {
    const int fu0 = (iskv ? 128 + 2 * v : 2 * (rt1 * 4 + (ct1 & 3))) + team;
    u32x4 fre[4], fim[4], fc[2], fs[2], fn[2];
    f32x2 tc[2][2], tsn[2][2];
    unsigned uo[2][4], ro[4];
    {
      const float a64 = (float)(16 * wave + r) * (1.f / 64.f);
      const unsigned m2 = (r & 2) ? 0x80008000u : 0u, m1 = (r & 1) ? 0x80008000u : 0u;
      const unsigned m3 = ((r + 1) & 2) ? 0x80008000u : 0u;
#pragma unroll
      for (int j = 0; j < 4; ++j) {
        const float ang = a64 * (float)(4 * g + j);
        const float c = __builtin_amdgcn_cosf(ang), s = __builtin_amdgcn_sinf(ang);
        const unsigned p = pk2(c, s), q = pk2(-s, c);
        fre[0][j] = p; fim[0][j] = q;
        fre[1][j] = ((r & 1) ? q : p) ^ m2;
        fim[1][j] = ((r & 1) ? p : q) ^ m3;
        fre[2][j] = p ^ m1; fim[2][j] = q ^ m1;
        fre[3][j] = fre[1][j] ^ m1; fim[3][j] = fim[1][j] ^ m1;
      }
      const float a32 = (float)r * (1.f / 32.f);
#pragma unroll
      for (int j = 0; j < 2; ++j) {
        const float a0 = a32 * (float)(4 * g + 2 * j), a1 = a0 + a32;
        const unsigned c = pk2(__builtin_amdgcn_cosf(a0), __builtin_amdgcn_cosf(a1)), s = pk2(__builtin_amdgcn_sinf(a0), __builtin_amdgcn_sinf(a1));
        fc[0][j] = c; fc[0][2 + j] = c ^ m1; fs[0][j] = s; fs[0][2 + j] = s ^ m1;
      }
#pragma unroll
      for (int j = 0; j < 4; ++j) {
        fc[1][j] = fc[0][j] ^ 0x80000000u; fs[1][j] = fs[0][j] ^ 0x80000000u;
        fn[0][j] = fs[0][j] ^ 0x80008000u; fn[1][j] = fs[0][j] ^ 0x00008000u;
        ro[j] = 8192 + r * 256 + (((4 * j + g) ^ r) * 16);
      }
#pragma unroll
      for (int t = 0; t < 2; ++t)
#pragma unroll
        for (int j = 0; j < 4; ++j) {
          const int s2 = 16 * t + 4 * g + j;
          const float a = a64 * (1.f / 32.f) * (float)s2;
          tc[t][j >> 1][j & 1] = __builtin_amdgcn_cosf(a);
          tsn[t][j >> 1][j & 1] = __builtin_amdgcn_sinf(a);
          uo[t][j] = 8192 + s2 * 256 + (((4 * wave + (r >> 2)) ^ (s2 & 15)) * 16) + (r & 3) * 4;
        }
    }
    unsigned vy = wave * 1024 + r * 64 + (g & 1) * 32 + (g >> 1) * 16;
#pragma unroll
    for (int ks = 0; ks < 4; ++ks) { asm volatile("" : "+v"(fre[ks]), "+v"(fim[ks]), "+v"(ro[ks])); }
#pragma unroll
    for (int t = 0; t < 2; ++t) {
      asm volatile("" : "+v"(fc[t]), "+v"(fs[t]), "+v"(fn[t]), "+v"(tc[t][0]), "+v"(tc[t][1]), "+v"(tsn[t][0]), "+v"(tsn[t][1]));
      asm volatile("" : "+v"(uo[t][0]), "+v"(uo[t][1]), "+v"(uo[t][2]), "+v"(uo[t][3]), "+v"(vy));
    }
    SCHED();
    TS(6);
    FftC T = {fre, fim, fc, fs, fn, tc, tsn, uo, ro, vy};
    if (fu0 < 34 && team == 0) fft_run<2>(ws, e, sm, T, fu0, wave8, lane TS_PASS);
    else fft_run<1>(ws, e, sm, T, fu0, wave8, lane TS_PASS);
  } else if (iskv) {
    const int ks = (v - 88) >> 1, cp = 2 * ((v - 88) & 1) + team;
    const int hf = (32 * ks) / 20, e0 = 20 * hf;
    const int q = r >> 2, p = lane & 3;
    const int hsw = ((q >> 1) & 1) | ((g & 1) << 1);
    const int L1 = (160 * cp) >> 7, L2 = (1120 - 160 * cp) >> 7;
    {
      const rsrc_t rwo = mk_rsrc(A.wo, 640 * 1280);
      const int line = (wave < 2 ? L1 : L2) + (wave & 1), row = lane >> 3;
#pragma unroll
      for (int kb = 0; kb < 8; ++kb) {
        const int pair = ((lane & 7) >> 1) ^ (((row >> 1) & 1) | ((kb & 1) << 1));
        dma16_asm(rwo, smu + (wave * 8 + kb) * 1024, min(e0 + 8 * kb + row, 639) * 1280 + line * 128 + pair * 32 + (lane & 1) * 16, 0);
      }
    }
    lptr ab = sm0 + 32768;
    if (threadIdx.x < 288) *(LDS_AS u32x4*)(ab + threadIdx.x * 16) = (u32x4){0u, 0u, 0u, 0u};
    const int t5 = min((int)threadIdx.x >> 1, 159), kl = t5 / 5, qd = t5 - kl * 5;
    const int k = 32 * ks + kl, h = k / 20, d = k - 20 * h;
    unsigned ko = h * 1920 + d * 96 + qd * 16 + (threadIdx.x & 1) * 8 * 61440, ao = kl * 144 + (20 * (h - hf) + 4 * qd) * 2;
    unsigned bo[3];
#pragma unroll
    for (int n = 0; n < 3; ++n) {
      const int tl = min(wave + 4 * n, 9);
      const int ctl = tl >= 5 ? 1 : 0, nt = tl - 5 * ctl, ctg = 2 * cp + ctl;
      const int i = 16 * nt + 4 * p;
      const int byte = 2 * (i < 40 ? 40 * ctg + i : 560 - 40 * ctg + i);
      const int pl = i < 40 ? (byte >> 7) - L1 : 2 + (byte >> 7) - L2;
      bo[n] = (pl * 8 + g) * 1024 + q * 128 + ((((byte >> 5) & 3) ^ hsw) * 32) + (byte & 31);
      asm volatile("" : "+v"(bo[n]));
    }
    asm volatile("" : "+v"(ko), "+v"(ao));
    WAIT_V(0);
    WAIT_L0();
    TS(6);
    await(ws, 1, e, 2, wave8, lane);
    TS(7);
    f32x4 pv[8];
    const rsrc_t rk = mk_rsrc(ws + WS_KVP, wave8 < 5 ? 983040 : 0);
#pragma unroll
    for (int i = 0; i < 8; ++i) pv[i] = __builtin_bit_cast(f32x4, LD16(rk, ko, i * 61440));
    SCHED();
    bf16x8 bw[3][2];
#pragma unroll
    for (int n = 0; n < 3; ++n)
#pragma unroll
      for (int s = 0; s < 2; ++s) {
        const s16x4 lo = TRREAD(sm + bo[n] + s * 4096), hi = TRREAD(sm + bo[n] + s * 4096 + 512);
        bw[n][s] = __builtin_shufflevector(lo, hi, 0, 1, 2, 3, 4, 5, 6, 7);
      }
    SCHED();
    f32x4 sum = ((pv[0] + pv[1]) + (pv[2] + pv[3])) + ((pv[4] + pv[5]) + (pv[6] + pv[7]));
#pragma unroll
    for (int j = 0; j < 4; ++j) sum[j] += __uint_as_float(__builtin_amdgcn_update_dpp(0u, __float_as_uint(sum[j]), 0xB1, 0xf, 0xf, false));
    if (threadIdx.x < 320 && !(threadIdx.x & 1)) *(LDS_AS u32x2*)(ab + ao) = (u32x2){pk2(sum[0], sum[1]), pk2(sum[2], sum[3])};
    WAIT_L0();
    BAR();
    TS(8);
    bf16x8 a[2][2];
#pragma unroll
    for (int t = 0; t < 2; ++t)
#pragma unroll
      for (int s = 0; s < 2; ++s) a[t][s] = *(LDS_AS bf16x8*)(ab + (8 * (r >> 2) + (r & 3) + 4 * t) * 144 + s * 64 + g * 16);
    const rsrc_t rm = mk_rsrc(ws + WS_M + (size_t)(10 * cp) * 20480 + ks * 1024, 9 * 20480 + 1024);
#pragma unroll
    for (int n = 0; n < 3; ++n) {
      f32x4 d1 = MFMA16(a[0][0], bw[n][0], z4), d2 = MFMA16(a[1][0], bw[n][0], z4);
      d1 = MFMA16(a[0][1], bw[n][1], d1); d2 = MFMA16(a[1][1], bw[n][1], d2);
      ST16(__builtin_bit_cast(u32x4, pack8(d1, d2)), rm, lane * 16, (wave + 4 * n) * 20480);
    }
  } else if (ct1 < 4) {
    const int t8 = threadIdx.x, wm = wave8 >> 1, wn = wave8 & 1;
    const int gb = rt1 * 4 + ct1;
      const rsrc_t rx = mk_rsrc(A.x + (size_t)gb * 32 * 640, 32 * 1280), rg = mk_rsrc(A.gw, 1280);
    u32x4 gx[5], gg[5];
#pragma unroll
      for (int i = 0; i < 5; ++i) {
        gx[i] = __builtin_amdgcn_raw_buffer_load_b128(rx, t8 * 80 + i * 16, 0, 0);
        gg[i] = __builtin_amdgcn_raw_buffer_load_b128(rg, (t8 & 15) * 80 + i * 16, 0, 0);
      }
    u32x2 pk[2][5];
    unsigned zm[5];
#pragma unroll
    for (int nt = 0; nt < 5; ++nt) {
#pragma unroll
      for (int mt = 0; mt < 2; ++mt) {
        pk[mt][nt] = (u32x2){pk2(pq[mt][nt][0], pq[mt][nt][1]), pk2(pq[mt][nt][2], pq[mt][nt][3])};
        asm volatile("" : "+v"(pk[mt][nt]));
      }
      zm[nt] = ((4 * nt + g) / 5 == (r & 3)) ? 0xffffffffu : 0u;
      asm volatile("" : "+v"(zm[nt]));
    }
    const rsrc_t rz = mk_rsrc(ws + WS_ZP + ct1 * 640, 40960);
    const rsrc_t rp = mk_rsrc(ws + WS_P + (size_t)(8 * rt1 + 2 * wm) * 20480 + (20 * ct1 + 10 * wn) * 256, 2 * 20480);
    const unsigned vo = (g & 1) * 20480 + (g >> 1) * 256 + r * 16;
    SCHED();
    TS(6);
    await(ws, 1, e, 2, wave8, lane);
    TS(7);
    if (wave8 < 3) {
      float zv[16];
#pragma unroll
      for (int i = 0; i < 16; ++i) zv[i] = __uint_as_float(__builtin_amdgcn_raw_buffer_load_b32(rz, min(t8, 159) * 4, i * 2560, LD_AUX));
#pragma unroll
      for (int i = 0; i < 8; ++i) zv[i] += zv[i + 8];
      const float zs = ((zv[0] + zv[1]) + (zv[2] + zv[3])) + ((zv[4] + zv[5]) + (zv[6] + zv[7]));
      *(LDS_AS u16*)(sm0 + t8 * 2) = (u16)pk2(zs, zs);
    }
    WAIT_L0();
    BAR();
    TS(8);
    u32x2 zq[6];
#pragma unroll
    for (int nt = 0; nt < 5; ++nt) { zq[nt] = *(LDS_AS u32x2*)(sm0 + wn * 160 + nt * 32 + g * 8); zq[nt][0] &= zm[nt]; zq[nt][1] &= zm[nt]; }
    zq[5] = (u32x2){0u, 0u};
    u32x2 po[2][5];
#pragma unroll
    for (int mt = 0; mt < 2; ++mt) {
      f32x4 den = {kEps, kEps, kEps, kEps};
#pragma unroll
      for (int m = 0; m < 3; ++m) {
        const u32x4 za = {zq[2 * m][0], zq[2 * m][1], zq[2 * m + 1][0], zq[2 * m + 1][1]};
        const u32x2 p1 = m < 2 ? pk[mt][2 * m + 1] : (u32x2){0u, 0u};
        const u32x4 pb = {pk[mt][2 * m][0], pk[mt][2 * m][1], p1[0], p1[1]};
        den = MFMA16(BC(za), BC(pb), den);
      }
      f32x4 inv;
#pragma unroll
      for (int j = 0; j < 4; ++j) inv[j] = __builtin_amdgcn_rcpf(den[j]);
#pragma unroll
      for (int nt = 0; nt < 5; ++nt) {
        const float iv = nt == 0 ? inv[0] : nt == 4 ? inv[3] : (g < nt ? inv[nt - 1] : inv[nt]);
        const f32x4 w = pq[mt][nt] * iv;
        po[mt][nt] = (u32x2){pk2(w[0], w[1]), pk2(w[2], w[3])};
      }
    }
#pragma unroll
    for (int nt = 0; nt < 5; ++nt) ST16(widen(po[0][nt], po[1][nt]), rp, vo + nt * 512, 0);
    SCHED();
    {
      float s0 = 0.f, s1 = 0.f;
#pragma unroll
      for (int i = 0; i < 5; ++i) { s0 = dot2(gx[i][0], gg[i][0], s0); s1 = dot2(gx[i][1], gg[i][1], s1); s0 = dot2(gx[i][2], gg[i][2], s0); s1 = dot2(gx[i][3], gg[i][3], s1); }
      float s = s0 + s1;
#pragma unroll
      for (int o = 8; o > 0; o >>= 1) s += __shfl_xor(s, o);
      if ((t8 & 15) == 0) __builtin_amdgcn_raw_buffer_store_b32(__float_as_uint(1.f / (1.f + __expf(-s))), mk_rsrc(ws + WS_G, 8192), (gb * 32 + (t8 >> 4)) * 4, 0, 16);
    }
  }
  TS(9);
}

constexpr int LDS_ALL = K1_LDS;
static_assert(2 * P3_HALF + 256 <= LDS_ALL, "lds");

template <bool F> DI void phase3(const Args& A, lptr sm, unsigned e TS_ARG) {
  char* ws = A.ws;
  const int tid = threadIdx.x, lane = tid & 63;
  const int wave = __builtin_amdgcn_readfirstlane(tid >> 6);
  const int b = blockIdx.x, xcd = b & 7, idx = b >> 3;
  const int rt = (xcd & 3) * 8 + (idx & 7);
  const int ct = (xcd >> 2) * 4 + (idx >> 3);
  const int r = lane & 15, g = lane >> 4;
  LDS_AS float* red = (LDS_AS float*)sm;
  constexpr int RA = 64 * 84, RE = 64 * 52;
  if (!F) { p3_load_g(sm, wave, lane); if (wave >= 4) WAIT_V(0); }
  TS(11);
  if (wave < 2) {
    const rsrc_t ra = mk_rsrc(ws + WS_P + (size_t)(4 * rt) * 20480 + wave * 10240, 4 * 20480);
    const rsrc_t rb = mk_rsrc(ws + WS_M + (size_t)(5 * ct) * 20480 + wave * 10240, 5 * 20480);
    const unsigned vo = lane * 16;
    f32x4 acc[4][5];
#pragma unroll
    for (int mt = 0; mt < 4; ++mt)
#pragma unroll
      for (int nt = 0; nt < 5; ++nt) acc[mt][nt] = (f32x4){0.f, 0.f, 0.f, 0.f};
    u32x4 fa[10][4], fb[10][5];
    constexpr int PF = 3;
    auto ld = [&](int ks) {
#pragma unroll
      for (int nt = 0; nt < 5; ++nt) fb[ks][nt] = LD16(rb, vo + (ks & 3) * 1024, nt * 20480 + (ks >> 2) * 4096);
#pragma unroll
      for (int mt = 0; mt < 4; ++mt) fa[ks][mt] = LD16(ra, vo + (ks & 3) * 1024, mt * 20480 + (ks >> 2) * 4096);
    };
#pragma unroll
    for (int ks = 0; ks < PF; ++ks) { ld(ks); SCHED(); }
    if (!F) { WAIT_V(27); BAR(); }
#pragma unroll
    for (int ks = 0; ks < 10; ++ks) {
#pragma unroll
      for (int mt = 0; mt < 4; ++mt)
#pragma unroll
        for (int nt = 0; nt < 5; ++nt) acc[mt][nt] = MFMA16(BC(fb[ks][nt]), BC(fa[ks][mt]), acc[mt][nt]);
      SCHED();
      if (ks + PF < 10) ld(ks + PF);
      SCHED();
    }
    TS(12);
    BAR();
#pragma unroll
    for (int mt = 0; mt < 4; ++mt)
#pragma unroll
      for (int nt = 0; nt < 5; ++nt) *(LDS_AS f32x4*)(red + wave * RA + (16 * mt + r) * 84 + 16 * nt + 4 * g) = acc[mt][nt];
  } else if (wave < 4) {
    const int od = wave - 2;
    lptr yb = sm + od * P3_HALF;
    const unsigned ybu = (unsigned)(size_t)yb;
    const rsrc_t ry = mk_rsrc(ws + WS_Y + (size_t)od * 320 * 4096 + rt * 128, 320 * 4096);
    const int row = lane >> 3;
    const unsigned vy0 = row * 4096 + ((((lane & 7) >> 1) ^ ((row >> 1) & 1)) * 32) + (lane & 1) * 16;
    const unsigned vy1 = vy0 ^ 64;
    auto issue = [&](int ks) {
#pragma unroll
      for (int i = 0; i < 4; ++i) dma16_sc1(ry, ybu + (ks * 4 + i) * 1024, (i & 1) ? vy1 : vy0, (ks * 4 + i) * 32768);
    };
    issue(0); issue(1); issue(2);
    if (!F) { WAIT_V(12); BAR(); }
    const int q = r >> 2, p = lane & 3;
    const int hsw = ((q >> 1) & 1) | ((g & 1) << 1);
    unsigned yo[4];
#pragma unroll
    for (int mt = 0; mt < 4; ++mt) yo[mt] = g * 1024 + q * 128 + ((mt ^ hsw) * 32) + p * 8;
    lptr gb = yb + 40960 + lane * 16;
    f32x4 acc[4][3];
#pragma unroll
    for (int mt = 0; mt < 4; ++mt)
#pragma unroll
      for (int nt = 0; nt < 3; ++nt) acc[mt][nt] = (f32x4){0.f, 0.f, 0.f, 0.f};
#define K3_STEP(ks, n)                                                                                         \
    {                                                                                                          \
      WAIT_V(n);                                                                                               \
      SCHED();                                                                                                 \
      bf16x8 fy[4], fg[3];                                                                                     \
      _Pragma("unroll") for (int nt = 0; nt < 3; ++nt) fg[nt] = *(LDS_AS bf16x8*)(gb + (ks * 3 + nt) * 1024);  \
      _Pragma("unroll") for (int mt = 0; mt < 4; ++mt) {                                                       \
        const s16x4 lo = TRREAD(yb + yo[mt] + ks * 4096), hi = TRREAD(yb + yo[mt] + ks * 4096 + 512);          \
        fy[mt] = __builtin_shufflevector(lo, hi, 0, 1, 2, 3, 4, 5, 6, 7);                                      \
      }                                                                                                        \
      SCHED();                                                                                                 \
      if (ks + 3 < 10) issue(ks + 3);                                                                          \
      SCHED();                                                                                                 \
      _Pragma("unroll") for (int mt = 0; mt < 4; ++mt)                                                         \
        _Pragma("unroll") for (int nt = 0; nt < 3; ++nt) acc[mt][nt] = MFMA16(fg[nt], fy[mt], acc[mt][nt]);   \
      SCHED();                                                                                                 \
    }
    K3_STEP(0, 8) K3_STEP(1, 8) K3_STEP(2, 8) K3_STEP(3, 8) K3_STEP(4, 8)
    K3_STEP(5, 8) K3_STEP(6, 8) K3_STEP(7, 8) K3_STEP(8, 4) K3_STEP(9, 0)
    TS(12);
    BAR();
#pragma unroll
    for (int mt = 0; mt < 4; ++mt)
#pragma unroll
      for (int nt = 0; nt < 3; ++nt) *(LDS_AS f32x4*)(red + 2 * RA + od * RE + (16 * mt + r) * 52 + 16 * nt + 4 * g) = acc[mt][nt];
  } else {
    if (!F) BAR();
    if (wave == 4) red[2 * P3_HALF / 4 + lane] = __uint_as_float(__builtin_amdgcn_raw_buffer_load_b32(mk_rsrc(ws + WS_G + rt * 256, 256), lane * 4, 0, LD_AUX));
    WAIT_L0();
    BAR();
  }
  const rsrc_t ro = mk_rsrc(A.out + (size_t)(64 * rt) * 640, 64 * 1280);
  unsigned oa[2], oe[2], oq[2], og[2];
  float gq[2];
#pragma unroll
  for (int i = 0; i < 2; ++i) {
    const int it = i ? 512 + wave * 16 + (lane & 15) : tid;
    const int row = it / 10, gr = it - row * 10;
    oa[i] = (row * 84 + 8 * gr) * 4;
    oe[i] = (2 * RA + row * 52) * 4;
    oq[i] = oe[i] + (gr < 5 ? 8 * gr : 72 - 8 * gr) * 4;
    og[i] = (i && lane >= 16) ? 0x7fffffffu : row * 1280 + (gr < 5 ? 80 * ct : 1120 - 80 * ct) + 16 * gr;
    gq[i] = red[2 * P3_HALF / 4 + row];
    oa[i] |= (gr < 5) ? 0u : 0x80000000u;
    asm volatile("" : "+v"(oa[i]), "+v"(oe[i]), "+v"(oq[i]), "+v"(og[i]), "+v"(gq[i]));
  }
  WAIT_L0();
  BAR();
  TS(13);
#pragma unroll
  for (int i = 0; i < 2; ++i) {
    const bool lo = (int)oa[i] >= 0;
    lptr pa = sm + (oa[i] & 0x7fffffffu), pq = sm + oq[i];
    const f32x4 a0 = *(LDS_AS f32x4*)pa + *(LDS_AS f32x4*)(pa + RA * 4);
    const f32x4 a1 = *(LDS_AS f32x4*)(pa + 16) + *(LDS_AS f32x4*)(pa + RA * 4 + 16);
    const float yn = *(LDS_AS float*)(sm + oe[i] + (RE + 41) * 4);
    const f32x4 e0 = *(LDS_AS f32x4*)pq, e1 = *(LDS_AS f32x4*)(pq + 16), o0 = *(LDS_AS f32x4*)(pq + RE * 4), o1 = *(LDS_AS f32x4*)(pq + RE * 4 + 16);
    const float p8 = *(LDS_AS float*)(pq + 32) + *(LDS_AS float*)(pq + RE * 4 + 32);
    const f32x4 m0 = e0 - o0, m1 = e1 - o1, p0 = e0 + o0, p1 = e1 + o1;
    const f32x4 yy = {yn, -yn, yn, -yn};
    const f32x4 b0 = (lo ? m0 : (f32x4){p8, p1[3], p1[2], p1[1]}) + yy;
    const f32x4 b1 = (lo ? m1 : (f32x4){p1[0], p0[3], p0[2], p0[1]}) + yy;
    const f32x4 r0 = b0 + gq[i] * (a0 - b0), r1 = b1 + gq[i] * (a1 - b1);
    __builtin_amdgcn_raw_buffer_store_b128(__builtin_bit_cast(u32x4, pack8(r0, r1)), ro, og[i], 0, OUT_AUX);
  }
  TS(14);
}

#define TS_END()

__global__ __launch_bounds__(512, 2) void fused_kernel(Args A) {
  extern __shared__ __attribute__((aligned(16))) char smem[];
  lptr sm = (lptr)smem;
  TS_DECL;
  TS(0);
  const rsrc_t re = mk_rsrc(A.ws + WS_EPOCH, 4);
  const unsigned e = *(const __attribute__((address_space(4))) unsigned*)(A.ws + WS_EPOCH);
  f32x4 pq[2][5];
  phase1(A, sm, pq, e TS_PASS);
  phase2<true>(A, sm, e, pq TS_PASS);
  publish(A.ws, 2, e);
  p3_load_g(sm, __builtin_amdgcn_readfirstlane(threadIdx.x >> 6), threadIdx.x & 63);
  if (threadIdx.x < 448) WAIT_V(0);
  TS(10);
  await(A.ws, 2, e, 7, __builtin_amdgcn_readfirstlane(threadIdx.x >> 6), threadIdx.x & 63);
  phase3<true>(A, sm, e TS_PASS);
  if (blockIdx.x == 0 && threadIdx.x == 0) __builtin_amdgcn_raw_buffer_store_b32(e + 1u, re, 0, 0, 16);
  TS_END();
}

extern "C" void kernel_launch(void* const* d_in, const int* in_sizes, int n_in, void* d_out, int out_size, void* d_ws,
                              size_t ws_size, hipStream_t stream) {
  Args A;
  A.x = (const u16*)d_in[0]; A.wqkv = (const u16*)d_in[1]; A.wo = (const u16*)d_in[2];
  A.wr = (const u16*)d_in[4]; A.wi = (const u16*)d_in[5]; A.gw = (const u16*)d_in[6];
  A.ws = (char*)d_ws; A.out = (u16*)d_out;
  (void)hipFuncSetAttribute((const void*)fused_kernel, hipFuncAttributeMaxDynamicSharedMemorySize, LDS_ALL);
  fused_kernel<<<256, 512, LDS_ALL, stream>>>(A);
}
```

```cpp
#include <hip/hip_runtime.h>
#include <hip/hip_bf16.h>
#include <utility>

typedef unsigned short u16;
typedef __attribute__((ext_vector_type(8))) short bf16x8;
typedef __attribute__((ext_vector_type(4))) short s16x4;
typedef __attribute__((ext_vector_type(4))) float f32x4;
typedef __attribute__((ext_vector_type(2))) float f32x2;
typedef __attribute__((ext_vector_type(2))) __bf16 bf16x2_t;
typedef __attribute__((ext_vector_type(4))) unsigned u32x4;
typedef __attribute__((ext_vector_type(2))) unsigned u32x2;
#define LDS_AS __attribute__((address_space(3)))
#define DI __device__ __forceinline__
#define MFMA16(a, b, c) __builtin_amdgcn_mfma_f32_16x16x32_bf16((a), (b), (c), 0, 0, 0)
typedef LDS_AS char* lptr;
typedef __amdgpu_buffer_rsrc_t rsrc_t;

#define ST_AUX 16
#define OUT_AUX 0
#define K1_ROT 5
#define FUSED 1
#define POLL_SLEEP 2
#define G_EARLY 0
#define CODE_PF 1
#define LD_AUX 16

constexpr double kPi = 3.14159265358979323846;
constexpr double ccos_s(double x) { double x2 = x * x, t = 1, s = 1; for (int i = 1; i < 12; ++i) { t *= -x2 / ((2 * i - 1) * (2 * i)); s += t; } return s; }
constexpr double csin_s(double x) { double x2 = x * x, t = x, s = x; for (int i = 1; i < 12; ++i) { t *= -x2 / ((2 * i) * (2 * i + 1)); s += t; } return s; }
constexpr double cos640(int m) {
  m %= 640; if (m > 320) m = 640 - m;
  bool neg = false; if (m > 160) { m = 320 - m; neg = true; }
  double v = (m <= 80) ? ccos_s(2 * kPi * m / 640) : csin_s(2 * kPi * (160 - m) / 640);
  return neg ? -v : v;
}
constexpr u16 to_bf16c(double v) { float f = (float)v; unsigned u = __builtin_bit_cast(unsigned, f); u += 0x7fffu + ((u >> 16) & 1u); return (u16)(u >> 16); }
struct C640 { u16 v[640]; };
constexpr C640 make_c640() { C640 c{}; for (int i = 0; i < 640; ++i) c.v[i] = to_bf16c(cos640(i)); return c; }
constexpr C640 COS640 = make_c640();

struct alignas(128) FRow { u16 v[10 * 512]; };
constexpr FRow make_frow(int id) {
  FRow t{};
  const int cq = id / 10, st = id % 10;
  for (int par = 0; par < 2; ++par) for (int nt = 0; nt < 5; ++nt) for (int l = 0; l < 64; ++l) {
    const int kk = 80 * cq + 16 * nt + (l & 15);
    for (int j = 0; j < 8; ++j) {
      const int d = 64 * st + 32 * (j >> 2) + 8 * (l >> 4) + 2 * (j & 3) + par;
      const int m = (kk == 1) ? (d * 160 + 160 * par) % 640 : (d * (kk >> 1) + 160 * (kk & 1)) % 640;
      t.v[((par * 5 + nt) * 64 + l) * 8 + j] = COS640.v[m];
    }
  }
  return t;
}
template <int I> constexpr FRow FROW = make_frow(I);
struct alignas(128) FTab { FRow r[40]; };
template <int... I> constexpr FTab make_ftab(std::integer_sequence<int, I...>) { return FTab{{FROW<I>...}}; }
__device__ const FTab FT = make_ftab(std::make_integer_sequence<int, 40>{});

struct alignas(128) GRow { u16 v[10 * 3 * 512]; };
constexpr GRow make_grow(int id) {
  GRow t{};
  const int ct = id >> 1, od = id & 1;
  for (int ks = 0; ks < 10; ++ks) for (int nt = 0; nt < 3; ++nt) for (int l = 0; l < 64; ++l) {
    const int ci = 16 * nt + (l & 15), d = 40 * ct + ci;
    for (int j = 0; j < 8; ++j) {
      const int f = 32 * ks + 8 * (l >> 4) + j;
      t.v[((ks * 3 + nt) * 64 + l) * 8 + j] = (ci <= 40) ? COS640.v[(f * d + (od ? 160 : 0)) % 640] : (u16)((od && ci == 41 && f == 0) ? 0x3f80 : 0);
    }
  }
  return t;
}
template <int I> constexpr GRow GROW = make_grow(I);
struct alignas(128) GTab { GRow r[16]; };
template <int... I> constexpr GTab make_gtab(std::integer_sequence<int, I...>) { return GTab{{GROW<I>...}}; }
__device__ const GTab GT = make_gtab(std::make_integer_sequence<int, 16>{});

constexpr size_t WS_P   = 0;
constexpr size_t WS_Y   = WS_P + 2621440;
constexpr size_t WS_X1  = WS_Y + 2621440;
constexpr size_t WS_WT  = WS_X1 + 321 * 8192;
constexpr size_t WS_KVP = WS_WT + 352 * 8192;
constexpr size_t WS_M   = WS_KVP + 983040;
constexpr size_t WS_G   = WS_M + 819200;
constexpr size_t WS_ZP  = WS_G + 8192;
constexpr size_t WS_SYN = WS_ZP + 40960;

DI rsrc_t mk_rsrc(const void* p, unsigned bytes) { return __builtin_amdgcn_make_buffer_rsrc((void*)p, 0, bytes, 0x00020000); }
DI unsigned pk2(float a, float b) { f32x2 v = {a, b}; return __builtin_bit_cast(unsigned, __builtin_convertvector(v, bf16x2_t)); }
DI float bf_lo(unsigned u) { return __uint_as_float(u << 16); }
DI float bf_hi(unsigned u) { return __uint_as_float(u & 0xffff0000u); }
DI float dot2(unsigned a, unsigned b, float c) { return __builtin_amdgcn_fdot2_f32_bf16(__builtin_bit_cast(bf16x2_t, a), __builtin_bit_cast(bf16x2_t, b), c, false); }
DI float rbf(float a) { return bf_lo(pk2(a, a)); }
constexpr float kEps = 1e-6f, kL2E = 1.4426950408889634f;
DI float phi_f(float a) {
  float e;
  const float u = __builtin_fmaf(a, kL2E, -kEps * kL2E);
  asm("v_exp_f32_e64 %0, %1 clamp" : "=v"(e) : "v"(u));
  return __builtin_amdgcn_fmed3f(a, kEps, __builtin_inff()) + e;
}
DI bf16x8 pack8(const f32x4& a, const f32x4& b) {
  u32x4 p = {pk2(a[0], a[1]), pk2(a[2], a[3]), pk2(b[0], b[1]), pk2(b[2], b[3])};
  return __builtin_bit_cast(bf16x8, p);
}
DI u32x4 widen(u32x2 a, u32x2 b) {
  auto x = __builtin_amdgcn_permlane16_swap(a[0], b[0], false, false);
  auto y = __builtin_amdgcn_permlane16_swap(a[1], b[1], false, false);
  return (u32x4){x[0], y[0], x[1], y[1]};
}
#define WAIT_V_(n) asm volatile("s_waitcnt vmcnt(" #n ")" ::: "memory")
#define WAIT_V(n) WAIT_V_(n)
#define BC(v) __builtin_bit_cast(bf16x8, (v))
#define WAIT_L0() __builtin_amdgcn_s_waitcnt(0xC07F)
#define BAR() __builtin_amdgcn_s_barrier()
#define SCHED() __builtin_amdgcn_sched_barrier(0)
#define DMA16(rs, dst, vo, so) __builtin_amdgcn_raw_ptr_buffer_load_lds((rs), (LDS_AS void*)(dst), 16, (vo), (so), 0, 0)
#define TRREAD(p) __builtin_amdgcn_ds_read_tr16_b64_v4i16((LDS_AS s16x4*)(p))
#define ST16(v, rs, vo, so) __builtin_amdgcn_raw_buffer_store_b128((v), (rs), (vo), (so), ST_AUX)

#define TS_DECL
#define TS(i)
#define TC(i)
#define TS_FLUSH(kern)
#define TS_FLUSH2()
#define TS_ARG
#define TS_PASS

constexpr int K1_STAGE = 40960;
constexpr int K1_WF = 3 * K1_STAGE;
constexpr int K1_JUNK = K1_WF + 32768;
constexpr int K1_LDS = K1_JUNK + 2048;
#define K1_PF 1

struct Frags { bf16x8 a[2][2]; bf16x8 b[2][5]; };
struct K1Ctx { rsrc_t ra, rb; unsigned va[2], vb[3], ao[3][2], bo[3][5], ldsa, ldsb; int st, rs, wave; };

struct Half { s16x4 lo, hi; };
template <int I0, int I1> DI void k1_read(Frags& F, lptr sm, const unsigned (&ao)[2], const unsigned (&bo)[5]) {
#pragma unroll
  for (int i = I0; i < I1; ++i) {
    const int k = i / 7, j = i % 7;
    if (j < 2) F.a[k][j] = *(LDS_AS bf16x8*)(sm + ao[k] + j * 2048);
    else {
      const s16x4 lo = TRREAD(sm + bo[j - 2] + k * 12288), hi = TRREAD(sm + bo[j - 2] + k * 12288 + 512);
      F.b[k][j - 2] = __builtin_shufflevector(lo, hi, 0, 1, 2, 3, 4, 5, 6, 7);
    }
  }
}
DI void dma16_asm(rsrc_t rs, unsigned lds, unsigned vo, int so) {
  asm volatile("s_mov_b32 m0, %2\n\ts_nop 0\n\tbuffer_load_dwordx4 %0, %1, %3 offen lds" :: "v"(vo), "s"(rs), "s"(lds), "s"(so));
}
DI void pf_asm(rsrc_t rs, unsigned lds, unsigned vo, int so) {
  asm volatile("s_mov_b32 m0, %2\n\ts_nop 0\n\tbuffer_load_dword %0, %1, %3 offen lds" :: "v"(vo), "s"(rs), "s"(lds), "s"(so));
}
template <int BUF, int I> DI void k1_issue1(const K1Ctx& c) {
  if (I < 2) dma16_asm(c.ra, c.ldsa + BUF * K1_STAGE + I * 1024, c.va[I < 2 ? I : 0], c.st * 128);
  else dma16_asm(c.rb, c.ldsb + BUF * K1_STAGE + (I - 2) * 1024, c.vb[I < 2 ? 0 : I - 2], c.st * 64 * c.rs);
}
template <int BUF> DI void k1_issue(K1Ctx& c) {
  k1_issue1<BUF, 0>(c); k1_issue1<BUF, 1>(c); k1_issue1<BUF, 2>(c); k1_issue1<BUF, 3>(c); k1_issue1<BUF, 4>(c);
  c.st = (c.st == 9) ? 0 : c.st + 1;
}
template <bool SWAP, int I0, int I1> DI void k1_mma(f32x4 (&acc)[2][5], const Frags& F) {
#pragma unroll
  for (int i = I0; i < I1; ++i) {
    const int k = i / 10, mt = (i / 5) & 1, nt = i % 5;
    acc[mt][nt] = SWAP ? MFMA16(F.b[k][nt], F.a[k][mt], acc[mt][nt]) : MFMA16(F.a[k][mt], F.b[k][nt], acc[mt][nt]);
  }
}
template <bool SWAP, int N, int SEG> DI void k1_seg(f32x4 (&acc)[2][5], Frags& Fn, const Frags& Fp, lptr sm, K1Ctx& c) {
  constexpr int R0[6] = {0, 4, 7, 10, 12, 14};
  k1_read<R0[SEG], R0[SEG + 1]>(Fn, sm, c.ao[N % 3], c.bo[N % 3]);
  if (N > 0) {
    k1_mma<SWAP, 4 * SEG, 4 * SEG + 4>(acc, Fp);
    if (SEG < 2) {
      __builtin_amdgcn_sched_group_barrier(0x008, 1, 0); __builtin_amdgcn_sched_group_barrier(0x100, 2, 0);
      __builtin_amdgcn_sched_group_barrier(0x008, 1, 0); __builtin_amdgcn_sched_group_barrier(0x100, 2, 0);
      __builtin_amdgcn_sched_group_barrier(0x008, 1, 0); __builtin_amdgcn_sched_group_barrier(0x100, 2, 0);
      __builtin_amdgcn_sched_group_barrier(0x008, 1, 0);
    } else {
#pragma unroll
      for (int i = 0; i < 4; ++i) { __builtin_amdgcn_sched_group_barrier(0x008, 1, 0); __builtin_amdgcn_sched_group_barrier(0x100, 2, 0); }
    }
  }
  SCHED();
  if (N + 2 <= 9) k1_issue1<(N + 2) % 3, SEG>(c);
  SCHED();
}
template <bool SWAP, int N> DI void k1_step(f32x4 (&acc)[2][5], Frags& Fn, const Frags& Fp, lptr sm, K1Ctx& c) {
  WAIT_L0();
  BAR();
  SCHED();
  k1_seg<SWAP, N, 0>(acc, Fn, Fp, sm, c);
  k1_seg<SWAP, N, 1>(acc, Fn, Fp, sm, c);
  k1_seg<SWAP, N, 2>(acc, Fn, Fp, sm, c);
  k1_seg<SWAP, N, 3>(acc, Fn, Fp, sm, c);
  k1_seg<SWAP, N, 4>(acc, Fn, Fp, sm, c);
  c.st = (c.st == 9) ? 0 : c.st + 1;
}

struct DFrags { u32x4 a[2][2]; bf16x8 b[2][3]; };
constexpr int KD_STAGE = 26624;
template <int BUF, int I> DI void kd_issue1(const K1Ctx& c) {
  if (I < 2) dma16_asm(c.ra, c.ldsa + BUF * KD_STAGE + I * 1024, c.va[I & 1], c.st * 128);
  else if (I == 2) dma16_asm(c.rb, c.ldsb + BUF * KD_STAGE, c.vb[0], c.st * 10240);
  else dma16_asm(c.rb, c.wave < 2 ? c.ldsb + BUF * KD_STAGE + 8192 : c.ldsa - 2048 * c.wave + K1_JUNK, c.vb[1], c.st * 10240);
}
template <int BUF> DI void kd_issue(K1Ctx& c) {
  kd_issue1<BUF, 0>(c); kd_issue1<BUF, 2>(c); kd_issue1<BUF, 1>(c); kd_issue1<BUF, 3>(c);
  c.st = (c.st == 9) ? 0 : c.st + 1;
}
template <int NT> DI void kd_mma(f32x4 (&acc)[2][2][3], const DFrags& F) {
#pragma unroll
  for (int mt = 0; mt < 2; ++mt) {
    u32x4 ev, od;
#pragma unroll
    for (int k = 0; k < 2; ++k)
#pragma unroll
      for (int i = 0; i < 2; ++i) {
        ev[2 * k + i] = __builtin_amdgcn_perm(F.a[mt][k][2 * i + 1], F.a[mt][k][2 * i], 0x05040100u);
        od[2 * k + i] = __builtin_amdgcn_perm(F.a[mt][k][2 * i + 1], F.a[mt][k][2 * i], 0x07060302u);
      }
#pragma unroll
    for (int nt = 0; nt < NT; ++nt) {
      acc[0][mt][nt] = MFMA16(F.b[0][nt], BC(ev), acc[0][mt][nt]);
      acc[1][mt][nt] = MFMA16(F.b[1][nt], BC(od), acc[1][mt][nt]);
    }
  }
}
template <int NT, int N> DI void kd_step(f32x4 (&acc)[2][2][3], DFrags& Fn, const DFrags& Fp, lptr sm, K1Ctx& c) {
  WAIT_L0();
  BAR();
  SCHED();
  if (N + 3 <= 9) kd_issue<(N + 3) % 4>(c);
  SCHED();
  constexpr int H = (N % 4) >> 1, O = (N & 1) * KD_STAGE;
#pragma unroll
  for (int mt = 0; mt < 2; ++mt)
#pragma unroll
    for (int k = 0; k < 2; ++k) Fn.a[mt][k] = *(LDS_AS u32x4*)(sm + c.ao[H][k] + O + mt * 2048);
#pragma unroll
  for (int par = 0; par < 2; ++par)
#pragma unroll
    for (int nt = 0; nt < NT; ++nt) Fn.b[par][nt] = *(LDS_AS bf16x8*)(sm + c.bo[H][0] + O + (par * 5 + nt) * 1024);
  if (N > 0) {
    kd_mma<NT>(acc, Fp);
    __builtin_amdgcn_sched_group_barrier(0x100, 2, 0);
    __builtin_amdgcn_sched_group_barrier(0x002, 8, 0);
#pragma unroll
    for (int i = 0; i < 2 * NT; ++i) { __builtin_amdgcn_sched_group_barrier(0x008, 1, 0); __builtin_amdgcn_sched_group_barrier(0x100, 1, 0); __builtin_amdgcn_sched_group_barrier(0x002, 2, 0); }
  }
  SCHED();
}

DI u32x2 make_tag(unsigned e) { return (u32x2){e + 1u, (e + 1u) * 0x9E3779B9u ^ 0x5bd1e995u}; }
DI int flag_slot(int b) { return ((b & 7) >> 1) * 64 + (b & 1) * 32 + (b >> 3); }
constexpr size_t WS_EPOCH = WS_SYN + 3 * 65536;
DI void flag_store(char* ws, int which, unsigned e, int lane) {
  __builtin_amdgcn_raw_buffer_store_b64(make_tag(e), mk_rsrc(ws + WS_SYN + which * 65536, 65536), lane * 2048 + flag_slot(blockIdx.x) * 8, 0, 16);
}
template <int NT, class FL, class FS> DI void kd_main(char* ws, lptr sm, K1Ctx& c, int rt, int cq, unsigned e, int lane, FL filt_load, FS filt_store TS_ARG) {
  const int wm = c.wave & 3, r = lane & 15, g = lane >> 4;
  constexpr int T0 = NT == 3 ? 0 : 3;
  DFrags D0, D1;
  f32x4 acc[2][2][3];
#pragma unroll
  for (int u = 0; u < 2; ++u) {
    c.ao[u][0] = 2 * u * KD_STAGE + (32 * wm + r) * 128 + ((g ^ (r >> 1)) * 16);
    c.ao[u][1] = 2 * u * KD_STAGE + (32 * wm + r) * 128 + (((4 + g) ^ (r >> 1)) * 16);
    c.bo[u][0] = 2 * u * KD_STAGE + 16384 + T0 * 1024 + lane * 16;
    asm volatile("" : "+v"(c.ao[u][0]), "+v"(c.ao[u][1]), "+v"(c.bo[u][0]));
  }
#pragma unroll
  for (int i = 0; i < 4 * NT; ++i) { acc[i / (2 * NT)][(i / NT) & 1][i % NT] = (f32x4){0.f, 0.f, 0.f, 0.f}; asm volatile("" : "+v"(acc[i / (2 * NT)][(i / NT) & 1][i % NT])); }
  TS(1);
  WAIT_V(8); kd_step<NT, 0>(acc, D0, D1, sm, c);
  TS(2);
  WAIT_V(8); kd_step<NT, 1>(acc, D1, D0, sm, c);
  filt_load();
  WAIT_V(12); kd_step<NT, 2>(acc, D0, D1, sm, c);
  WAIT_V(12); kd_step<NT, 3>(acc, D1, D0, sm, c);
  WAIT_V(12); kd_step<NT, 4>(acc, D0, D1, sm, c);
  WAIT_V(8); kd_step<NT, 5>(acc, D1, D0, sm, c);
  WAIT_V(8); kd_step<NT, 6>(acc, D0, D1, sm, c);
  filt_store();
  WAIT_V(11); kd_step<NT, 7>(acc, D1, D0, sm, c);
  WAIT_V(7); kd_step<NT, 8>(acc, D0, D1, sm, c);
  WAIT_V(3); kd_step<NT, 9>(acc, D1, D0, sm, c);
  kd_mma<NT>(acc, D1);
  TS(3);
  const rsrc_t rx = mk_rsrc(ws + WS_X1 + (wm >> 1) * 4096 + (rt >> 2) * 1024 + (rt & 3) * 256 + (wm & 1) * 128, 321 * 8192);
  const bool up = r >= 8;
  const int fb = 40 * cq + 8 * T0 + 2 * g;
  const bool sp = (fb == 0);
#pragma unroll
  for (int nt = 0; nt < NT; ++nt) {
    unsigned it[4][2];
#pragma unroll
    for (int mt = 0; mt < 2; ++mt) {
      const f32x4 E = acc[0][mt][nt], O = acc[1][mt][nt], P = E + O, M = E - O;
      it[0][mt] = pk2(P[0], P[1]); it[1][mt] = pk2(P[2], P[3]); it[2][mt] = pk2(M[0], -M[1]); it[3][mt] = pk2(M[2], -M[3]);
      if (NT == 3 && nt == 0) {
        it[0][mt] = sp ? (it[0][mt] & 0xffffu) : it[0][mt]; it[2][mt] = sp ? (it[2][mt] & 0xffffu) : it[2][mt];
        __builtin_amdgcn_raw_buffer_store_b32(pk2(E[1], O[1]), rx, sp ? 160 * 8192 + (r & 7) * 16 + (r >> 3) * 8 + mt * 4 : 0x7fffffff, 0, ST_AUX);
      }
    }
#pragma unroll
    for (int h = 0; h < 2; ++h) {
      const unsigned t0 = __builtin_amdgcn_update_dpp(0u, up ? it[2 * h][0] : it[2 * h + 1][0], 0x128, 0xf, 0xf, false);
      const unsigned t1 = __builtin_amdgcn_update_dpp(0u, up ? it[2 * h][1] : it[2 * h + 1][1], 0x128, 0xf, 0xf, false);
      const u32x4 o = {up ? t0 : it[2 * h][0], up ? t1 : it[2 * h][1], up ? it[2 * h + 1][0] : t0, up ? it[2 * h + 1][1] : t1};
      const int f0 = fb + 8 * nt + (r >> 3);
      ST16(o, rx, (h ? 320 - f0 : f0) * 8192 + (r & 7) * 16, 0);
    }
  }
}

struct Args { const u16 *x, *wqkv, *wo, *wr, *wi, *gw; char* ws; u16* out; };

DI void phase1(const Args& A, lptr sm, f32x4 (&pq)[2][5], unsigned e TS_ARG) {
  const u16* x = A.x; const u16* wqkv = A.wqkv; const u16* wr = A.wr; const u16* wi = A.wi; char* ws = A.ws;
  const int tid = threadIdx.x, lane = tid & 63;
  const int wave = __builtin_amdgcn_readfirstlane(tid >> 6);
  const int b = blockIdx.x;
  const int xcd = b & 7, idx = b >> 3;
  const int rt = (xcd & 1) * 8 + (idx & 7);
  const int ct = (xcd >> 1) * 4 + (idx >> 3);
  const bool dft = ct >= 12, iskv = ct >= 4 && ct < 12;
  const int c0 = iskv ? 80 * (ct - 4) : 160 * (ct & 3);
  const int L0 = c0 >> 6, p0 = (c0 & 63) >> 4;
  const int xs = (p0 == 0 || p0 == 3) ? 1 : 2;
  const int bigB = p0 >= 2 ? 1 : 0;

  K1Ctx c;
  c.wave = wave;
  c.ra = mk_rsrc(x, 2048 * 1280);
  c.rb = dft ? mk_rsrc((const char*)&FT + (ct & 3) * 102400, 102400) : mk_rsrc(wqkv, 640 * 3840);
  c.rs = 3840;
  auto grow = [&](int row) { return dft ? 32 * (4 * rt + 2 * ((row >> 3) & 1) + ((row >> 4) & 1)) + 8 * (row >> 5) + (row & 7) : 128 * rt + row; };
#pragma unroll
  for (int i = 0; i < 2; ++i) {
    const int row = 16 * wave + 8 * i + (lane >> 3);
    c.va[i] = grow(row) * 1280 + ((lane & 7) ^ ((row >> 1) & 7)) * 16;
  }
  c.ldsa = (unsigned)(size_t)sm + 2 * wave * 1024;
  c.st = (((((idx & 7) * 5 + (idx >> 3) * 10) >> 2) % 10) * K1_ROT / 10) * (10 / K1_ROT);
  k1_issue1<0, 0>(c); k1_issue1<0, 1>(c);
  SCHED();
  {
    const int row = lane >> 3, hf = lane & 1;
    const int pair = ((lane & 7) >> 1) ^ (((row >> 1) & 1) | ((wave & 1) << 1));
    const unsigned rowb = (8 * wave + row) * c.rs;
    const bool kown = bigB ? (pair >= p0) : (pair <= p0);
    c.vb[0] = rowb + (iskv ? 1280 + (L0 + bigB) * 128 : L0 * 128) + pair * 32 + hf * 16;
    c.vb[1] = rowb + (iskv ? 2560 + (L0 + bigB) * 128 + (hf ^ 1) * 16 : (L0 + 1) * 128 + hf * 16) + pair * 32;
    c.vb[2] = rowb + (iskv ? (L0 + 1 - bigB) * 128 + (kown ? 1280 + pair * 32 + hf * 16 : 2560 + (pair ^ xs) * 32 + (hf ^ 1) * 16)
                           : (L0 + 2) * 128 + pair * 32 + hf * 16);
  }
  c.ldsb = (unsigned)(size_t)sm + 16384 + (dft ? 1 : 3) * wave * 1024;
  if (dft) { c.vb[0] = wave * 1024 + lane * 16; c.vb[1] = wave < 2 ? c.vb[0] + 8192 : 0x7fffffffu; }
  if (dft) { kd_issue1<0, 2>(c); kd_issue1<0, 3>(c); }
  else { k1_issue1<0, 2>(c); k1_issue1<0, 3>(c); k1_issue1<0, 4>(c); }
  c.st = (c.st == 9) ? 0 : c.st + 1;
  BAR();
  if (dft) { kd_issue<1>(c); kd_issue<2>(c); }
  else k1_issue<1>(c);

  const int wm = wave >> 1, wn = wave & 1;
  const int r = lane & 15, g = lane >> 4, q = (lane & 15) >> 2, p = lane & 3;
  const int hsw = ((q >> 1) & 1) | ((g & 1) << 1);
  const unsigned bbase = 16384 + g * 3072 + q * 128;
  c.ao[0][0] = (32 * wm + r) * 128 + ((g ^ (r >> 1)) * 16);
  c.ao[0][1] = (32 * wm + r) * 128 + (((4 + g) ^ (r >> 1)) * 16);
  f32x4 acc[2][5];
  Frags F0, F1;
  const rsrc_t rpf = (wave & 1) ? mk_rsrc(&GT, 491520) : mk_rsrc(A.wo, 819200);
  const unsigned vpf = (wave < 2 && lane < ((wave & 1) ? 20 : 34)) ? ((ct * 16 + rt) * ((wave & 1) ? 20 : 34) + lane) * 128 : 0x7fffffffu;

#define K1_HEAD(SWAP, HOOK, W2)                                                                          \
  _Pragma("unroll") for (int u = 1; u < 3; ++u) {                                              \
    _Pragma("unroll") for (int k = 0; k < 2; ++k) { c.ao[u][k] = c.ao[0][k] + u * K1_STAGE; asm volatile("" : "+v"(c.ao[u][k])); }     \
    _Pragma("unroll") for (int k = 0; k < 5; ++k) { c.bo[u][k] = c.bo[0][k] + u * K1_STAGE; asm volatile("" : "+v"(c.bo[u][k])); }     \
  }                                                                                            \
  _Pragma("unroll") for (int mt = 0; mt < 2; ++mt)                                             \
    _Pragma("unroll") for (int nt = 0; nt < 5; ++nt) asm volatile("" : "+v"(acc[mt][nt]));     \
  TS(1);                                                                                       \
  WAIT_V(5); k1_step<SWAP, 0>(acc, F0, F1, sm, c);                                           \
  TS(2);                                                                                       \
  WAIT_V(5); k1_step<SWAP, 1>(acc, F1, F0, sm, c); HOOK;                                             \
  WAIT_V(W2); k1_step<SWAP, 2>(acc, F0, F1, sm, c);                                             \
  pf_asm(rpf, (unsigned)(size_t)sm + K1_JUNK + wave * 256, vpf, 0);

  if (iskv) {
#pragma unroll
    for (int nt = 0; nt < 5; ++nt) {
      const int u = 4 * nt + p;
      const int isv = u >= 10 ? 1 : 0;
      const int pc = 4 * p0 + 10 * wn + u - 10 * isv;
      const int gp = pc >> 2, big = ((gp >> 2) == bigB);
      const int slot = (gp & 3) ^ ((!big && isv) ? xs : 0);
      c.bo[0][nt] = bbase + (big ? isv : 2) * 1024 + ((slot ^ hsw) * 32) + (((pc & 3) ^ (isv << 1)) * 8);
      const float i0 = ((nt < 2) || (nt == 2 && r < 8)) ? kEps : 0.f;
#pragma unroll
      for (int mt = 0; mt < 2; ++mt) acc[mt][nt] = (f32x4){i0, i0, i0, i0};
    }
    unsigned ko[10];
    {
      constexpr int TI[10] = {0, 0, 1, 1, 1, 2, 2, 0, 1, 2}, TJ[10] = {2, 3, 2, 3, 4, 3, 4, -1, -1, -1};
#pragma unroll
      for (int i = 0; i < 10; ++i) {
        const int ti = TI[i], tj = TJ[i];
        const int ur = 16 * ti + r, hk = ur >= 20 ? 1 : 0;
        const int uc = 16 * tj + 4 * g - 40, hv = uc >= 20 ? 1 : 0;
        const bool valid = tj < 0 ? (ur < 40 && g == 0) : (ur < 40 && uc >= 0 && hk == hv);
        const int off = wave * 3840 + hk * 1920 + (ur - 20 * hk) * 96 + (tj < 0 ? 80 : (uc - 20 * hv) * 4);
        ko[i] = K1_STAGE + (valid ? off : 8 * 3840 + lane * 16);
        asm volatile("" : "+v"(ko[i]));
      }
    }
    K1_HEAD(false, , 5)
    WAIT_V(6); k1_step<false, 3>(acc, F1, F0, sm, c);
    WAIT_V(6); k1_step<false, 4>(acc, F0, F1, sm, c);
    WAIT_V(5); k1_step<false, 5>(acc, F1, F0, sm, c);
    WAIT_V(5); k1_step<false, 6>(acc, F0, F1, sm, c);
    WAIT_V(5); k1_step<false, 7>(acc, F1, F0, sm, c);
    WAIT_V(5); k1_step<false, 8>(acc, F0, F1, sm, c);
    WAIT_V(0); k1_step<false, 9>(acc, F1, F0, sm, c);
    k1_mma<false, 0, 20>(acc, F1);
    TS(3);
    bf16x8 fr[5];
#pragma unroll
    for (int nt = 0; nt < 5; ++nt) {
#pragma unroll
      for (int m = 0; m < 2; ++m)
#pragma unroll
        for (int j = 0; j < 4; ++j) {
          if (nt < 2) acc[m][nt][j] = phi_f(acc[m][nt][j]);
          if (nt == 2) { const float ph = phi_f(acc[m][nt][j]); acc[m][nt][j] = (r < 8) ? ph : acc[m][nt][j]; }
        }
      fr[nt] = pack8(acc[0][nt], acc[1][nt]);
    }
    const bf16x8 ones = {0x3f80, 0x3f80, 0x3f80, 0x3f80, 0x3f80, 0x3f80, 0x3f80, 0x3f80};
    {
      constexpr int TI[10] = {0, 0, 1, 1, 1, 2, 2, 0, 1, 2}, TJ[10] = {2, 3, 2, 3, 4, 3, 4, -1, -1, -1};
      const f32x4 zz = {0.f, 0.f, 0.f, 0.f};
      f32x4 d[10];
#pragma unroll
      for (int i = 0; i < 10; ++i) d[i] = MFMA16(TJ[i] < 0 ? ones : fr[TJ[i] < 0 ? 0 : TJ[i]], fr[TI[i]], zz);
#pragma unroll
      for (int i = 0; i < 10; ++i) *(LDS_AS f32x4*)(sm + ko[i]) = d[i];
    }
    WAIT_L0();
    BAR();
    const rsrc_t rk = mk_rsrc(ws + WS_KVP + ((size_t)rt * 32 + 4 * (ct - 4)) * 1920, 7680);
    LDS_AS f32x4* src = (LDS_AS f32x4*)(sm + K1_STAGE);
    const f32x4 s0 = (src[tid] + src[tid + 480]) + (src[tid + 960] + src[tid + 1440]);
    ST16(__builtin_bit_cast(u32x4, s0), rk, tid * 16, 0);
    if (wave >= 6) {
      LDS_AS float* zs = (LDS_AS float*)(sm + K1_STAGE) + min(tid - 384, 79) * 24 + 20;
      const float z = (zs[0] + zs[1920]) + (zs[3840] + zs[5760]);
      __builtin_amdgcn_raw_buffer_store_b32(__float_as_uint(z), mk_rsrc(ws + WS_ZP + rt * 2560 + (ct - 4) * 320, 320), (tid - 384) * 4, 0, ST_AUX);
    }
  } else {
    const int un = (dft ? 64 : 0) + rt * 4 + (ct & 3), mo = un >> 1, fh = un & 1;
    auto filt_load = [&]() {
      const rsrc_t rf = mk_rsrc((wave >> 2) ? wi : wr, 2048 * 642);
      const int kb = 16 * (mo >> 4) + 8 * (mo & 1) + 64 * (16 * ((mo >> 3) & 1) + 4 * ((mo >> 1) & 3) + (wave & 3));
      const unsigned v0 = (lane & 31) < 21 ? (lane >> 5) * 640 + (lane & 31) * 16 : 0x7fffffffu;
#pragma unroll
      for (int i = 0; i < 4; ++i)
        dma16_asm(rf, (unsigned)(size_t)sm + K1_WF + (wave * 4 + i) * 1024, v0, kb * 642 + 320 * fh + i * 1280);
    };
    auto filt_store = [&]() {
      const rsrc_t rw = mk_rsrc(ws + WS_WT + (size_t)fh * 160 * 8192 + mo * 128, (160 + fh) * 8192 - mo * 128);
      const int i8 = lane & 7;
#pragma unroll
      for (int rd = 0; rd < 3; ++rd) {
        const int fl = (rd * 8 + wave) * 8 + (lane >> 3);
        lptr src = sm + K1_WF + i8 * 514 + min(fl, 160) * 2;
        u32x4 o;
#pragma unroll
        for (int j = 0; j < 4; ++j) o[j] = (unsigned)*(LDS_AS u16*)(src + j * 4096) | ((unsigned)*(LDS_AS u16*)(src + 16384 + j * 4096) << 16);
        ST16(o, rw, fl * 8192 + i8 * 16, 0);
      }
      SCHED();
    };
    if (!dft) {
#pragma unroll
      for (int nt = 0; nt < 5; ++nt) {
        const int gp = p0 + 5 * wn + nt;
        c.bo[0][nt] = bbase + (gp >> 2) * 1024 + (((gp & 3) ^ hsw) * 32) + p * 8;
      }
#pragma unroll
      for (int mt = 0; mt < 2; ++mt)
#pragma unroll
        for (int nt = 0; nt < 5; ++nt) acc[mt][nt] = (f32x4){kEps, kEps, kEps, kEps};
      K1_HEAD(true, filt_load(), 9)
      WAIT_V(10); k1_step<true, 3>(acc, F1, F0, sm, c);
      WAIT_V(6); k1_step<true, 4>(acc, F0, F1, sm, c);
      filt_store();
      WAIT_V(8); k1_step<true, 5>(acc, F1, F0, sm, c);
      WAIT_V(8); k1_step<true, 6>(acc, F0, F1, sm, c);
      WAIT_V(5); k1_step<true, 7>(acc, F1, F0, sm, c);
      if (wave == 0) flag_store(ws, 1, e, lane);
      WAIT_V(5); k1_step<true, 8>(acc, F0, F1, sm, c);
      WAIT_V(0); k1_step<true, 9>(acc, F1, F0, sm, c);
      k1_mma<true, 0, 20>(acc, F1);
      TS(3);
#pragma unroll
      for (int mt = 0; mt < 2; ++mt)
#pragma unroll
        for (int nt = 0; nt < 5; ++nt)
#pragma unroll
          for (int j = 0; j < 4; ++j) pq[mt][nt][j] = phi_f(acc[mt][nt][j]);
    } else {
      if (wave < 4) kd_main<3>(ws, sm, c, rt, ct & 3, e, lane, filt_load, filt_store TS_PASS);
      else kd_main<2>(ws, sm, c, rt, ct & 3, e, lane, filt_load, filt_store TS_PASS);
    }
  }
  TS(4);
}

DI void publish(char* ws, int which, unsigned e) {
  WAIT_V(0);
  BAR();
  if (threadIdx.x < 64) flag_store(ws, which, e, threadIdx.x);
}
DI bool poll(char* ws, int which, unsigned e, int need, int lane, unsigned tries) {
  {
    const rsrc_t rf = mk_rsrc(ws + WS_SYN + which * 65536 + (blockIdx.x & 31) * 2048, 2048);
    const u32x2 t = make_tag(e);
    bool n0 = (need >> (lane < 32 ? 0 : 1)) & 1, n1 = (need >> (lane < 32 ? 1 : 2)) & 1;
    for (unsigned spin = 0; spin < tries; ++spin) {
      const u32x4 f0 = __builtin_amdgcn_raw_buffer_load_b128(rf, n0 ? lane * 16 : 0x7fffffffu, 0, 16);
      const u32x4 f1 = __builtin_amdgcn_raw_buffer_load_b128(rf, n1 ? lane * 16 + 1024 : 0x7fffffffu, 0, 16);
      n0 = n0 && !(f0[0] == t[0] && f0[1] == t[1] && f0[2] == t[0] && f0[3] == t[1]);
      n1 = n1 && !(f1[0] == t[0] && f1[1] == t[1] && f1[2] == t[0] && f1[3] == t[1]);
      if (!__any(n0 || n1)) return true;
      __builtin_amdgcn_s_sleep(POLL_SLEEP);
    }
  }
  return false;
}
DI void await(char* ws, int which, unsigned e, int need, int wave, int lane) {
  if (wave == 7) poll(ws, which, e, need, lane, 1u << 17);
  asm volatile("" ::: "memory");
  BAR();
  asm volatile("" ::: "memory");
}
DI void dma16_sc1(rsrc_t rs, unsigned lds, unsigned vo, int so) {
  asm volatile("s_mov_b32 m0, %2\n\ts_nop 0\n\tbuffer_load_dwordx4 %0, %1, %3 offen sc1 lds" :: "v"(vo), "s"(rs), "s"(lds), "s"(so));
}
#define LD16(rs, vo, so) __builtin_amdgcn_raw_buffer_load_b128((rs), (vo), (so), LD_AUX)

constexpr int P3_HALF = 71680;

DI void p3_load_g(lptr sm, int wave, int lane) {
  const int b = blockIdx.x, ct = ((b & 7) >> 2) * 4 + (b >> 6);
  const rsrc_t rg = mk_rsrc((const char*)&GT + (size_t)(2 * ct) * 30720, 61440);
#pragma unroll
  for (int i = 0; i < 9; ++i) {
    const int n = min(wave * 9 + i, 59);
    if (wave < 7)
    dma16_asm(rg, (unsigned)(size_t)sm + (n >= 30 ? P3_HALF + 40960 - 30720 : 40960) + n * 1024, lane * 16, n * 1024);
  }
}

struct FftC {
  const u32x4 (&fre)[4]; const u32x4 (&fim)[4]; const u32x4 (&fc)[2]; const u32x4 (&fs)[2]; const u32x4 (&fn)[2];
  const f32x2 (&tc)[2][2]; const f32x2 (&tsn)[2][2]; const unsigned (&uo)[2][4]; const unsigned (&ro)[4]; unsigned vy;
};
DI f32x2 half2(const f32x4& v, int h) { return h ? (f32x2){v[2], v[3]} : (f32x2){v[0], v[1]}; }
constexpr int FSECOND = 304;
template <int NC> DI void fft_run(char* ws, unsigned e, lptr sm, const FftC& T, int fu0, int wave8, int lane TS_ARG) {
  const int wave = wave8 & 3;
  const unsigned smu = (unsigned)(size_t)sm;
  const f32x4 z4 = {0.f, 0.f, 0.f, 0.f};
  await(ws, 1, e, 5, wave8, lane);
  TS(7);
  u32x4 wv[NC][2];
#pragma unroll
  for (int c = 0; c < NC; ++c) {
    const rsrc_t rx = mk_rsrc(ws + WS_X1 + (size_t)min((c ? FSECOND + (fu0 >> 1) : fu0), 320) * 8192, 8192);
    dma16_sc1(rx, smu + c * 16384 + wave * 2048, lane * 16, wave * 2048);
    dma16_sc1(rx, smu + c * 16384 + wave * 2048 + 1024, lane * 16, wave * 2048 + 1024);
  }
#pragma unroll
  for (int c = 0; c < NC; ++c) {
    const rsrc_t rw = mk_rsrc(ws + WS_WT + (size_t)min((c ? FSECOND + (fu0 >> 1) : fu0), 320) * 8192, 8192);
    wv[c][0] = LD16(rw, lane * 16, wave * 2048);
    wv[c][1] = LD16(rw, lane * 16 + 1024, wave * 2048);
  }
  SCHED();
  WAIT_V(0);
#pragma unroll
  for (int c = 0; c < NC; ++c) asm volatile("" : "+v"(wv[c][0]), "+v"(wv[c][1]));
  BAR();
  SCHED();
  TS(8);
  f32x2 wre[NC][2][2], wim[NC][2][2];
#pragma unroll
  for (int c = 0; c < NC; ++c) {
    const int f = min((c ? FSECOND + (fu0 >> 1) : fu0), 320);
    const float sc = ((f == 0 || f == 320) ? 1.f : 2.f) / (2048.f * 640.f);
#pragma unroll
    for (int t = 0; t < 2; ++t)
#pragma unroll
      for (int h = 0; h < 2; ++h) {
        wre[c][t][h] = (f32x2){bf_lo(wv[c][t][2 * h]), bf_lo(wv[c][t][2 * h + 1])} * sc;
        wim[c][t][h] = (f32x2){bf_hi(wv[c][t][2 * h]), bf_hi(wv[c][t][2 * h + 1])} * sc;
      }
  }
#define FOR_C _Pragma("unroll") for (int c = 0; c < NC; ++c)
#define FOR_T _Pragma("unroll") for (int t = 0; t < 2; ++t)
#define FOR_H _Pragma("unroll") for (int h = 0; h < 2; ++h)
#define FOR_K _Pragma("unroll") for (int ks = 0; ks < 4; ++ks)
  f32x4 re[NC][2], im[NC][2];
  u32x4 bre[NC], bim[NC];
  {
    bf16x8 xa[NC][2][4];
    FOR_K FOR_C FOR_T xa[c][t][ks] = *(LDS_AS bf16x8*)(sm + c * 16384 + lane * 16 + (t * 4 + ks) * 1024);
    FOR_K FOR_C FOR_T {
      re[c][t] = MFMA16(xa[c][t][ks], BC(T.fre[ks]), ks ? re[c][t] : z4);
      im[c][t] = MFMA16(xa[c][t][ks], BC(T.fim[ks]), ks ? im[c][t] : z4);
    }
  }
  FOR_C FOR_T FOR_H {
    const f32x2 a = half2(re[c][t], h), d = half2(im[c][t], h);
    const f32x2 x = a * T.tc[t][h] + d * T.tsn[t][h], y = d * T.tc[t][h] - a * T.tsn[t][h];
    bre[c][2 * t + h] = pk2(x[0], x[1]); bim[c][2 * t + h] = pk2(y[0], y[1]);
  }
  FOR_C FOR_T { re[c][t] = MFMA16(BC(T.fc[t]), BC(bre[c]), z4); im[c][t] = MFMA16(BC(T.fc[t]), BC(bim[c]), z4); }
  FOR_C FOR_T { re[c][t] = MFMA16(BC(T.fs[t]), BC(bim[c]), re[c][t]); im[c][t] = MFMA16(BC(T.fn[t]), BC(bre[c]), im[c][t]); }
  FOR_C FOR_T FOR_H {
    const f32x2 a = half2(re[c][t], h), d = half2(im[c][t], h);
    const f32x2 x = a * wre[c][t][h] - d * wim[c][t][h], y = a * wim[c][t][h] + d * wre[c][t][h];
    bre[c][2 * t + h] = pk2(x[0], x[1]); bim[c][2 * t + h] = pk2(y[0], y[1]);
  }
  FOR_C FOR_T { re[c][t] = MFMA16(BC(T.fc[t]), BC(bre[c]), z4); im[c][t] = MFMA16(BC(T.fc[t]), BC(bim[c]), z4); }
  FOR_C FOR_T { re[c][t] = MFMA16(BC(T.fn[t]), BC(bim[c]), re[c][t]); im[c][t] = MFMA16(BC(T.fs[t]), BC(bre[c]), im[c][t]); }
  FOR_C FOR_T FOR_H {
    const f32x2 a = half2(re[c][t], h), d = half2(im[c][t], h);
    const f32x2 x = a * T.tc[t][h] - d * T.tsn[t][h], y = -(d * T.tc[t][h]) - a * T.tsn[t][h];
    *(LDS_AS unsigned*)(sm + c * 16384 + T.uo[t][2 * h]) = pk2(x[0], y[0]);
    *(LDS_AS unsigned*)(sm + c * 16384 + T.uo[t][2 * h + 1]) = pk2(x[1], y[1]);
  }
  WAIT_L0();
  BAR();
  SCHED();
  {
    bf16x8 ua[NC][2][4];
    FOR_K FOR_C FOR_T ua[c][t][ks] = *(LDS_AS bf16x8*)(sm + c * 16384 + T.ro[ks] + t * 4096);
    FOR_K FOR_C FOR_T {
      re[c][t] = MFMA16(ua[c][t][ks], BC(T.fre[ks]), ks ? re[c][t] : z4);
      im[c][t] = MFMA16(ua[c][t][ks], BC(T.fim[ks]), ks ? im[c][t] : z4);
    }
  }
  FOR_C {
    const int fu = (c ? FSECOND + (fu0 >> 1) : fu0), f = min(fu, 320);
    const rsrc_t ry = mk_rsrc(ws + WS_Y + (size_t)f * 4096, fu > 320 ? 0 : ((f == 0 || f == 320) ? 4096 : 321 * 4096));
    ST16(widen((u32x2){pk2(re[c][0][0], re[c][0][1]), pk2(re[c][0][2], re[c][0][3])}, (u32x2){pk2(re[c][1][0], re[c][1][1]), pk2(re[c][1][2], re[c][1][3])}), ry, T.vy, 0);
    ST16(widen((u32x2){pk2(im[c][0][0], im[c][0][1]), pk2(im[c][0][2], im[c][0][3])}, (u32x2){pk2(im[c][1][0], im[c][1][1]), pk2(im[c][1][2], im[c][1][3])}), ry, T.vy, 320 * 4096);
  }
}

template <bool F> DI void phase2(const Args& A, lptr sm0, unsigned e, const f32x4 (&pq)[2][5] TS_ARG) {
  char* ws = A.ws;
  const int lane = threadIdx.x & 63, tid = threadIdx.x & 255;
  const int wave8 = __builtin_amdgcn_readfirstlane(threadIdx.x >> 6);
  const int wave = wave8 & 3, team = wave8 >> 2;
  lptr sm = sm0 + team * P3_HALF;
  const unsigned smu = (unsigned)(size_t)sm;
  const int b = blockIdx.x, xcd = b & 7, idx = b >> 3;
  const int rt1 = (xcd & 1) * 8 + (idx & 7), ct1 = (xcd >> 1) * 4 + (idx >> 3);
  const bool iskv = ct1 >= 4 && ct1 < 12;
  const int v = (ct1 - 4) * 16 + rt1;
  const int r = lane & 15, g = lane >> 4;
  const f32x4 z4 = {0.f, 0.f, 0.f, 0.f};
  const bool isfft = ct1 >= 12 || (iskv && v < 88);
  publish(ws, 1, e);
  TS(5);
  if (isfft) {
    const int fu0 = (iskv ? 128 + 2 * v : 2 * (rt1 * 4 + (ct1 & 3))) + team;
    u32x4 fre[4], fim[4], fc[2], fs[2], fn[2];
    f32x2 tc[2][2], tsn[2][2];
    unsigned uo[2][4], ro[4];
    {
      const float a64 = (float)(16 * wave + r) * (1.f / 64.f);
      const unsigned m2 = (r & 2) ? 0x80008000u : 0u, m1 = (r & 1) ? 0x80008000u : 0u;
      const unsigned m3 = ((r + 1) & 2) ? 0x80008000u : 0u;
#pragma unroll
      for (int j = 0; j < 4; ++j) {
        const float ang = a64 * (float)(4 * g + j);
        const float c = __builtin_amdgcn_cosf(ang), s = __builtin_amdgcn_sinf(ang);
        const unsigned p = pk2(c, s), q = pk2(-s, c);
        fre[0][j] = p; fim[0][j] = q;
        fre[1][j] = ((r & 1) ? q : p) ^ m2;
        fim[1][j] = ((r & 1) ? p : q) ^ m3;
        fre[2][j] = p ^ m1; fim[2][j] = q ^ m1;
        fre[3][j] = fre[1][j] ^ m1; fim[3][j] = fim[1][j] ^ m1;
      }
      const float a32 = (float)r * (1.f / 32.f);
#pragma unroll
      for (int j = 0; j < 2; ++j) {
        const float a0 = a32 * (float)(4 * g + 2 * j), a1 = a0 + a32;
        const unsigned c = pk2(__builtin_amdgcn_cosf(a0), __builtin_amdgcn_cosf(a1)), s = pk2(__builtin_amdgcn_sinf(a0), __builtin_amdgcn_sinf(a1));
        fc[0][j] = c; fc[0][2 + j] = c ^ m1; fs[0][j] = s; fs[0][2 + j] = s ^ m1;
      }
#pragma unroll
      for (int j = 0; j < 4; ++j) {
        fc[1][j] = fc[0][j] ^ 0x80000000u; fs[1][j] = fs[0][j] ^ 0x80000000u;
        fn[0][j] = fs[0][j] ^ 0x80008000u; fn[1][j] = fs[0][j] ^ 0x00008000u;
        ro[j] = 8192 + r * 256 + (((4 * j + g) ^ r) * 16);
      }
#pragma unroll
      for (int t = 0; t < 2; ++t)
#pragma unroll
        for (int j = 0; j < 4; ++j) {
          const int s2 = 16 * t + 4 * g + j;
          const float a = a64 * (1.f / 32.f) * (float)s2;
          tc[t][j >> 1][j & 1] = __builtin_amdgcn_cosf(a);
          tsn[t][j >> 1][j & 1] = __builtin_amdgcn_sinf(a);
          uo[t][j] = 8192 + s2 * 256 + (((4 * wave + (r >> 2)) ^ (s2 & 15)) * 16) + (r & 3) * 4;
        }
    }
    unsigned vy = wave * 1024 + r * 64 + (g & 1) * 32 + (g >> 1) * 16;
#pragma unroll
    for (int ks = 0; ks < 4; ++ks) { asm volatile("" : "+v"(fre[ks]), "+v"(fim[ks]), "+v"(ro[ks])); }
#pragma unroll
    for (int t = 0; t < 2; ++t) {
      asm volatile("" : "+v"(fc[t]), "+v"(fs[t]), "+v"(fn[t]), "+v"(tc[t][0]), "+v"(tc[t][1]), "+v"(tsn[t][0]), "+v"(tsn[t][1]));
      asm volatile("" : "+v"(uo[t][0]), "+v"(uo[t][1]), "+v"(uo[t][2]), "+v"(uo[t][3]), "+v"(vy));
    }
    SCHED();
    TS(6);
    FftC T = {fre, fim, fc, fs, fn, tc, tsn, uo, ro, vy};
    if (fu0 < 34 && team == 0) fft_run<2>(ws, e, sm, T, fu0, wave8, lane TS_PASS);
    else fft_run<1>(ws, e, sm, T, fu0, wave8, lane TS_PASS);
  } else if (iskv) {
    const int ks = (v - 88) >> 1, cp = 2 * ((v - 88) & 1) + team;
    const int hf = (32 * ks) / 20, e0 = 20 * hf;
    const int q = r >> 2, p = lane & 3;
    const int hsw = ((q >> 1) & 1) | ((g & 1) << 1);
    const int L1 = (160 * cp) >> 7, L2 = (1120 - 160 * cp) >> 7;
    {
      const rsrc_t rwo = mk_rsrc(A.wo, 640 * 1280);
      const int line = (wave < 2 ? L1 : L2) + (wave & 1), row = lane >> 3;
#pragma unroll
      for (int kb = 0; kb < 8; ++kb) {
        const int pair = ((lane & 7) >> 1) ^ (((row >> 1) & 1) | ((kb & 1) << 1));
        dma16_asm(rwo, smu + (wave * 8 + kb) * 1024, min(e0 + 8 * kb + row, 639) * 1280 + line * 128 + pair * 32 + (lane & 1) * 16, 0);
      }
    }
    lptr ab = sm0 + 32768;
    if (threadIdx.x < 288) *(LDS_AS u32x4*)(ab + threadIdx.x * 16) = (u32x4){0u, 0u, 0u, 0u};
    const int t5 = min((int)threadIdx.x >> 1, 159), kl = t5 / 5, qd = t5 - kl * 5;
    const int k = 32 * ks + kl, h = k / 20, d = k - 20 * h;
    unsigned ko = h * 1920 + d * 96 + qd * 16 + (threadIdx.x & 1) * 8 * 61440, ao = kl * 144 + (20 * (h - hf) + 4 * qd) * 2;
    unsigned bo[3];
#pragma unroll
    for (int n = 0; n < 3; ++n) {
      const int tl = min(wave + 4 * n, 9);
      const int ctl = tl >= 5 ? 1 : 0, nt = tl - 5 * ctl, ctg = 2 * cp + ctl;
      const int i = 16 * nt + 4 * p;
      const int byte = 2 * (i < 40 ? 40 * ctg + i : 560 - 40 * ctg + i);
      const int pl = i < 40 ? (byte >> 7) - L1 : 2 + (byte >> 7) - L2;
      bo[n] = (pl * 8 + g) * 1024 + q * 128 + ((((byte >> 5) & 3) ^ hsw) * 32) + (byte & 31);
      asm volatile("" : "+v"(bo[n]));
    }
    asm volatile("" : "+v"(ko), "+v"(ao));
    WAIT_V(0);
    WAIT_L0();
    TS(6);
    await(ws, 1, e, 2, wave8, lane);
    TS(7);
    f32x4 pv[8];
    const rsrc_t rk = mk_rsrc(ws + WS_KVP, wave8 < 5 ? 983040 : 0);
#pragma unroll
    for (int i = 0; i < 8; ++i) pv[i] = __builtin_bit_cast(f32x4, LD16(rk, ko, i * 61440));
    SCHED();
    bf16x8 bw[3][2];
#pragma unroll
    for (int n = 0; n < 3; ++n)
#pragma unroll
      for (int s = 0; s < 2; ++s) {
        const s16x4 lo = TRREAD(sm + bo[n] + s * 4096), hi = TRREAD(sm + bo[n] + s * 4096 + 512);
        bw[n][s] = __builtin_shufflevector(lo, hi, 0, 1, 2, 3, 4, 5, 6, 7);
      }
    SCHED();
    f32x4 sum = ((pv[0] + pv[1]) + (pv[2] + pv[3])) + ((pv[4] + pv[5]) + (pv[6] + pv[7]));
#pragma unroll
    for (int j = 0; j < 4; ++j) sum[j] += __uint_as_float(__builtin_amdgcn_update_dpp(0u, __float_as_uint(sum[j]), 0xB1, 0xf, 0xf, false));
    if (threadIdx.x < 320 && !(threadIdx.x & 1)) *(LDS_AS u32x2*)(ab + ao) = (u32x2){pk2(sum[0], sum[1]), pk2(sum[2], sum[3])};
    WAIT_L0();
    BAR();
    TS(8);
    bf16x8 a[2][2];
#pragma unroll
    for (int t = 0; t < 2; ++t)
#pragma unroll
      for (int s = 0; s < 2; ++s) a[t][s] = *(LDS_AS bf16x8*)(ab + (8 * (r >> 2) + (r & 3) + 4 * t) * 144 + s * 64 + g * 16);
    const rsrc_t rm = mk_rsrc(ws + WS_M + (size_t)(10 * cp) * 20480 + ks * 1024, 9 * 20480 + 1024);
#pragma unroll
    for (int n = 0; n < 3; ++n) {
      f32x4 d1 = MFMA16(a[0][0], bw[n][0], z4), d2 = MFMA16(a[1][0], bw[n][0], z4);
      d1 = MFMA16(a[0][1], bw[n][1], d1); d2 = MFMA16(a[1][1], bw[n][1], d2);
      ST16(__builtin_bit_cast(u32x4, pack8(d1, d2)), rm, lane * 16, (wave + 4 * n) * 20480);
    }
  } else if (ct1 < 4) {
    const int t8 = threadIdx.x, wm = wave8 >> 1, wn = wave8 & 1;
    const int gb = rt1 * 4 + ct1;
      const rsrc_t rx = mk_rsrc(A.x + (size_t)gb * 32 * 640, 32 * 1280), rg = mk_rsrc(A.gw, 1280);
    u32x4 gx[5], gg[5];
#pragma unroll
      for (int i = 0; i < 5; ++i) {
        gx[i] = __builtin_amdgcn_raw_buffer_load_b128(rx, t8 * 80 + i * 16, 0, 0);
        gg[i] = __builtin_amdgcn_raw_buffer_load_b128(rg, (t8 & 15) * 80 + i * 16, 0, 0);
      }
    u32x2 pk[2][5];
    unsigned zm[5];
#pragma unroll
    for (int nt = 0; nt < 5; ++nt) {
#pragma unroll
      for (int mt = 0; mt < 2; ++mt) {
        pk[mt][nt] = (u32x2){pk2(pq[mt][nt][0], pq[mt][nt][1]), pk2(pq[mt][nt][2], pq[mt][nt][3])};
        asm volatile("" : "+v"(pk[mt][nt]));
      }
      zm[nt] = ((4 * nt + g) / 5 == (r & 3)) ? 0xffffffffu : 0u;
      asm volatile("" : "+v"(zm[nt]));
    }
    const rsrc_t rz = mk_rsrc(ws + WS_ZP + ct1 * 640, 40960);
    const rsrc_t rp = mk_rsrc(ws + WS_P + (size_t)(8 * rt1 + 2 * wm) * 20480 + (20 * ct1 + 10 * wn) * 256, 2 * 20480);
    const unsigned vo = (g & 1) * 20480 + (g >> 1) * 256 + r * 16;
    SCHED();
    TS(6);
    await(ws, 1, e, 2, wave8, lane);
    TS(7);
    if (wave8 < 3) {
      float zv[16];
#pragma unroll
      for (int i = 0; i < 16; ++i) zv[i] = __uint_as_float(__builtin_amdgcn_raw_buffer_load_b32(rz, min(t8, 159) * 4, i * 2560, LD_AUX));
#pragma unroll
      for (int i = 0; i < 8; ++i) zv[i] += zv[i + 8];
      const float zs = ((zv[0] + zv[1]) + (zv[2] + zv[3])) + ((zv[4] + zv[5]) + (zv[6] + zv[7]));
      *(LDS_AS u16*)(sm0 + t8 * 2) = (u16)pk2(zs, zs);
    }
    WAIT_L0();
    BAR();
    TS(8);
    u32x2 zq[6];
#pragma unroll
    for (int nt = 0; nt < 5; ++nt) { zq[nt] = *(LDS_AS u32x2*)(sm0 + wn * 160 + nt * 32 + g * 8); zq[nt][0] &= zm[nt]; zq[nt][1] &= zm[nt]; }
    zq[5] = (u32x2){0u, 0u};
    u32x2 po[2][5];
#pragma unroll
    for (int mt = 0; mt < 2; ++mt) {
      f32x4 den = {kEps, kEps, kEps, kEps};
#pragma unroll
      for (int m = 0; m < 3; ++m) {
        const u32x4 za = {zq[2 * m][0], zq[2 * m][1], zq[2 * m + 1][0], zq[2 * m + 1][1]};
        const u32x2 p1 = m < 2 ? pk[mt][2 * m + 1] : (u32x2){0u, 0u};
        const u32x4 pb = {pk[mt][2 * m][0], pk[mt][2 * m][1], p1[0], p1[1]};
        den = MFMA16(BC(za), BC(pb), den);
      }
      f32x4 inv;
#pragma unroll
      for (int j = 0; j < 4; ++j) inv[j] = __builtin_amdgcn_rcpf(den[j]);
#pragma unroll
      for (int nt = 0; nt < 5; ++nt) {
        const float iv = nt == 0 ? inv[0] : nt == 4 ? inv[3] : (g < nt ? inv[nt - 1] : inv[nt]);
        const f32x4 w = pq[mt][nt] * iv;
        po[mt][nt] = (u32x2){pk2(w[0], w[1]), pk2(w[2], w[3])};
      }
    }
#pragma unroll
    for (int nt = 0; nt < 5; ++nt) ST16(widen(po[0][nt], po[1][nt]), rp, vo + nt * 512, 0);
    SCHED();
    {
      float s0 = 0.f, s1 = 0.f;
#pragma unroll
      for (int i = 0; i < 5; ++i) { s0 = dot2(gx[i][0], gg[i][0], s0); s1 = dot2(gx[i][1], gg[i][1], s1); s0 = dot2(gx[i][2], gg[i][2], s0); s1 = dot2(gx[i][3], gg[i][3], s1); }
      float s = s0 + s1;
#pragma unroll
      for (int o = 8; o > 0; o >>= 1) s += __shfl_xor(s, o);
      if ((t8 & 15) == 0) __builtin_amdgcn_raw_buffer_store_b32(__float_as_uint(1.f / (1.f + __expf(-s))), mk_rsrc(ws + WS_G, 8192), (gb * 32 + (t8 >> 4)) * 4, 0, 16);
    }
  }
  TS(9);
}

constexpr int LDS_ALL = K1_LDS;
static_assert(2 * P3_HALF + 256 <= LDS_ALL, "lds");

template <bool F> DI void phase3(const Args& A, lptr sm, unsigned e TS_ARG) {
  char* ws = A.ws;
  const int tid = threadIdx.x, lane = tid & 63;
  const int wave = __builtin_amdgcn_readfirstlane(tid >> 6);
  const int b = blockIdx.x, xcd = b & 7, idx = b >> 3;
  const int rt = (xcd & 3) * 8 + (idx & 7);
  const int ct = (xcd >> 2) * 4 + (idx >> 3);
  const int r = lane & 15, g = lane >> 4;
  LDS_AS float* red = (LDS_AS float*)sm;
  constexpr int RA = 64 * 84, RE = 64 * 52;
  if (!F) { p3_load_g(sm, wave, lane); if (wave >= 4) WAIT_V(0); }
  TS(11);
  if (wave < 2) {
    const rsrc_t ra = mk_rsrc(ws + WS_P + (size_t)(4 * rt) * 20480 + wave * 10240, 4 * 20480);
    const rsrc_t rb = mk_rsrc(ws + WS_M + (size_t)(5 * ct) * 20480 + wave * 10240, 5 * 20480);
    const unsigned vo = lane * 16;
    f32x4 acc[4][5];
#pragma unroll
    for (int mt = 0; mt < 4; ++mt)
#pragma unroll
      for (int nt = 0; nt < 5; ++nt) acc[mt][nt] = (f32x4){0.f, 0.f, 0.f, 0.f};
    u32x4 fa[10][4], fb[10][5];
    constexpr int PF = 3;
    auto ld = [&](int ks) {
#pragma unroll
      for (int nt = 0; nt < 5; ++nt) fb[ks][nt] = LD16(rb, vo + (ks & 3) * 1024, nt * 20480 + (ks >> 2) * 4096);
#pragma unroll
      for (int mt = 0; mt < 4; ++mt) fa[ks][mt] = LD16(ra, vo + (ks & 3) * 1024, mt * 20480 + (ks >> 2) * 4096);
    };
#pragma unroll
    for (int ks = 0; ks < PF; ++ks) { ld(ks); SCHED(); }
    if (!F) { WAIT_V(27); BAR(); }
#pragma unroll
    for (int ks = 0; ks < 10; ++ks) {
#pragma unroll
      for (int mt = 0; mt < 4; ++mt)
#pragma unroll
        for (int nt = 0; nt < 5; ++nt) acc[mt][nt] = MFMA16(BC(fb[ks][nt]), BC(fa[ks][mt]), acc[mt][nt]);
      SCHED();
      if (ks + PF < 10) ld(ks + PF);
      SCHED();
    }
    TS(12);
    BAR();
#pragma unroll
    for (int mt = 0; mt < 4; ++mt)
#pragma unroll
      for (int nt = 0; nt < 5; ++nt) *(LDS_AS f32x4*)(red + wave * RA + (16 * mt + r) * 84 + 16 * nt + 4 * g) = acc[mt][nt];
  } else if (wave < 4) {
    const int od = wave - 2;
    lptr yb = sm + od * P3_HALF;
    const unsigned ybu = (unsigned)(size_t)yb;
    const rsrc_t ry = mk_rsrc(ws + WS_Y + (size_t)od * 320 * 4096 + rt * 128, 320 * 4096);
    const int row = lane >> 3;
    const unsigned vy0 = row * 4096 + ((((lane & 7) >> 1) ^ ((row >> 1) & 1)) * 32) + (lane & 1) * 16;
    const unsigned vy1 = vy0 ^ 64;
    auto issue = [&](int ks) {
#pragma unroll
      for (int i = 0; i < 4; ++i) dma16_sc1(ry, ybu + (ks * 4 + i) * 1024, (i & 1) ? vy1 : vy0, (ks * 4 + i) * 32768);
    };
    issue(0); issue(1); issue(2);
    if (!F) { WAIT_V(12); BAR(); }
    const int q = r >> 2, p = lane & 3;
    const int hsw = ((q >> 1) & 1) | ((g & 1) << 1);
    unsigned yo[4];
#pragma unroll
    for (int mt = 0; mt < 4; ++mt) yo[mt] = g * 1024 + q * 128 + ((mt ^ hsw) * 32) + p * 8;
    lptr gb = yb + 40960 + lane * 16;
    f32x4 acc[4][3];
#pragma unroll
    for (int mt = 0; mt < 4; ++mt)
#pragma unroll
      for (int nt = 0; nt < 3; ++nt) acc[mt][nt] = (f32x4){0.f, 0.f, 0.f, 0.f};
#define K3_STEP(ks, n)                                                                                         \
    {                                                                                                          \
      WAIT_V(n);                                                                                               \
      SCHED();                                                                                                 \
      bf16x8 fy[4], fg[3];                                                                                     \
      _Pragma("unroll") for (int nt = 0; nt < 3; ++nt) fg[nt] = *(LDS_AS bf16x8*)(gb + (ks * 3 + nt) * 1024);  \
      _Pragma("unroll") for (int mt = 0; mt < 4; ++mt) {                                                       \
        const s16x4 lo = TRREAD(yb + yo[mt] + ks * 4096), hi = TRREAD(yb + yo[mt] + ks * 4096 + 512);          \
        fy[mt] = __builtin_shufflevector(lo, hi, 0, 1, 2, 3, 4, 5, 6, 7);                                      \
      }                                                                                                        \
      SCHED();                                                                                                 \
      if (ks + 3 < 10) issue(ks + 3);                                                                          \
      SCHED();                                                                                                 \
      _Pragma("unroll") for (int mt = 0; mt < 4; ++mt)                                                         \
        _Pragma("unroll") for (int nt = 0; nt < 3; ++nt) acc[mt][nt] = MFMA16(fg[nt], fy[mt], acc[mt][nt]);   \
      SCHED();                                                                                                 \
    }
    K3_STEP(0, 8) K3_STEP(1, 8) K3_STEP(2, 8) K3_STEP(3, 8) K3_STEP(4, 8)
    K3_STEP(5, 8) K3_STEP(6, 8) K3_STEP(7, 8) K3_STEP(8, 4) K3_STEP(9, 0)
    TS(12);
    BAR();
#pragma unroll
    for (int mt = 0; mt < 4; ++mt)
#pragma unroll
      for (int nt = 0; nt < 3; ++nt) *(LDS_AS f32x4*)(red + 2 * RA + od * RE + (16 * mt + r) * 52 + 16 * nt + 4 * g) = acc[mt][nt];
  } else {
    if (!F) BAR();
    if (wave == 4) red[2 * P3_HALF / 4 + lane] = __uint_as_float(__builtin_amdgcn_raw_buffer_load_b32(mk_rsrc(ws + WS_G + rt * 256, 256), lane * 4, 0, LD_AUX));
    WAIT_L0();
    BAR();
  }
  const rsrc_t ro = mk_rsrc(A.out + (size_t)(64 * rt) * 640, 64 * 1280);
  unsigned oa[2], oe[2], oq[2], og[2];
  float gq[2];
#pragma unroll
  for (int i = 0; i < 2; ++i) {
    const int it = i ? 512 + wave * 16 + (lane & 15) : tid;
    const int row = it / 10, gr = it - row * 10;
    oa[i] = (row * 84 + 8 * gr) * 4;
    oe[i] = (2 * RA + row * 52) * 4;
    oq[i] = oe[i] + (gr < 5 ? 8 * gr : 72 - 8 * gr) * 4;
    og[i] = (i && lane >= 16) ? 0x7fffffffu : row * 1280 + (gr < 5 ? 80 * ct : 1120 - 80 * ct) + 16 * gr;
    gq[i] = red[2 * P3_HALF / 4 + row];
    oa[i] |= (gr < 5) ? 0u : 0x80000000u;
    asm volatile("" : "+v"(oa[i]), "+v"(oe[i]), "+v"(oq[i]), "+v"(og[i]), "+v"(gq[i]));
  }
  WAIT_L0();
  BAR();
  TS(13);
#pragma unroll
  for (int i = 0; i < 2; ++i) {
    const bool lo = (int)oa[i] >= 0;
    lptr pa = sm + (oa[i] & 0x7fffffffu), pq = sm + oq[i];
    const f32x4 a0 = *(LDS_AS f32x4*)pa + *(LDS_AS f32x4*)(pa + RA * 4);
    const f32x4 a1 = *(LDS_AS f32x4*)(pa + 16) + *(LDS_AS f32x4*)(pa + RA * 4 + 16);
    const float yn = *(LDS_AS float*)(sm + oe[i] + (RE + 41) * 4);
    const f32x4 e0 = *(LDS_AS f32x4*)pq, e1 = *(LDS_AS f32x4*)(pq + 16), o0 = *(LDS_AS f32x4*)(pq + RE * 4), o1 = *(LDS_AS f32x4*)(pq + RE * 4 + 16);
    const float p8 = *(LDS_AS float*)(pq + 32) + *(LDS_AS float*)(pq + RE * 4 + 32);
    const f32x4 m0 = e0 - o0, m1 = e1 - o1, p0 = e0 + o0, p1 = e1 + o1;
    const f32x4 yy = {yn, -yn, yn, -yn};
    const f32x4 b0 = (lo ? m0 : (f32x4){p8, p1[3], p1[2], p1[1]}) + yy;
    const f32x4 b1 = (lo ? m1 : (f32x4){p1[0], p0[3], p0[2], p0[1]}) + yy;
    const f32x4 r0 = b0 + gq[i] * (a0 - b0), r1 = b1 + gq[i] * (a1 - b1);
    __builtin_amdgcn_raw_buffer_store_b128(__builtin_bit_cast(u32x4, pack8(r0, r1)), ro, og[i], 0, OUT_AUX);
  }
  TS(14);
}

#define TS_END()

__global__ __launch_bounds__(512, 2) void fused_kernel(Args A) {
  extern __shared__ __attribute__((aligned(16))) char smem[];
  lptr sm = (lptr)smem;
  TS_DECL;
  TS(0);
  {
    unsigned long long pc; unsigned len;
    asm volatile("s_getpc_b64 %0\n.Lfk_start:\n\ts_mov_b32 %1, .Lfk_end-.Lfk_start" : "=s"(pc), "=s"(len));
    pf_asm(mk_rsrc((const void*)pc, len), (unsigned)(size_t)sm + K1_JUNK + 1792, (threadIdx.x * 32 + (blockIdx.x >> 3)) * 128, 0);
  }
  const rsrc_t re = mk_rsrc(A.ws + WS_EPOCH, 4);
  const unsigned e = *(const __attribute__((address_space(4))) unsigned*)(A.ws + WS_EPOCH);
  f32x4 pq[2][5];
  phase1(A, sm, pq, e TS_PASS);
  phase2<true>(A, sm, e, pq TS_PASS);
  publish(A.ws, 2, e);
  p3_load_g(sm, __builtin_amdgcn_readfirstlane(threadIdx.x >> 6), threadIdx.x & 63);
  if (threadIdx.x < 448) WAIT_V(0);
  TS(10);
  await(A.ws, 2, e, 7, __builtin_amdgcn_readfirstlane(threadIdx.x >> 6), threadIdx.x & 63);
  phase3<true>(A, sm, e TS_PASS);
  if (blockIdx.x == 0 && threadIdx.x == 0) __builtin_amdgcn_raw_buffer_store_b32(e + 1u, re, 0, 0, 16);
  TS_END();
  asm volatile(".Lfk_end:");
}

extern "C" void kernel_launch(void* const* d_in, const int* in_sizes, int n_in, void* d_out, int out_size, void* d_ws,
                              size_t ws_size, hipStream_t stream) {
  Args A;
  A.x = (const u16*)d_in[0]; A.wqkv = (const u16*)d_in[1]; A.wo = (const u16*)d_in[2];
  A.wr = (const u16*)d_in[4]; A.wi = (const u16*)d_in[5]; A.gw = (const u16*)d_in[6];
  A.ws = (char*)d_ws + ((ws_size - (16u  __H0__ )) & ~(size_t)0x1fffff); A.out = (u16*)d_out;
  (void)hipFuncSetAttribute((const void*)fused_kernel, hipFuncAttributeMaxDynamicSharedMemorySize, LDS_ALL);
  fused_kernel<<<256, 512, LDS_ALL, stream>>>(A);
}
```

```cpp
#include <hip/hip_runtime.h>
#include <hip/hip_bf16.h>
#include <utility>

typedef unsigned short u16;
typedef __attribute__((ext_vector_type(8))) short bf16x8;
typedef __attribute__((ext_vector_type(4))) short s16x4;
typedef __attribute__((ext_vector_type(4))) float f32x4;
typedef __attribute__((ext_vector_type(2))) float f32x2;
typedef __attribute__((ext_vector_type(2))) __bf16 bf16x2_t;
typedef __attribute__((ext_vector_type(4))) unsigned u32x4;
typedef __attribute__((ext_vector_type(2))) unsigned u32x2;
#define LDS_AS __attribute__((address_space(3)))
#define DI __device__ __forceinline__
#define MFMA16(a, b, c) __builtin_amdgcn_mfma_f32_16x16x32_bf16((a), (b), (c), 0, 0, 0)
typedef LDS_AS char* lptr;
typedef __amdgpu_buffer_rsrc_t rsrc_t;

#define ST_AUX 16
#define OUT_AUX 0
#define K1_ROT 5
#define FUSED 1
#define POLL_SLEEP 2
#define G_EARLY 0
#define CODE_PF 1
#define TLB_STRIDE 0
#define LD_AUX 16

constexpr double kPi = 3.14159265358979323846;
constexpr double ccos_s(double x) { double x2 = x * x, t = 1, s = 1; for (int i = 1; i < 12; ++i) { t *= -x2 / ((2 * i - 1) * (2 * i)); s += t; } return s; }
constexpr double csin_s(double x) { double x2 = x * x, t = x, s = x; for (int i = 1; i < 12; ++i) { t *= -x2 / ((2 * i) * (2 * i + 1)); s += t; } return s; }
constexpr double cos640(int m) {
  m %= 640; if (m > 320) m = 640 - m;
  bool neg = false; if (m > 160) { m = 320 - m; neg = true; }
  double v = (m <= 80) ? ccos_s(2 * kPi * m / 640) : csin_s(2 * kPi * (160 - m) / 640);
  return neg ? -v : v;
}
constexpr u16 to_bf16c(double v) { float f = (float)v; unsigned u = __builtin_bit_cast(unsigned, f); u += 0x7fffu + ((u >> 16) & 1u); return (u16)(u >> 16); }
struct C640 { u16 v[640]; };
constexpr C640 make_c640() { C640 c{}; for (int i = 0; i < 640; ++i) c.v[i] = to_bf16c(cos640(i)); return c; }
constexpr C640 COS640 = make_c640();

struct alignas(128) FRow { u16 v[10 * 512]; };
constexpr FRow make_frow(int id) {
  FRow t{};
  const int cq = id / 10, st = id % 10;
  for (int par = 0; par < 2; ++par) for (int nt = 0; nt < 5; ++nt) for (int l = 0; l < 64; ++l) {
    const int kk = 80 * cq + 16 * nt + (l & 15);
    for (int j = 0; j < 8; ++j) {
      const int d = 64 * st + 32 * (j >> 2) + 8 * (l >> 4) + 2 * (j & 3) + par;
      const int m = (kk == 1) ? (d * 160 + 160 * par) % 640 : (d * (kk >> 1) + 160 * (kk & 1)) % 640;
      t.v[((par * 5 + nt) * 64 + l) * 8 + j] = COS640.v[m];
    }
  }
  return t;
}
template <int I> constexpr FRow FROW = make_frow(I);
struct alignas(128) FTab { FRow r[40]; };
template <int... I> constexpr FTab make_ftab(std::integer_sequence<int, I...>) { return FTab{{FROW<I>...}}; }
__device__ const FTab FT = make_ftab(std::make_integer_sequence<int, 40>{});

struct alignas(128) GRow { u16 v[10 * 3 * 512]; };
constexpr GRow make_grow(int id) {
  GRow t{};
  const int ct = id >> 1, od = id & 1;
  for (int ks = 0; ks < 10; ++ks) for (int nt = 0; nt < 3; ++nt) for (int l = 0; l < 64; ++l) {
    const int ci = 16 * nt + (l & 15), d = 40 * ct + ci;
    for (int j = 0; j < 8; ++j) {
      const int f = 32 * ks + 8 * (l >> 4) + j;
      t.v[((ks * 3 + nt) * 64 + l) * 8 + j] = (ci <= 40) ? COS640.v[(f * d + (od ? 160 : 0)) % 640] : (u16)((od && ci == 41 && f == 0) ? 0x3f80 : 0);
    }
  }
  return t;
}
template <int I> constexpr GRow GROW = make_grow(I);
struct alignas(128) GTab { GRow r[16]; };
template <int... I> constexpr GTab make_gtab(std::integer_sequence<int, I...>) { return GTab{{GROW<I>...}}; }
__device__ const GTab GT = make_gtab(std::make_integer_sequence<int, 16>{});

constexpr size_t WS_P   = 0;
constexpr size_t WS_Y   = WS_P + 2621440;
constexpr size_t WS_X1  = WS_Y + 2621440;
constexpr size_t WS_WT  = WS_X1 + 321 * 8192;
constexpr size_t WS_KVP = WS_WT + 352 * 8192;
constexpr size_t WS_M   = WS_KVP + 983040;
constexpr size_t WS_G   = WS_M + 819200;
constexpr size_t WS_ZP  = WS_G + 8192;
constexpr size_t WS_SYN = WS_ZP + 40960;

DI rsrc_t mk_rsrc(const void* p, unsigned bytes) { return __builtin_amdgcn_make_buffer_rsrc((void*)p, 0, bytes, 0x00020000); }
DI unsigned pk2(float a, float b) { f32x2 v = {a, b}; return __builtin_bit_cast(unsigned, __builtin_convertvector(v, bf16x2_t)); }
DI float bf_lo(unsigned u) { return __uint_as_float(u << 16); }
DI float bf_hi(unsigned u) { return __uint_as_float(u & 0xffff0000u); }
DI float dot2(unsigned a, unsigned b, float c) { return __builtin_amdgcn_fdot2_f32_bf16(__builtin_bit_cast(bf16x2_t, a), __builtin_bit_cast(bf16x2_t, b), c, false); }
DI float rbf(float a) { return bf_lo(pk2(a, a)); }
constexpr float kEps = 1e-6f, kL2E = 1.4426950408889634f;
DI float phi_f(float a) {
  float e;
  const float u = __builtin_fmaf(a, kL2E, -kEps * kL2E);
  asm("v_exp_f32_e64 %0, %1 clamp" : "=v"(e) : "v"(u));
  return __builtin_amdgcn_fmed3f(a, kEps, __builtin_inff()) + e;
}
DI bf16x8 pack8(const f32x4& a, const f32x4& b) {
  u32x4 p = {pk2(a[0], a[1]), pk2(a[2], a[3]), pk2(b[0], b[1]), pk2(b[2], b[3])};
  return __builtin_bit_cast(bf16x8, p);
}
DI u32x4 widen(u32x2 a, u32x2 b) {
  auto x = __builtin_amdgcn_permlane16_swap(a[0], b[0], false, false);
  auto y = __builtin_amdgcn_permlane16_swap(a[1], b[1], false, false);
  return (u32x4){x[0], y[0], x[1], y[1]};
}
#define WAIT_V_(n) asm volatile("s_waitcnt vmcnt(" #n ")" ::: "memory")
#define WAIT_V(n) WAIT_V_(n)
#define BC(v) __builtin_bit_cast(bf16x8, (v))
#define WAIT_L0() __builtin_amdgcn_s_waitcnt(0xC07F)
#define BAR() __builtin_amdgcn_s_barrier()
#define SCHED() __builtin_amdgcn_sched_barrier(0)
#define DMA16(rs, dst, vo, so) __builtin_amdgcn_raw_ptr_buffer_load_lds((rs), (LDS_AS void*)(dst), 16, (vo), (so), 0, 0)
#define TRREAD(p) __builtin_amdgcn_ds_read_tr16_b64_v4i16((LDS_AS s16x4*)(p))
#define ST16(v, rs, vo, so) __builtin_amdgcn_raw_buffer_store_b128((v), (rs), (vo), (so), ST_AUX)

#define TS_DECL
#define TS(i)
#define TC(i)
#define TS_FLUSH(kern)
#define TS_FLUSH2()
#define TS_ARG
#define TS_PASS

constexpr int K1_STAGE = 40960;
constexpr int K1_WF = 3 * K1_STAGE;
constexpr int K1_JUNK = K1_WF + 32768;
constexpr int K1_LDS = K1_JUNK + 2048;
#define K1_PF 1

struct Frags { bf16x8 a[2][2]; bf16x8 b[2][5]; };
struct K1Ctx { rsrc_t ra, rb; unsigned va[2], vb[3], ao[3][2], bo[3][5], ldsa, ldsb; int st, rs, wave; };

struct Half { s16x4 lo, hi; };
template <int I0, int I1> DI void k1_read(Frags& F, lptr sm, const unsigned (&ao)[2], const unsigned (&bo)[5]) {
#pragma unroll
  for (int i = I0; i < I1; ++i) {
    const int k = i / 7, j = i % 7;
    if (j < 2) F.a[k][j] = *(LDS_AS bf16x8*)(sm + ao[k] + j * 2048);
    else {
      const s16x4 lo = TRREAD(sm + bo[j - 2] + k * 12288), hi = TRREAD(sm + bo[j - 2] + k * 12288 + 512);
      F.b[k][j - 2] = __builtin_shufflevector(lo, hi, 0, 1, 2, 3, 4, 5, 6, 7);
    }
  }
}
DI void dma16_asm(rsrc_t rs, unsigned lds, unsigned vo, int so) {
  asm volatile("s_mov_b32 m0, %2\n\ts_nop 0\n\tbuffer_load_dwordx4 %0, %1, %3 offen lds" :: "v"(vo), "s"(rs), "s"(lds), "s"(so));
}
DI void pf_asm(rsrc_t rs, unsigned lds, unsigned vo, int so) {
  asm volatile("s_mov_b32 m0, %2\n\ts_nop 0\n\tbuffer_load_dword %0, %1, %3 offen lds" :: "v"(vo), "s"(rs), "s"(lds), "s"(so));
}
template <int BUF, int I> DI void k1_issue1(const K1Ctx& c) {
  if (I < 2) dma16_asm(c.ra, c.ldsa + BUF * K1_STAGE + I * 1024, c.va[I < 2 ? I : 0], c.st * 128);
  else dma16_asm(c.rb, c.ldsb + BUF * K1_STAGE + (I - 2) * 1024, c.vb[I < 2 ? 0 : I - 2], c.st * 64 * c.rs);
}
template <int BUF> DI void k1_issue(K1Ctx& c) {
  k1_issue1<BUF, 0>(c); k1_issue1<BUF, 1>(c); k1_issue1<BUF, 2>(c); k1_issue1<BUF, 3>(c); k1_issue1<BUF, 4>(c);
  c.st = (c.st == 9) ? 0 : c.st + 1;
}
template <bool SWAP, int I0, int I1> DI void k1_mma(f32x4 (&acc)[2][5], const Frags& F) {
#pragma unroll
  for (int i = I0; i < I1; ++i) {
    const int k = i / 10, mt = (i / 5) & 1, nt = i % 5;
    acc[mt][nt] = SWAP ? MFMA16(F.b[k][nt], F.a[k][mt], acc[mt][nt]) : MFMA16(F.a[k][mt], F.b[k][nt], acc[mt][nt]);
  }
}
template <bool SWAP, int N, int SEG> DI void k1_seg(f32x4 (&acc)[2][5], Frags& Fn, const Frags& Fp, lptr sm, K1Ctx& c) {
  constexpr int R0[6] = {0, 4, 7, 10, 12, 14};
  k1_read<R0[SEG], R0[SEG + 1]>(Fn, sm, c.ao[N % 3], c.bo[N % 3]);
  if (N > 0) {
    k1_mma<SWAP, 4 * SEG, 4 * SEG + 4>(acc, Fp);
    if (SEG < 2) {
      __builtin_amdgcn_sched_group_barrier(0x008, 1, 0); __builtin_amdgcn_sched_group_barrier(0x100, 2, 0);
      __builtin_amdgcn_sched_group_barrier(0x008, 1, 0); __builtin_amdgcn_sched_group_barrier(0x100, 2, 0);
      __builtin_amdgcn_sched_group_barrier(0x008, 1, 0); __builtin_amdgcn_sched_group_barrier(0x100, 2, 0);
      __builtin_amdgcn_sched_group_barrier(0x008, 1, 0);
    } else {
#pragma unroll
      for (int i = 0; i < 4; ++i) { __builtin_amdgcn_sched_group_barrier(0x008, 1, 0); __builtin_amdgcn_sched_group_barrier(0x100, 2, 0); }
    }
  }
  SCHED();
  if (N + 2 <= 9) k1_issue1<(N + 2) % 3, SEG>(c);
  SCHED();
}
template <bool SWAP, int N> DI void k1_step(f32x4 (&acc)[2][5], Frags& Fn, const Frags& Fp, lptr sm, K1Ctx& c) {
  WAIT_L0();
  BAR();
  SCHED();
  k1_seg<SWAP, N, 0>(acc, Fn, Fp, sm, c);
  k1_seg<SWAP, N, 1>(acc, Fn, Fp, sm, c);
  k1_seg<SWAP, N, 2>(acc, Fn, Fp, sm, c);
  k1_seg<SWAP, N, 3>(acc, Fn, Fp, sm, c);
  k1_seg<SWAP, N, 4>(acc, Fn, Fp, sm, c);
  c.st = (c.st == 9) ? 0 : c.st + 1;
}

struct DFrags { u32x4 a[2][2]; bf16x8 b[2][3]; };
constexpr int KD_STAGE = 26624;
template <int BUF, int I> DI void kd_issue1(const K1Ctx& c) {
  if (I < 2) dma16_asm(c.ra, c.ldsa + BUF * KD_STAGE + I * 1024, c.va[I & 1], c.st * 128);
  else if (I == 2) dma16_asm(c.rb, c.ldsb + BUF * KD_STAGE, c.vb[0], c.st * 10240);
  else dma16_asm(c.rb, c.wave < 2 ? c.ldsb + BUF * KD_STAGE + 8192 : c.ldsa - 2048 * c.wave + K1_JUNK, c.vb[1], c.st * 10240);
}
template <int BUF> DI void kd_issue(K1Ctx& c) {
  kd_issue1<BUF, 0>(c); kd_issue1<BUF, 2>(c); kd_issue1<BUF, 1>(c); kd_issue1<BUF, 3>(c);
  c.st = (c.st == 9) ? 0 : c.st + 1;
}
template <int NT> DI void kd_mma(f32x4 (&acc)[2][2][3], const DFrags& F) {
#pragma unroll
  for (int mt = 0; mt < 2; ++mt) {
    u32x4 ev, od;
#pragma unroll
    for (int k = 0; k < 2; ++k)
#pragma unroll
      for (int i = 0; i < 2; ++i) {
        ev[2 * k + i] = __builtin_amdgcn_perm(F.a[mt][k][2 * i + 1], F.a[mt][k][2 * i], 0x05040100u);
        od[2 * k + i] = __builtin_amdgcn_perm(F.a[mt][k][2 * i + 1], F.a[mt][k][2 * i], 0x07060302u);
      }
#pragma unroll
    for (int nt = 0; nt < NT; ++nt) {
      acc[0][mt][nt] = MFMA16(F.b[0][nt], BC(ev), acc[0][mt][nt]);
      acc[1][mt][nt] = MFMA16(F.b[1][nt], BC(od), acc[1][mt][nt]);
    }
  }
}
template <int NT, int N> DI void kd_step(f32x4 (&acc)[2][2][3], DFrags& Fn, const DFrags& Fp, lptr sm, K1Ctx& c) {
  WAIT_L0();
  BAR();
  SCHED();
  if (N + 3 <= 9) kd_issue<(N + 3) % 4>(c);
  SCHED();
  constexpr int H = (N % 4) >> 1, O = (N & 1) * KD_STAGE;
#pragma unroll
  for (int mt = 0; mt < 2; ++mt)
#pragma unroll
    for (int k = 0; k < 2; ++k) Fn.a[mt][k] = *(LDS_AS u32x4*)(sm + c.ao[H][k] + O + mt * 2048);
#pragma unroll
  for (int par = 0; par < 2; ++par)
#pragma unroll
    for (int nt = 0; nt < NT; ++nt) Fn.b[par][nt] = *(LDS_AS bf16x8*)(sm + c.bo[H][0] + O + (par * 5 + nt) * 1024);
  if (N > 0) {
    kd_mma<NT>(acc, Fp);
    __builtin_amdgcn_sched_group_barrier(0x100, 2, 0);
    __builtin_amdgcn_sched_group_barrier(0x002, 8, 0);
#pragma unroll
    for (int i = 0; i < 2 * NT; ++i) { __builtin_amdgcn_sched_group_barrier(0x008, 1, 0); __builtin_amdgcn_sched_group_barrier(0x100, 1, 0); __builtin_amdgcn_sched_group_barrier(0x002, 2, 0); }
  }
  SCHED();
}

DI u32x2 make_tag(unsigned e) { return (u32x2){e + 1u, (e + 1u) * 0x9E3779B9u ^ 0x5bd1e995u}; }
DI int flag_slot(int b) { return ((b & 7) >> 1) * 64 + (b & 1) * 32 + (b >> 3); }
constexpr size_t WS_EPOCH = WS_SYN + 3 * 65536;
DI void flag_store(char* ws, int which, unsigned e, int lane) {
  __builtin_amdgcn_raw_buffer_store_b64(make_tag(e), mk_rsrc(ws + WS_SYN + which * 65536, 65536), lane * 2048 + flag_slot(blockIdx.x) * 8, 0, 16);
}
template <int NT, class FL, class FS> DI void kd_main(char* ws, lptr sm, K1Ctx& c, int rt, int cq, unsigned e, int lane, FL filt_load, FS filt_store TS_ARG) {
  const int wm = c.wave & 3, r = lane & 15, g = lane >> 4;
  constexpr int T0 = NT == 3 ? 0 : 3;
  DFrags D0, D1;
  f32x4 acc[2][2][3];
#pragma unroll
  for (int u = 0; u < 2; ++u) {
    c.ao[u][0] = 2 * u * KD_STAGE + (32 * wm + r) * 128 + ((g ^ (r >> 1)) * 16);
    c.ao[u][1] = 2 * u * KD_STAGE + (32 * wm + r) * 128 + (((4 + g) ^ (r >> 1)) * 16);
    c.bo[u][0] = 2 * u * KD_STAGE + 16384 + T0 * 1024 + lane * 16;
    asm volatile("" : "+v"(c.ao[u][0]), "+v"(c.ao[u][1]), "+v"(c.bo[u][0]));
  }
#pragma unroll
  for (int i = 0; i < 4 * NT; ++i) { acc[i / (2 * NT)][(i / NT) & 1][i % NT] = (f32x4){0.f, 0.f, 0.f, 0.f}; asm volatile("" : "+v"(acc[i / (2 * NT)][(i / NT) & 1][i % NT])); }
  TS(1);
  WAIT_V(8); kd_step<NT, 0>(acc, D0, D1, sm, c);
  TS(2);
  WAIT_V(8); kd_step<NT, 1>(acc, D1, D0, sm, c);
  filt_load();
  WAIT_V(13); kd_step<NT, 2>(acc, D0, D1, sm, c);
  WAIT_V(13); kd_step<NT, 3>(acc, D1, D0, sm, c);
  WAIT_V(13); kd_step<NT, 4>(acc, D0, D1, sm, c);
  WAIT_V(8); kd_step<NT, 5>(acc, D1, D0, sm, c);
  WAIT_V(8); kd_step<NT, 6>(acc, D0, D1, sm, c);
  filt_store();
  WAIT_V(11); kd_step<NT, 7>(acc, D1, D0, sm, c);
  WAIT_V(7); kd_step<NT, 8>(acc, D0, D1, sm, c);
  WAIT_V(3); kd_step<NT, 9>(acc, D1, D0, sm, c);
  kd_mma<NT>(acc, D1);
  TS(3);
  const rsrc_t rx = mk_rsrc(ws + WS_X1 + (wm >> 1) * 4096 + (rt >> 2) * 1024 + (rt & 3) * 256 + (wm & 1) * 128, 321 * 8192);
  const bool up = r >= 8;
  const int fb = 40 * cq + 8 * T0 + 2 * g;
  const bool sp = (fb == 0);
#pragma unroll
  for (int nt = 0; nt < NT; ++nt) {
    unsigned it[4][2];
#pragma unroll
    for (int mt = 0; mt < 2; ++mt) {
      const f32x4 E = acc[0][mt][nt], O = acc[1][mt][nt], P = E + O, M = E - O;
      it[0][mt] = pk2(P[0], P[1]); it[1][mt] = pk2(P[2], P[3]); it[2][mt] = pk2(M[0], -M[1]); it[3][mt] = pk2(M[2], -M[3]);
      if (NT == 3 && nt == 0) {
        it[0][mt] = sp ? (it[0][mt] & 0xffffu) : it[0][mt]; it[2][mt] = sp ? (it[2][mt] & 0xffffu) : it[2][mt];
        __builtin_amdgcn_raw_buffer_store_b32(pk2(E[1], O[1]), rx, sp ? 160 * 8192 + (r & 7) * 16 + (r >> 3) * 8 + mt * 4 : 0x7fffffff, 0, ST_AUX);
      }
    }
#pragma unroll
    for (int h = 0; h < 2; ++h) {
      const unsigned t0 = __builtin_amdgcn_update_dpp(0u, up ? it[2 * h][0] : it[2 * h + 1][0], 0x128, 0xf, 0xf, false);
      const unsigned t1 = __builtin_amdgcn_update_dpp(0u, up ? it[2 * h][1] : it[2 * h + 1][1], 0x128, 0xf, 0xf, false);
      const u32x4 o = {up ? t0 : it[2 * h][0], up ? t1 : it[2 * h][1], up ? it[2 * h + 1][0] : t0, up ? it[2 * h + 1][1] : t1};
      const int f0 = fb + 8 * nt + (r >> 3);
      ST16(o, rx, (h ? 320 - f0 : f0) * 8192 + (r & 7) * 16, 0);
    }
  }
}

struct Args { const u16 *x, *wqkv, *wo, *wr, *wi, *gw; char* ws; u16* out; };

DI void phase1(const Args& A, lptr sm, f32x4 (&pq)[2][5], unsigned e TS_ARG) {
  const u16* x = A.x; const u16* wqkv = A.wqkv; const u16* wr = A.wr; const u16* wi = A.wi; char* ws = A.ws;
  const int tid = threadIdx.x, lane = tid & 63;
  const int wave = __builtin_amdgcn_readfirstlane(tid >> 6);
  const int b = blockIdx.x;
  const int xcd = b & 7, idx = b >> 3;
  const int rt = (xcd & 1) * 8 + (idx & 7);
  const int ct = (xcd >> 1) * 4 + (idx >> 3);
  const bool dft = ct >= 12, iskv = ct >= 4 && ct < 12;
  const int c0 = iskv ? 80 * (ct - 4) : 160 * (ct & 3);
  const int L0 = c0 >> 6, p0 = (c0 & 63) >> 4;
  const int xs = (p0 == 0 || p0 == 3) ? 1 : 2;
  const int bigB = p0 >= 2 ? 1 : 0;

  K1Ctx c;
  c.wave = wave;
  c.ra = mk_rsrc(x, 2048 * 1280);
  c.rb = dft ? mk_rsrc((const char*)&FT + (ct & 3) * 102400, 102400) : mk_rsrc(wqkv, 640 * 3840);
  c.rs = 3840;
  auto grow = [&](int row) { return dft ? 32 * (4 * rt + 2 * ((row >> 3) & 1) + ((row >> 4) & 1)) + 8 * (row >> 5) + (row & 7) : 128 * rt + row; };
#pragma unroll
  for (int i = 0; i < 2; ++i) {
    const int row = 16 * wave + 8 * i + (lane >> 3);
    c.va[i] = grow(row) * 1280 + ((lane & 7) ^ ((row >> 1) & 7)) * 16;
  }
  c.ldsa = (unsigned)(size_t)sm + 2 * wave * 1024;
  c.st = (((((idx & 7) * 5 + (idx >> 3) * 10) >> 2) % 10) * K1_ROT / 10) * (10 / K1_ROT);
  k1_issue1<0, 0>(c); k1_issue1<0, 1>(c);
  SCHED();
  {
    const int row = lane >> 3, hf = lane & 1;
    const int pair = ((lane & 7) >> 1) ^ (((row >> 1) & 1) | ((wave & 1) << 1));
    const unsigned rowb = (8 * wave + row) * c.rs;
    const bool kown = bigB ? (pair >= p0) : (pair <= p0);
    c.vb[0] = rowb + (iskv ? 1280 + (L0 + bigB) * 128 : L0 * 128) + pair * 32 + hf * 16;
    c.vb[1] = rowb + (iskv ? 2560 + (L0 + bigB) * 128 + (hf ^ 1) * 16 : (L0 + 1) * 128 + hf * 16) + pair * 32;
    c.vb[2] = rowb + (iskv ? (L0 + 1 - bigB) * 128 + (kown ? 1280 + pair * 32 + hf * 16 : 2560 + (pair ^ xs) * 32 + (hf ^ 1) * 16)
                           : (L0 + 2) * 128 + pair * 32 + hf * 16);
  }
  c.ldsb = (unsigned)(size_t)sm + 16384 + (dft ? 1 : 3) * wave * 1024;
  if (dft) { c.vb[0] = wave * 1024 + lane * 16; c.vb[1] = wave < 2 ? c.vb[0] + 8192 : 0x7fffffffu; }
  if (dft) { kd_issue1<0, 2>(c); kd_issue1<0, 3>(c); }
  else { k1_issue1<0, 2>(c); k1_issue1<0, 3>(c); k1_issue1<0, 4>(c); }
  c.st = (c.st == 9) ? 0 : c.st + 1;
  BAR();
  if (dft) { kd_issue<1>(c); kd_issue<2>(c); }
  else k1_issue<1>(c);

  const int wm = wave >> 1, wn = wave & 1;
  const int r = lane & 15, g = lane >> 4, q = (lane & 15) >> 2, p = lane & 3;
  const int hsw = ((q >> 1) & 1) | ((g & 1) << 1);
  const unsigned bbase = 16384 + g * 3072 + q * 128;
  c.ao[0][0] = (32 * wm + r) * 128 + ((g ^ (r >> 1)) * 16);
  c.ao[0][1] = (32 * wm + r) * 128 + (((4 + g) ^ (r >> 1)) * 16);
  f32x4 acc[2][5];
  Frags F0, F1;
  const rsrc_t rpf = wave == 0 ? mk_rsrc(A.wo, dft ? 0 : 819200) : wave == 1 ? mk_rsrc(&GT, dft ? 0 : 491520)
                   : wave == 2 ? mk_rsrc(ws, (unsigned)WS_EPOCH + 4) : mk_rsrc(A.out, wave == 3 ? 2621440 : 0);
  const unsigned vpf = wave >= 2 ? (TLB_STRIDE ? (lane * 32 + idx) * TLB_STRIDE : 0x7fffffffu)
                     : (lane < (wave ? 20 : 34)) ? ((ct * 16 + rt) * (wave ? 20 : 34) + lane) * 128 : 0x7fffffffu;

#define K1_HEAD(SWAP, HOOK, W2)                                                                          \
  _Pragma("unroll") for (int u = 1; u < 3; ++u) {                                              \
    _Pragma("unroll") for (int k = 0; k < 2; ++k) { c.ao[u][k] = c.ao[0][k] + u * K1_STAGE; asm volatile("" : "+v"(c.ao[u][k])); }     \
    _Pragma("unroll") for (int k = 0; k < 5; ++k) { c.bo[u][k] = c.bo[0][k] + u * K1_STAGE; asm volatile("" : "+v"(c.bo[u][k])); }     \
  }                                                                                            \
  _Pragma("unroll") for (int mt = 0; mt < 2; ++mt)                                             \
    _Pragma("unroll") for (int nt = 0; nt < 5; ++nt) asm volatile("" : "+v"(acc[mt][nt]));     \
  TS(1);                                                                                       \
  WAIT_V(5); k1_step<SWAP, 0>(acc, F0, F1, sm, c);                                           \
  TS(2);                                                                                       \
  WAIT_V(5); k1_step<SWAP, 1>(acc, F1, F0, sm, c); HOOK;                                             \
  WAIT_V(W2); k1_step<SWAP, 2>(acc, F0, F1, sm, c);                                             \
  pf_asm(rpf, (unsigned)(size_t)sm + K1_JUNK + wave * 256, vpf, 0);

  if (iskv) {
#pragma unroll
    for (int nt = 0; nt < 5; ++nt) {
      const int u = 4 * nt + p;
      const int isv = u >= 10 ? 1 : 0;
      const int pc = 4 * p0 + 10 * wn + u - 10 * isv;
      const int gp = pc >> 2, big = ((gp >> 2) == bigB);
      const int slot = (gp & 3) ^ ((!big && isv) ? xs : 0);
      c.bo[0][nt] = bbase + (big ? isv : 2) * 1024 + ((slot ^ hsw) * 32) + (((pc & 3) ^ (isv << 1)) * 8);
      const float i0 = ((nt < 2) || (nt == 2 && r < 8)) ? kEps : 0.f;
#pragma unroll
      for (int mt = 0; mt < 2; ++mt) acc[mt][nt] = (f32x4){i0, i0, i0, i0};
    }
    unsigned ko[10];
    {
      constexpr int TI[10] = {0, 0, 1, 1, 1, 2, 2, 0, 1, 2}, TJ[10] = {2, 3, 2, 3, 4, 3, 4, -1, -1, -1};
#pragma unroll
      for (int i = 0; i < 10; ++i) {
        const int ti = TI[i], tj = TJ[i];
        const int ur = 16 * ti + r, hk = ur >= 20 ? 1 : 0;
        const int uc = 16 * tj + 4 * g - 40, hv = uc >= 20 ? 1 : 0;
        const bool valid = tj < 0 ? (ur < 40 && g == 0) : (ur < 40 && uc >= 0 && hk == hv);
        const int off = wave * 3840 + hk * 1920 + (ur - 20 * hk) * 96 + (tj < 0 ? 80 : (uc - 20 * hv) * 4);
        ko[i] = K1_STAGE + (valid ? off : 8 * 3840 + lane * 16);
        asm volatile("" : "+v"(ko[i]));
      }
    }
    K1_HEAD(false, , 5)
    WAIT_V(6); k1_step<false, 3>(acc, F1, F0, sm, c);
    WAIT_V(6); k1_step<false, 4>(acc, F0, F1, sm, c);
    WAIT_V(5); k1_step<false, 5>(acc, F1, F0, sm, c);
    WAIT_V(5); k1_step<false, 6>(acc, F0, F1, sm, c);
    WAIT_V(5); k1_step<false, 7>(acc, F1, F0, sm, c);
    WAIT_V(5); k1_step<false, 8>(acc, F0, F1, sm, c);
    WAIT_V(0); k1_step<false, 9>(acc, F1, F0, sm, c);
    k1_mma<false, 0, 20>(acc, F1);
    TS(3);
    bf16x8 fr[5];
#pragma unroll
    for (int nt = 0; nt < 5; ++nt) {
#pragma unroll
      for (int m = 0; m < 2; ++m)
#pragma unroll
        for (int j = 0; j < 4; ++j) {
          if (nt < 2) acc[m][nt][j] = phi_f(acc[m][nt][j]);
          if (nt == 2) { const float ph = phi_f(acc[m][nt][j]); acc[m][nt][j] = (r < 8) ? ph : acc[m][nt][j]; }
        }
      fr[nt] = pack8(acc[0][nt], acc[1][nt]);
    }
    const bf16x8 ones = {0x3f80, 0x3f80, 0x3f80, 0x3f80, 0x3f80, 0x3f80, 0x3f80, 0x3f80};
    {
      constexpr int TI[10] = {0, 0, 1, 1, 1, 2, 2, 0, 1, 2}, TJ[10] = {2, 3, 2, 3, 4, 3, 4, -1, -1, -1};
      const f32x4 zz = {0.f, 0.f, 0.f, 0.f};
      f32x4 d[10];
#pragma unroll
      for (int i = 0; i < 10; ++i) d[i] = MFMA16(TJ[i] < 0 ? ones : fr[TJ[i] < 0 ? 0 : TJ[i]], fr[TI[i]], zz);
#pragma unroll
      for (int i = 0; i < 10; ++i) *(LDS_AS f32x4*)(sm + ko[i]) = d[i];
    }
    WAIT_L0();
    BAR();
    const rsrc_t rk = mk_rsrc(ws + WS_KVP + ((size_t)rt * 32 + 4 * (ct - 4)) * 1920, 7680);
    LDS_AS f32x4* src = (LDS_AS f32x4*)(sm + K1_STAGE);
    const f32x4 s0 = (src[tid] + src[tid + 480]) + (src[tid + 960] + src[tid + 1440]);
    ST16(__builtin_bit_cast(u32x4, s0), rk, tid * 16, 0);
    if (wave >= 6) {
      LDS_AS float* zs = (LDS_AS float*)(sm + K1_STAGE) + min(tid - 384, 79) * 24 + 20;
      const float z = (zs[0] + zs[1920]) + (zs[3840] + zs[5760]);
      __builtin_amdgcn_raw_buffer_store_b32(__float_as_uint(z), mk_rsrc(ws + WS_ZP + rt * 2560 + (ct - 4) * 320, 320), (tid - 384) * 4, 0, ST_AUX);
    }
  } else {
    const int un = (dft ? 64 : 0) + rt * 4 + (ct & 3), mo = un >> 1, fh = un & 1;
    auto filt_load = [&]() {
      const rsrc_t rf = mk_rsrc((wave >> 2) ? wi : wr, 2048 * 642);
      const int kb = 16 * (mo >> 4) + 8 * (mo & 1) + 64 * (16 * ((mo >> 3) & 1) + 4 * ((mo >> 1) & 3) + (wave & 3));
      const unsigned v0 = (lane & 31) < 21 ? (lane >> 5) * 640 + (lane & 31) * 16 : 0x7fffffffu;
#pragma unroll
      for (int i = 0; i < 4; ++i)
        dma16_asm(rf, (unsigned)(size_t)sm + K1_WF + (wave * 4 + i) * 1024, v0, kb * 642 + 320 * fh + i * 1280);
    };
    auto filt_store = [&]() {
      const rsrc_t rw = mk_rsrc(ws + WS_WT + (size_t)fh * 160 * 8192 + mo * 128, (160 + fh) * 8192 - mo * 128);
      const int i8 = lane & 7;
#pragma unroll
      for (int rd = 0; rd < 3; ++rd) {
        const int fl = (rd * 8 + wave) * 8 + (lane >> 3);
        lptr src = sm + K1_WF + i8 * 514 + min(fl, 160) * 2;
        u32x4 o;
#pragma unroll
        for (int j = 0; j < 4; ++j) o[j] = (unsigned)*(LDS_AS u16*)(src + j * 4096) | ((unsigned)*(LDS_AS u16*)(src + 16384 + j * 4096) << 16);
        ST16(o, rw, fl * 8192 + i8 * 16, 0);
      }
      SCHED();
    };
    if (!dft) {
#pragma unroll
      for (int nt = 0; nt < 5; ++nt) {
        const int gp = p0 + 5 * wn + nt;
        c.bo[0][nt] = bbase + (gp >> 2) * 1024 + (((gp & 3) ^ hsw) * 32) + p * 8;
      }
#pragma unroll
      for (int mt = 0; mt < 2; ++mt)
#pragma unroll
        for (int nt = 0; nt < 5; ++nt) acc[mt][nt] = (f32x4){kEps, kEps, kEps, kEps};
      K1_HEAD(true, filt_load(), 9)
      WAIT_V(10); k1_step<true, 3>(acc, F1, F0, sm, c);
      WAIT_V(6); k1_step<true, 4>(acc, F0, F1, sm, c);
      filt_store();
      WAIT_V(8); k1_step<true, 5>(acc, F1, F0, sm, c);
      WAIT_V(8); k1_step<true, 6>(acc, F0, F1, sm, c);
      WAIT_V(5); k1_step<true, 7>(acc, F1, F0, sm, c);
      if (wave == 0) flag_store(ws, 1, e, lane);
      WAIT_V(5); k1_step<true, 8>(acc, F0, F1, sm, c);
      WAIT_V(0); k1_step<true, 9>(acc, F1, F0, sm, c);
      k1_mma<true, 0, 20>(acc, F1);
      TS(3);
#pragma unroll
      for (int mt = 0; mt < 2; ++mt)
#pragma unroll
        for (int nt = 0; nt < 5; ++nt)
#pragma unroll
          for (int j = 0; j < 4; ++j) pq[mt][nt][j] = phi_f(acc[mt][nt][j]);
    } else {
      auto filt_load2 = [&]() { filt_load(); pf_asm(rpf, (unsigned)(size_t)sm + K1_JUNK + 1024 + (wave & 3) * 256, vpf, 0); };
      if (wave < 4) kd_main<3>(ws, sm, c, rt, ct & 3, e, lane, filt_load2, filt_store TS_PASS);
      else kd_main<2>(ws, sm, c, rt, ct & 3, e, lane, filt_load2, filt_store TS_PASS);
    }
  }
  TS(4);
}

DI void publish(char* ws, int which, unsigned e) {
  WAIT_V(0);
  BAR();
  if (threadIdx.x < 64) flag_store(ws, which, e, threadIdx.x);
}
DI bool poll(char* ws, int which, unsigned e, int need, int lane, unsigned tries) {
  {
    const rsrc_t rf = mk_rsrc(ws + WS_SYN + which * 65536 + (blockIdx.x & 31) * 2048, 2048);
    const u32x2 t = make_tag(e);
    bool n0 = (need >> (lane < 32 ? 0 : 1)) & 1, n1 = (need >> (lane < 32 ? 1 : 2)) & 1;
    for (unsigned spin = 0; spin < tries; ++spin) {
      const u32x4 f0 = __builtin_amdgcn_raw_buffer_load_b128(rf, n0 ? lane * 16 : 0x7fffffffu, 0, 16);
      const u32x4 f1 = __builtin_amdgcn_raw_buffer_load_b128(rf, n1 ? lane * 16 + 1024 : 0x7fffffffu, 0, 16);
      n0 = n0 && !(f0[0] == t[0] && f0[1] == t[1] && f0[2] == t[0] && f0[3] == t[1]);
      n1 = n1 && !(f1[0] == t[0] && f1[1] == t[1] && f1[2] == t[0] && f1[3] == t[1]);
      if (!__any(n0 || n1)) return true;
      __builtin_amdgcn_s_sleep(POLL_SLEEP);
    }
  }
  return false;
}
DI void await(char* ws, int which, unsigned e, int need, int wave, int lane) {
  if (wave == 7) poll(ws, which, e, need, lane, 1u << 17);
  asm volatile("" ::: "memory");
  BAR();
  asm volatile("" ::: "memory");
}
DI void dma16_sc1(rsrc_t rs, unsigned lds, unsigned vo, int so) {
  asm volatile("s_mov_b32 m0, %2\n\ts_nop 0\n\tbuffer_load_dwordx4 %0, %1, %3 offen sc1 lds" :: "v"(vo), "s"(rs), "s"(lds), "s"(so));
}
#define LD16(rs, vo, so) __builtin_amdgcn_raw_buffer_load_b128((rs), (vo), (so), LD_AUX)

constexpr int P3_HALF = 71680;

DI void p3_load_g(lptr sm, int wave, int lane) {
  const int b = blockIdx.x, ct = ((b & 7) >> 2) * 4 + (b >> 6);
  const rsrc_t rg = mk_rsrc((const char*)&GT + (size_t)(2 * ct) * 30720, 61440);
#pragma unroll
  for (int i = 0; i < 9; ++i) {
    const int n = min(wave * 9 + i, 59);
    if (wave < 7)
    dma16_asm(rg, (unsigned)(size_t)sm + (n >= 30 ? P3_HALF + 40960 - 30720 : 40960) + n * 1024, lane * 16, n * 1024);
  }
}

struct FftC {
  const u32x4 (&fre)[4]; const u32x4 (&fim)[4]; const u32x4 (&fc)[2]; const u32x4 (&fs)[2]; const u32x4 (&fn)[2];
  const f32x2 (&tc)[2][2]; const f32x2 (&tsn)[2][2]; const unsigned (&uo)[2][4]; const unsigned (&ro)[4]; unsigned vy;
};
DI f32x2 half2(const f32x4& v, int h) { return h ? (f32x2){v[2], v[3]} : (f32x2){v[0], v[1]}; }
constexpr int FSECOND = 304;
template <int NC> DI void fft_run(char* ws, unsigned e, lptr sm, const FftC& T, int fu0, int wave8, int lane TS_ARG) {
  const int wave = wave8 & 3;
  const unsigned smu = (unsigned)(size_t)sm;
  const f32x4 z4 = {0.f, 0.f, 0.f, 0.f};
  await(ws, 1, e, 5, wave8, lane);
  TS(7);
  u32x4 wv[NC][2];
#pragma unroll
  for (int c = 0; c < NC; ++c) {
    const rsrc_t rx = mk_rsrc(ws + WS_X1 + (size_t)min((c ? FSECOND + (fu0 >> 1) : fu0), 320) * 8192, 8192);
    dma16_sc1(rx, smu + c * 16384 + wave * 2048, lane * 16, wave * 2048);
    dma16_sc1(rx, smu + c * 16384 + wave * 2048 + 1024, lane * 16, wave * 2048 + 1024);
  }
#pragma unroll
  for (int c = 0; c < NC; ++c) {
    const rsrc_t rw = mk_rsrc(ws + WS_WT + (size_t)min((c ? FSECOND + (fu0 >> 1) : fu0), 320) * 8192, 8192);
    wv[c][0] = LD16(rw, lane * 16, wave * 2048);
    wv[c][1] = LD16(rw, lane * 16 + 1024, wave * 2048);
  }
  SCHED();
  WAIT_V(0);
#pragma unroll
  for (int c = 0; c < NC; ++c) asm volatile("" : "+v"(wv[c][0]), "+v"(wv[c][1]));
  BAR();
  SCHED();
  TS(8);
  f32x2 wre[NC][2][2], wim[NC][2][2];
#pragma unroll
  for (int c = 0; c < NC; ++c) {
    const int f = min((c ? FSECOND + (fu0 >> 1) : fu0), 320);
    const float sc = ((f == 0 || f == 320) ? 1.f : 2.f) / (2048.f * 640.f);
#pragma unroll
    for (int t = 0; t < 2; ++t)
#pragma unroll
      for (int h = 0; h < 2; ++h) {
        wre[c][t][h] = (f32x2){bf_lo(wv[c][t][2 * h]), bf_lo(wv[c][t][2 * h + 1])} * sc;
        wim[c][t][h] = (f32x2){bf_hi(wv[c][t][2 * h]), bf_hi(wv[c][t][2 * h + 1])} * sc;
      }
  }
#define FOR_C _Pragma("unroll") for (int c = 0; c < NC; ++c)
#define FOR_T _Pragma("unroll") for (int t = 0; t < 2; ++t)
#define FOR_H _Pragma("unroll") for (int h = 0; h < 2; ++h)
#define FOR_K _Pragma("unroll") for (int ks = 0; ks < 4; ++ks)
  f32x4 re[NC][2], im[NC][2];
  u32x4 bre[NC], bim[NC];
  {
    bf16x8 xa[NC][2][4];
    FOR_K FOR_C FOR_T xa[c][t][ks] = *(LDS_AS bf16x8*)(sm + c * 16384 + lane * 16 + (t * 4 + ks) * 1024);
    FOR_K FOR_C FOR_T {
      re[c][t] = MFMA16(xa[c][t][ks], BC(T.fre[ks]), ks ? re[c][t] : z4);
      im[c][t] = MFMA16(xa[c][t][ks], BC(T.fim[ks]), ks ? im[c][t] : z4);
    }
  }
  FOR_C FOR_T FOR_H {
    const f32x2 a = half2(re[c][t], h), d = half2(im[c][t], h);
    const f32x2 x = a * T.tc[t][h] + d * T.tsn[t][h], y = d * T.tc[t][h] - a * T.tsn[t][h];
    bre[c][2 * t + h] = pk2(x[0], x[1]); bim[c][2 * t + h] = pk2(y[0], y[1]);
  }
  FOR_C FOR_T { re[c][t] = MFMA16(BC(T.fc[t]), BC(bre[c]), z4); im[c][t] = MFMA16(BC(T.fc[t]), BC(bim[c]), z4); }
  FOR_C FOR_T { re[c][t] = MFMA16(BC(T.fs[t]), BC(bim[c]), re[c][t]); im[c][t] = MFMA16(BC(T.fn[t]), BC(bre[c]), im[c][t]); }
  FOR_C FOR_T FOR_H {
    const f32x2 a = half2(re[c][t], h), d = half2(im[c][t], h);
    const f32x2 x = a * wre[c][t][h] - d * wim[c][t][h], y = a * wim[c][t][h] + d * wre[c][t][h];
    bre[c][2 * t + h] = pk2(x[0], x[1]); bim[c][2 * t + h] = pk2(y[0], y[1]);
  }
  FOR_C FOR_T { re[c][t] = MFMA16(BC(T.fc[t]), BC(bre[c]), z4); im[c][t] = MFMA16(BC(T.fc[t]), BC(bim[c]), z4); }
  FOR_C FOR_T { re[c][t] = MFMA16(BC(T.fn[t]), BC(bim[c]), re[c][t]); im[c][t] = MFMA16(BC(T.fs[t]), BC(bre[c]), im[c][t]); }
  FOR_C FOR_T FOR_H {
    const f32x2 a = half2(re[c][t], h), d = half2(im[c][t], h);
    const f32x2 x = a * T.tc[t][h] - d * T.tsn[t][h], y = -(d * T.tc[t][h]) - a * T.tsn[t][h];
    *(LDS_AS unsigned*)(sm + c * 16384 + T.uo[t][2 * h]) = pk2(x[0], y[0]);
    *(LDS_AS unsigned*)(sm + c * 16384 + T.uo[t][2 * h + 1]) = pk2(x[1], y[1]);
  }
  WAIT_L0();
  BAR();
  SCHED();
  {
    bf16x8 ua[NC][2][4];
    FOR_K FOR_C FOR_T ua[c][t][ks] = *(LDS_AS bf16x8*)(sm + c * 16384 + T.ro[ks] + t * 4096);
    FOR_K FOR_C FOR_T {
      re[c][t] = MFMA16(ua[c][t][ks], BC(T.fre[ks]), ks ? re[c][t] : z4);
      im[c][t] = MFMA16(ua[c][t][ks], BC(T.fim[ks]), ks ? im[c][t] : z4);
    }
  }
  FOR_C {
    const int fu = (c ? FSECOND + (fu0 >> 1) : fu0), f = min(fu, 320);
    const rsrc_t ry = mk_rsrc(ws + WS_Y + (size_t)f * 4096, fu > 320 ? 0 : ((f == 0 || f == 320) ? 4096 : 321 * 4096));
    ST16(widen((u32x2){pk2(re[c][0][0], re[c][0][1]), pk2(re[c][0][2], re[c][0][3])}, (u32x2){pk2(re[c][1][0], re[c][1][1]), pk2(re[c][1][2], re[c][1][3])}), ry, T.vy, 0);
    ST16(widen((u32x2){pk2(im[c][0][0], im[c][0][1]), pk2(im[c][0][2], im[c][0][3])}, (u32x2){pk2(im[c][1][0], im[c][1][1]), pk2(im[c][1][2], im[c][1][3])}), ry, T.vy, 320 * 4096);
  }
}

template <bool F> DI void phase2(const Args& A, lptr sm0, unsigned e, const f32x4 (&pq)[2][5] TS_ARG) {
  char* ws = A.ws;
  const int lane = threadIdx.x & 63, tid = threadIdx.x & 255;
  const int wave8 = __builtin_amdgcn_readfirstlane(threadIdx.x >> 6);
  const int wave = wave8 & 3, team = wave8 >> 2;
  lptr sm = sm0 + team * P3_HALF;
  const unsigned smu = (unsigned)(size_t)sm;
  const int b = blockIdx.x, xcd = b & 7, idx = b >> 3;
  const int rt1 = (xcd & 1) * 8 + (idx & 7), ct1 = (xcd >> 1) * 4 + (idx >> 3);
  const bool iskv = ct1 >= 4 && ct1 < 12;
  const int v = (ct1 - 4) * 16 + rt1;
  const int r = lane & 15, g = lane >> 4;
  const f32x4 z4 = {0.f, 0.f, 0.f, 0.f};
  const bool isfft = ct1 >= 12 || (iskv && v < 88);
  publish(ws, 1, e);
  TS(5);
  if (isfft) {
    const int fu0 = (iskv ? 128 + 2 * v : 2 * (rt1 * 4 + (ct1 & 3))) + team;
    u32x4 fre[4], fim[4], fc[2], fs[2], fn[2];
    f32x2 tc[2][2], tsn[2][2];
    unsigned uo[2][4], ro[4];
    {
      const float a64 = (float)(16 * wave + r) * (1.f / 64.f);
      const unsigned m2 = (r & 2) ? 0x80008000u : 0u, m1 = (r & 1) ? 0x80008000u : 0u;
      const unsigned m3 = ((r + 1) & 2) ? 0x80008000u : 0u;
#pragma unroll
      for (int j = 0; j < 4; ++j) {
        const float ang = a64 * (float)(4 * g + j);
        const float c = __builtin_amdgcn_cosf(ang), s = __builtin_amdgcn_sinf(ang);
        const unsigned p = pk2(c, s), q = pk2(-s, c);
        fre[0][j] = p; fim[0][j] = q;
        fre[1][j] = ((r & 1) ? q : p) ^ m2;
        fim[1][j] = ((r & 1) ? p : q) ^ m3;
        fre[2][j] = p ^ m1; fim[2][j] = q ^ m1;
        fre[3][j] = fre[1][j] ^ m1; fim[3][j] = fim[1][j] ^ m1;
      }
      const float a32 = (float)r * (1.f / 32.f);
#pragma unroll
      for (int j = 0; j < 2; ++j) {
        const float a0 = a32 * (float)(4 * g + 2 * j), a1 = a0 + a32;
        const unsigned c = pk2(__builtin_amdgcn_cosf(a0), __builtin_amdgcn_cosf(a1)), s = pk2(__builtin_amdgcn_sinf(a0), __builtin_amdgcn_sinf(a1));
        fc[0][j] = c; fc[0][2 + j] = c ^ m1; fs[0][j] = s; fs[0][2 + j] = s ^ m1;
      }
#pragma unroll
      for (int j = 0; j < 4; ++j) {
        fc[1][j] = fc[0][j] ^ 0x80000000u; fs[1][j] = fs[0][j] ^ 0x80000000u;
        fn[0][j] = fs[0][j] ^ 0x80008000u; fn[1][j] = fs[0][j] ^ 0x00008000u;
        ro[j] = 8192 + r * 256 + (((4 * j + g) ^ r) * 16);
      }
#pragma unroll
      for (int t = 0; t < 2; ++t)
#pragma unroll
        for (int j = 0; j < 4; ++j) {
          const int s2 = 16 * t + 4 * g + j;
          const float a = a64 * (1.f / 32.f) * (float)s2;
          tc[t][j >> 1][j & 1] = __builtin_amdgcn_cosf(a);
          tsn[t][j >> 1][j & 1] = __builtin_amdgcn_sinf(a);
          uo[t][j] = 8192 + s2 * 256 + (((4 * wave + (r >> 2)) ^ (s2 & 15)) * 16) + (r & 3) * 4;
        }
    }
    unsigned vy = wave * 1024 + r * 64 + (g & 1) * 32 + (g >> 1) * 16;
#pragma unroll
    for (int ks = 0; ks < 4; ++ks) { asm volatile("" : "+v"(fre[ks]), "+v"(fim[ks]), "+v"(ro[ks])); }
#pragma unroll
    for (int t = 0; t < 2; ++t) {
      asm volatile("" : "+v"(fc[t]), "+v"(fs[t]), "+v"(fn[t]), "+v"(tc[t][0]), "+v"(tc[t][1]), "+v"(tsn[t][0]), "+v"(tsn[t][1]));
      asm volatile("" : "+v"(uo[t][0]), "+v"(uo[t][1]), "+v"(uo[t][2]), "+v"(uo[t][3]), "+v"(vy));
    }
    SCHED();
    TS(6);
    FftC T = {fre, fim, fc, fs, fn, tc, tsn, uo, ro, vy};
    if (fu0 < 34 && team == 0) fft_run<2>(ws, e, sm, T, fu0, wave8, lane TS_PASS);
    else fft_run<1>(ws, e, sm, T, fu0, wave8, lane TS_PASS);
  } else if (iskv) {
    const int ks = (v - 88) >> 1, cp = 2 * ((v - 88) & 1) + team;
    const int hf = (32 * ks) / 20, e0 = 20 * hf;
    const int q = r >> 2, p = lane & 3;
    const int hsw = ((q >> 1) & 1) | ((g & 1) << 1);
    const int L1 = (160 * cp) >> 7, L2 = (1120 - 160 * cp) >> 7;
    {
      const rsrc_t rwo = mk_rsrc(A.wo, 640 * 1280);
      const int line = (wave < 2 ? L1 : L2) + (wave & 1), row = lane >> 3;
#pragma unroll
      for (int kb = 0; kb < 8; ++kb) {
        const int pair = ((lane & 7) >> 1) ^ (((row >> 1) & 1) | ((kb & 1) << 1));
        dma16_asm(rwo, smu + (wave * 8 + kb) * 1024, min(e0 + 8 * kb + row, 639) * 1280 + line * 128 + pair * 32 + (lane & 1) * 16, 0);
      }
    }
    lptr ab = sm0 + 32768;
    if (threadIdx.x < 288) *(LDS_AS u32x4*)(ab + threadIdx.x * 16) = (u32x4){0u, 0u, 0u, 0u};
    const int t5 = min((int)threadIdx.x >> 1, 159), kl = t5 / 5, qd = t5 - kl * 5;
    const int k = 32 * ks + kl, h = k / 20, d = k - 20 * h;
    unsigned ko = h * 1920 + d * 96 + qd * 16 + (threadIdx.x & 1) * 8 * 61440, ao = kl * 144 + (20 * (h - hf) + 4 * qd) * 2;
    unsigned bo[3];
#pragma unroll
    for (int n = 0; n < 3; ++n) {
      const int tl = min(wave + 4 * n, 9);
      const int ctl = tl >= 5 ? 1 : 0, nt = tl - 5 * ctl, ctg = 2 * cp + ctl;
      const int i = 16 * nt + 4 * p;
      const int byte = 2 * (i < 40 ? 40 * ctg + i : 560 - 40 * ctg + i);
      const int pl = i < 40 ? (byte >> 7) - L1 : 2 + (byte >> 7) - L2;
      bo[n] = (pl * 8 + g) * 1024 + q * 128 + ((((byte >> 5) & 3) ^ hsw) * 32) + (byte & 31);
      asm volatile("" : "+v"(bo[n]));
    }
    asm volatile("" : "+v"(ko), "+v"(ao));
    WAIT_V(0);
    WAIT_L0();
    TS(6);
    await(ws, 1, e, 2, wave8, lane);
    TS(7);
    f32x4 pv[8];
    const rsrc_t rk = mk_rsrc(ws + WS_KVP, wave8 < 5 ? 983040 : 0);
#pragma unroll
    for (int i = 0; i < 8; ++i) pv[i] = __builtin_bit_cast(f32x4, LD16(rk, ko, i * 61440));
    SCHED();
    bf16x8 bw[3][2];
#pragma unroll
    for (int n = 0; n < 3; ++n)
#pragma unroll
      for (int s = 0; s < 2; ++s) {
        const s16x4 lo = TRREAD(sm + bo[n] + s * 4096), hi = TRREAD(sm + bo[n] + s * 4096 + 512);
        bw[n][s] = __builtin_shufflevector(lo, hi, 0, 1, 2, 3, 4, 5, 6, 7);
      }
    SCHED();
    f32x4 sum = ((pv[0] + pv[1]) + (pv[2] + pv[3])) + ((pv[4] + pv[5]) + (pv[6] + pv[7]));
#pragma unroll
    for (int j = 0; j < 4; ++j) sum[j] += __uint_as_float(__builtin_amdgcn_update_dpp(0u, __float_as_uint(sum[j]), 0xB1, 0xf, 0xf, false));
    if (threadIdx.x < 320 && !(threadIdx.x & 1)) *(LDS_AS u32x2*)(ab + ao) = (u32x2){pk2(sum[0], sum[1]), pk2(sum[2], sum[3])};
    WAIT_L0();
    BAR();
    TS(8);
    bf16x8 a[2][2];
#pragma unroll
    for (int t = 0; t < 2; ++t)
#pragma unroll
      for (int s = 0; s < 2; ++s) a[t][s] = *(LDS_AS bf16x8*)(ab + (8 * (r >> 2) + (r & 3) + 4 * t) * 144 + s * 64 + g * 16);
    const rsrc_t rm = mk_rsrc(ws + WS_M + (size_t)(10 * cp) * 20480 + ks * 1024, 9 * 20480 + 1024);
#pragma unroll
    for (int n = 0; n < 3; ++n) {
      f32x4 d1 = MFMA16(a[0][0], bw[n][0], z4), d2 = MFMA16(a[1][0], bw[n][0], z4);
      d1 = MFMA16(a[0][1], bw[n][1], d1); d2 = MFMA16(a[1][1], bw[n][1], d2);
      ST16(__builtin_bit_cast(u32x4, pack8(d1, d2)), rm, lane * 16, (wave + 4 * n) * 20480);
    }
  } else if (ct1 < 4) {
    const int t8 = threadIdx.x, wm = wave8 >> 1, wn = wave8 & 1;
    const int gb = rt1 * 4 + ct1;
      const rsrc_t rx = mk_rsrc(A.x + (size_t)gb * 32 * 640, 32 * 1280), rg = mk_rsrc(A.gw, 1280);
    u32x4 gx[5], gg[5];
#pragma unroll
      for (int i = 0; i < 5; ++i) {
        gx[i] = __builtin_amdgcn_raw_buffer_load_b128(rx, t8 * 80 + i * 16, 0, 0);
        gg[i] = __builtin_amdgcn_raw_buffer_load_b128(rg, (t8 & 15) * 80 + i * 16, 0, 0);
      }
    u32x2 pk[2][5];
    unsigned zm[5];
#pragma unroll
    for (int nt = 0; nt < 5; ++nt) {
#pragma unroll
      for (int mt = 0; mt < 2; ++mt) {
        pk[mt][nt] = (u32x2){pk2(pq[mt][nt][0], pq[mt][nt][1]), pk2(pq[mt][nt][2], pq[mt][nt][3])};
        asm volatile("" : "+v"(pk[mt][nt]));
      }
      zm[nt] = ((4 * nt + g) / 5 == (r & 3)) ? 0xffffffffu : 0u;
      asm volatile("" : "+v"(zm[nt]));
    }
    const rsrc_t rz = mk_rsrc(ws + WS_ZP + ct1 * 640, 40960);
    const rsrc_t rp = mk_rsrc(ws + WS_P + (size_t)(8 * rt1 + 2 * wm) * 20480 + (20 * ct1 + 10 * wn) * 256, 2 * 20480);
    const unsigned vo = (g & 1) * 20480 + (g >> 1) * 256 + r * 16;
    SCHED();
    TS(6);
    await(ws, 1, e, 2, wave8, lane);
    TS(7);
    if (wave8 < 3) {
      float zv[16];
#pragma unroll
      for (int i = 0; i < 16; ++i) zv[i] = __uint_as_float(__builtin_amdgcn_raw_buffer_load_b32(rz, min(t8, 159) * 4, i * 2560, LD_AUX));
#pragma unroll
      for (int i = 0; i < 8; ++i) zv[i] += zv[i + 8];
      const float zs = ((zv[0] + zv[1]) + (zv[2] + zv[3])) + ((zv[4] + zv[5]) + (zv[6] + zv[7]));
      *(LDS_AS u16*)(sm0 + t8 * 2) = (u16)pk2(zs, zs);
    }
    WAIT_L0();
    BAR();
    TS(8);
    u32x2 zq[6];
#pragma unroll
    for (int nt = 0; nt < 5; ++nt) { zq[nt] = *(LDS_AS u32x2*)(sm0 + wn * 160 + nt * 32 + g * 8); zq[nt][0] &= zm[nt]; zq[nt][1] &= zm[nt]; }
    zq[5] = (u32x2){0u, 0u};
    u32x2 po[2][5];
#pragma unroll
    for (int mt = 0; mt < 2; ++mt) {
      f32x4 den = {kEps, kEps, kEps, kEps};
#pragma unroll
      for (int m = 0; m < 3; ++m) {
        const u32x4 za = {zq[2 * m][0], zq[2 * m][1], zq[2 * m + 1][0], zq[2 * m + 1][1]};
        const u32x2 p1 = m < 2 ? pk[mt][2 * m + 1] : (u32x2){0u, 0u};
        const u32x4 pb = {pk[mt][2 * m][0], pk[mt][2 * m][1], p1[0], p1[1]};
        den = MFMA16(BC(za), BC(pb), den);
      }
      f32x4 inv;
#pragma unroll
      for (int j = 0; j < 4; ++j) inv[j] = __builtin_amdgcn_rcpf(den[j]);
#pragma unroll
      for (int nt = 0; nt < 5; ++nt) {
        const float iv = nt == 0 ? inv[0] : nt == 4 ? inv[3] : (g < nt ? inv[nt - 1] : inv[nt]);
        const f32x4 w = pq[mt][nt] * iv;
        po[mt][nt] = (u32x2){pk2(w[0], w[1]), pk2(w[2], w[3])};
      }
    }
#pragma unroll
    for (int nt = 0; nt < 5; ++nt) ST16(widen(po[0][nt], po[1][nt]), rp, vo + nt * 512, 0);
    SCHED();
    {
      float s0 = 0.f, s1 = 0.f;
#pragma unroll
      for (int i = 0; i < 5; ++i) { s0 = dot2(gx[i][0], gg[i][0], s0); s1 = dot2(gx[i][1], gg[i][1], s1); s0 = dot2(gx[i][2], gg[i][2], s0); s1 = dot2(gx[i][3], gg[i][3], s1); }
      float s = s0 + s1;
#pragma unroll
      for (int o = 8; o > 0; o >>= 1) s += __shfl_xor(s, o);
      if ((t8 & 15) == 0) __builtin_amdgcn_raw_buffer_store_b32(__float_as_uint(1.f / (1.f + __expf(-s))), mk_rsrc(ws + WS_G, 8192), (gb * 32 + (t8 >> 4)) * 4, 0, 16);
    }
  }
  TS(9);
}

constexpr int LDS_ALL = K1_LDS;
static_assert(2 * P3_HALF + 256 <= LDS_ALL, "lds");

template <bool F> DI void phase3(const Args& A, lptr sm, unsigned e TS_ARG) {
  char* ws = A.ws;
  const int tid = threadIdx.x, lane = tid & 63;
  const int wave = __builtin_amdgcn_readfirstlane(tid >> 6);
  const int b = blockIdx.x, xcd = b & 7, idx = b >> 3;
  const int rt = (xcd & 3) * 8 + (idx & 7);
  const int ct = (xcd >> 2) * 4 + (idx >> 3);
  const int r = lane & 15, g = lane >> 4;
  LDS_AS float* red = (LDS_AS float*)sm;
  constexpr int RA = 64 * 84, RE = 64 * 52;
  if (!F) { p3_load_g(sm, wave, lane); if (wave >= 4) WAIT_V(0); }
  TS(11);
  if (wave < 2) {
    const rsrc_t ra = mk_rsrc(ws + WS_P + (size_t)(4 * rt) * 20480 + wave * 10240, 4 * 20480);
    const rsrc_t rb = mk_rsrc(ws + WS_M + (size_t)(5 * ct) * 20480 + wave * 10240, 5 * 20480);
    const unsigned vo = lane * 16;
    f32x4 acc[4][5];
#pragma unroll
    for (int mt = 0; mt < 4; ++mt)
#pragma unroll
      for (int nt = 0; nt < 5; ++nt) acc[mt][nt] = (f32x4){0.f, 0.f, 0.f, 0.f};
    u32x4 fa[10][4], fb[10][5];
    constexpr int PF = 3;
    auto ld = [&](int ks) {
#pragma unroll
      for (int nt = 0; nt < 5; ++nt) fb[ks][nt] = LD16(rb, vo + (ks & 3) * 1024, nt * 20480 + (ks >> 2) * 4096);
#pragma unroll
      for (int mt = 0; mt < 4; ++mt) fa[ks][mt] = LD16(ra, vo + (ks & 3) * 1024, mt * 20480 + (ks >> 2) * 4096);
    };
#pragma unroll
    for (int ks = 0; ks < PF; ++ks) { ld(ks); SCHED(); }
    if (!F) { WAIT_V(27); BAR(); }
#pragma unroll
    for (int ks = 0; ks < 10; ++ks) {
#pragma unroll
      for (int mt = 0; mt < 4; ++mt)
#pragma unroll
        for (int nt = 0; nt < 5; ++nt) acc[mt][nt] = MFMA16(BC(fb[ks][nt]), BC(fa[ks][mt]), acc[mt][nt]);
      SCHED();
      if (ks + PF < 10) ld(ks + PF);
      SCHED();
    }
    TS(12);
    BAR();
#pragma unroll
    for (int mt = 0; mt < 4; ++mt)
#pragma unroll
      for (int nt = 0; nt < 5; ++nt) *(LDS_AS f32x4*)(red + wave * RA + (16 * mt + r) * 84 + 16 * nt + 4 * g) = acc[mt][nt];
  } else if (wave < 4) {
    const int od = wave - 2;
    lptr yb = sm + od * P3_HALF;
    const unsigned ybu = (unsigned)(size_t)yb;
    const rsrc_t ry = mk_rsrc(ws + WS_Y + (size_t)od * 320 * 4096 + rt * 128, 320 * 4096);
    const int row = lane >> 3;
    const unsigned vy0 = row * 4096 + ((((lane & 7) >> 1) ^ ((row >> 1) & 1)) * 32) + (lane & 1) * 16;
    const unsigned vy1 = vy0 ^ 64;
    auto issue = [&](int ks) {
#pragma unroll
      for (int i = 0; i < 4; ++i) dma16_sc1(ry, ybu + (ks * 4 + i) * 1024, (i & 1) ? vy1 : vy0, (ks * 4 + i) * 32768);
    };
    issue(0); issue(1); issue(2);
    if (!F) { WAIT_V(12); BAR(); }
    const int q = r >> 2, p = lane & 3;
    const int hsw = ((q >> 1) & 1) | ((g & 1) << 1);
    unsigned yo[4];
#pragma unroll
    for (int mt = 0; mt < 4; ++mt) yo[mt] = g * 1024 + q * 128 + ((mt ^ hsw) * 32) + p * 8;
    lptr gb = yb + 40960 + lane * 16;
    f32x4 acc[4][3];
#pragma unroll
    for (int mt = 0; mt < 4; ++mt)
#pragma unroll
      for (int nt = 0; nt < 3; ++nt) acc[mt][nt] = (f32x4){0.f, 0.f, 0.f, 0.f};
#define K3_STEP(ks, n)                                                                                         \
    {                                                                                                          \
      WAIT_V(n);                                                                                               \
      SCHED();                                                                                                 \
      bf16x8 fy[4], fg[3];                                                                                     \
      _Pragma("unroll") for (int nt = 0; nt < 3; ++nt) fg[nt] = *(LDS_AS bf16x8*)(gb + (ks * 3 + nt) * 1024);  \
      _Pragma("unroll") for (int mt = 0; mt < 4; ++mt) {                                                       \
        const s16x4 lo = TRREAD(yb + yo[mt] + ks * 4096), hi = TRREAD(yb + yo[mt] + ks * 4096 + 512);          \
        fy[mt] = __builtin_shufflevector(lo, hi, 0, 1, 2, 3, 4, 5, 6, 7);                                      \
      }                                                                                                        \
      SCHED();                                                                                                 \
      if (ks + 3 < 10) issue(ks + 3);                                                                          \
      SCHED();                                                                                                 \
      _Pragma("unroll") for (int mt = 0; mt < 4; ++mt)                                                         \
        _Pragma("unroll") for (int nt = 0; nt < 3; ++nt) acc[mt][nt] = MFMA16(fg[nt], fy[mt], acc[mt][nt]);   \
      SCHED();                                                                                                 \
    }
    K3_STEP(0, 8) K3_STEP(1, 8) K3_STEP(2, 8) K3_STEP(3, 8) K3_STEP(4, 8)
    K3_STEP(5, 8) K3_STEP(6, 8) K3_STEP(7, 8) K3_STEP(8, 4) K3_STEP(9, 0)
    TS(12);
    BAR();
#pragma unroll
    for (int mt = 0; mt < 4; ++mt)
#pragma unroll
      for (int nt = 0; nt < 3; ++nt) *(LDS_AS f32x4*)(red + 2 * RA + od * RE + (16 * mt + r) * 52 + 16 * nt + 4 * g) = acc[mt][nt];
  } else {
    if (!F) BAR();
    if (wave == 4) red[2 * P3_HALF / 4 + lane] = __uint_as_float(__builtin_amdgcn_raw_buffer_load_b32(mk_rsrc(ws + WS_G + rt * 256, 256), lane * 4, 0, LD_AUX));
    WAIT_L0();
    BAR();
  }
  const rsrc_t ro = mk_rsrc(A.out + (size_t)(64 * rt) * 640, 64 * 1280);
  unsigned oa[2], oe[2], oq[2], og[2];
  float gq[2];
#pragma unroll
  for (int i = 0; i < 2; ++i) {
    const int it = i ? 512 + wave * 16 + (lane & 15) : tid;
    const int row = it / 10, gr = it - row * 10;
    oa[i] = (row * 84 + 8 * gr) * 4;
    oe[i] = (2 * RA + row * 52) * 4;
    oq[i] = oe[i] + (gr < 5 ? 8 * gr : 72 - 8 * gr) * 4;
    og[i] = (i && lane >= 16) ? 0x7fffffffu : row * 1280 + (gr < 5 ? 80 * ct : 1120 - 80 * ct) + 16 * gr;
    gq[i] = red[2 * P3_HALF / 4 + row];
    oa[i] |= (gr < 5) ? 0u : 0x80000000u;
    asm volatile("" : "+v"(oa[i]), "+v"(oe[i]), "+v"(oq[i]), "+v"(og[i]), "+v"(gq[i]));
  }
  WAIT_L0();
  BAR();
  TS(13);
#pragma unroll
  for (int i = 0; i < 2; ++i) {
    const bool lo = (int)oa[i] >= 0;
    lptr pa = sm + (oa[i] & 0x7fffffffu), pq = sm + oq[i];
    const f32x4 a0 = *(LDS_AS f32x4*)pa + *(LDS_AS f32x4*)(pa + RA * 4);
    const f32x4 a1 = *(LDS_AS f32x4*)(pa + 16) + *(LDS_AS f32x4*)(pa + RA * 4 + 16);
    const float yn = *(LDS_AS float*)(sm + oe[i] + (RE + 41) * 4);
    const f32x4 e0 = *(LDS_AS f32x4*)pq, e1 = *(LDS_AS f32x4*)(pq + 16), o0 = *(LDS_AS f32x4*)(pq + RE * 4), o1 = *(LDS_AS f32x4*)(pq + RE * 4 + 16);
    const float p8 = *(LDS_AS float*)(pq + 32) + *(LDS_AS float*)(pq + RE * 4 + 32);
    const f32x4 m0 = e0 - o0, m1 = e1 - o1, p0 = e0 + o0, p1 = e1 + o1;
    const f32x4 yy = {yn, -yn, yn, -yn};
    const f32x4 b0 = (lo ? m0 : (f32x4){p8, p1[3], p1[2], p1[1]}) + yy;
    const f32x4 b1 = (lo ? m1 : (f32x4){p1[0], p0[3], p0[2], p0[1]}) + yy;
    const f32x4 r0 = b0 + gq[i] * (a0 - b0), r1 = b1 + gq[i] * (a1 - b1);
    __builtin_amdgcn_raw_buffer_store_b128(__builtin_bit_cast(u32x4, pack8(r0, r1)), ro, og[i], 0, OUT_AUX);
  }
  TS(14);
}

#define TS_END()

__global__ __launch_bounds__(512, 2) void fused_kernel(Args A) {
  extern __shared__ __attribute__((aligned(16))) char smem[];
  lptr sm = (lptr)smem;
  TS_DECL;
  TS(0);
  {
    unsigned long long pc; unsigned len;
    asm volatile("s_getpc_b64 %0\n.Lfk_start:\n\ts_mov_b32 %1, .Lfk_end-.Lfk_start" : "=s"(pc), "=s"(len));
    pf_asm(mk_rsrc((const void*)pc, len), (unsigned)(size_t)sm + K1_JUNK + 1792, (threadIdx.x * 32 + (blockIdx.x >> 3)) * 128, 0);
  }
  const rsrc_t re = mk_rsrc(A.ws + WS_EPOCH, 4);
  const unsigned e = *(const __attribute__((address_space(4))) unsigned*)(A.ws + WS_EPOCH);
  f32x4 pq[2][5];
  phase1(A, sm, pq, e TS_PASS);
  phase2<true>(A, sm, e, pq TS_PASS);
  publish(A.ws, 2, e);
  p3_load_g(sm, __builtin_amdgcn_readfirstlane(threadIdx.x >> 6), threadIdx.x & 63);
  if (threadIdx.x < 448) WAIT_V(0);
  TS(10);
  await(A.ws, 2, e, 7, __builtin_amdgcn_readfirstlane(threadIdx.x >> 6), threadIdx.x & 63);
  phase3<true>(A, sm, e TS_PASS);
  if (blockIdx.x == 0 && threadIdx.x == 0) __builtin_amdgcn_raw_buffer_store_b32(e + 1u, re, 0, 0, 16);
  TS_END();
  asm volatile(".Lfk_end:");
}

extern "C" void kernel_launch(void* const* d_in, const int* in_sizes, int n_in, void* d_out, int out_size, void* d_ws,
                              size_t ws_size, hipStream_t stream) {
  Args A;
  A.x = (const u16*)d_in[0]; A.wqkv = (const u16*)d_in[1]; A.wo = (const u16*)d_in[2];
  A.wr = (const u16*)d_in[4]; A.wi = (const u16*)d_in[5]; A.gw = (const u16*)d_in[6];
  A.ws = (char*)d_ws; A.out = (u16*)d_out;
  (void)hipFuncSetAttribute((const void*)fused_kernel, hipFuncAttributeMaxDynamicSharedMemorySize, LDS_ALL);
  fused_kernel<<<256, 512, LDS_ALL, stream>>>(A);
}
```

```cpp
#include <hip/hip_runtime.h>
#include <hip/hip_bf16.h>
#include <utility>

typedef unsigned short u16;
typedef __attribute__((ext_vector_type(8))) short bf16x8;
typedef __attribute__((ext_vector_type(4))) short s16x4;
typedef __attribute__((ext_vector_type(4))) float f32x4;
typedef __attribute__((ext_vector_type(2))) float f32x2;
typedef __attribute__((ext_vector_type(2))) __bf16 bf16x2_t;
typedef __attribute__((ext_vector_type(4))) unsigned u32x4;
typedef __attribute__((ext_vector_type(2))) unsigned u32x2;
#define LDS_AS __attribute__((address_space(3)))
#define DI __device__ __forceinline__
#define MFMA16(a, b, c) __builtin_amdgcn_mfma_f32_16x16x32_bf16((a), (b), (c), 0, 0, 0)
typedef LDS_AS char* lptr;
typedef __amdgpu_buffer_rsrc_t rsrc_t;

#define ST_AUX 16
#define OUT_AUX 0
#define K1_ROT 5
#define FUSED 1
#define POLL_SLEEP 2
#define CODE_PF 1
#define TLB_STRIDE 0
#define P3_REV 0
#define P3_PF 3
#define P3_YW 3
#define P3_MAP 1
#define LD_AUX 16

constexpr double kPi = 3.14159265358979323846;
constexpr double ccos_s(double x) { double x2 = x * x, t = 1, s = 1; for (int i = 1; i < 12; ++i) { t *= -x2 / ((2 * i - 1) * (2 * i)); s += t; } return s; }
constexpr double csin_s(double x) { double x2 = x * x, t = x, s = x; for (int i = 1; i < 12; ++i) { t *= -x2 / ((2 * i) * (2 * i + 1)); s += t; } return s; }
constexpr double cos640(int m) {
  m %= 640; if (m > 320) m = 640 - m;
  bool neg = false; if (m > 160) { m = 320 - m; neg = true; }
  double v = (m <= 80) ? ccos_s(2 * kPi * m / 640) : csin_s(2 * kPi * (160 - m) / 640);
  return neg ? -v : v;
}
constexpr u16 to_bf16c(double v) { float f = (float)v; unsigned u = __builtin_bit_cast(unsigned, f); u += 0x7fffu + ((u >> 16) & 1u); return (u16)(u >> 16); }
struct C640 { u16 v[640]; };
constexpr C640 make_c640() { C640 c{}; for (int i = 0; i < 640; ++i) c.v[i] = to_bf16c(cos640(i)); return c; }
constexpr C640 COS640 = make_c640();

struct alignas(128) FRow { u16 v[10 * 512]; };
constexpr FRow make_frow(int id) {
  FRow t{};
  const int cq = id / 10, st = id % 10;
  for (int par = 0; par < 2; ++par) for (int nt = 0; nt < 5; ++nt) for (int l = 0; l < 64; ++l) {
    const int kk = 80 * cq + 16 * nt + (l & 15);
    for (int j = 0; j < 8; ++j) {
      const int d = 64 * st + 32 * (j >> 2) + 8 * (l >> 4) + 2 * (j & 3) + par;
      const int m = (kk == 1) ? (d * 160 + 160 * par) % 640 : (d * (kk >> 1) + 160 * (kk & 1)) % 640;
      t.v[((par * 5 + nt) * 64 + l) * 8 + j] = COS640.v[m];
    }
  }
  return t;
}
template <int I> constexpr FRow FROW = make_frow(I);
struct alignas(128) FTab { FRow r[40]; };
template <int... I> constexpr FTab make_ftab(std::integer_sequence<int, I...>) { return FTab{{FROW<I>...}}; }
__device__ const FTab FT = make_ftab(std::make_integer_sequence<int, 40>{});

struct alignas(128) GRow { u16 v[10 * 3 * 512]; };
constexpr GRow make_grow(int id) {
  GRow t{};
  const int ct = id >> 1, od = id & 1;
  for (int ks = 0; ks < 10; ++ks) for (int nt = 0; nt < 3; ++nt) for (int l = 0; l < 64; ++l) {
    const int ci = 16 * nt + (l & 15), d = 40 * ct + ci;
    for (int j = 0; j < 8; ++j) {
      const int f = 32 * ks + 8 * (l >> 4) + j;
      t.v[((ks * 3 + nt) * 64 + l) * 8 + j] = (ci <= 40) ? COS640.v[(f * d + (od ? 160 : 0)) % 640] : (u16)((od && ci == 41 && f == 0) ? 0x3f80 : 0);
    }
  }
  return t;
}
template <int I> constexpr GRow GROW = make_grow(I);
struct alignas(128) GTab { GRow r[16]; };
template <int... I> constexpr GTab make_gtab(std::integer_sequence<int, I...>) { return GTab{{GROW<I>...}}; }
__device__ const GTab GT = make_gtab(std::make_integer_sequence<int, 16>{});

constexpr size_t WS_P   = 0;
constexpr size_t WS_Y   = WS_P + 2621440;
constexpr size_t WS_X1  = WS_Y + 2621440;
constexpr size_t WS_WT  = WS_X1 + 321 * 8192;
constexpr size_t WS_KVP = WS_WT + 352 * 8192;
constexpr size_t WS_M   = WS_KVP + 983040;
constexpr size_t WS_G   = WS_M + 819200;
constexpr size_t WS_ZP  = WS_G + 8192;
constexpr size_t WS_SYN = WS_ZP + 40960;

DI rsrc_t mk_rsrc(const void* p, unsigned bytes) { return __builtin_amdgcn_make_buffer_rsrc((void*)p, 0, bytes, 0x00020000); }
DI unsigned pk2(float a, float b) { f32x2 v = {a, b}; return __builtin_bit_cast(unsigned, __builtin_convertvector(v, bf16x2_t)); }
DI float bf_lo(unsigned u) { return __uint_as_float(u << 16); }
DI float bf_hi(unsigned u) { return __uint_as_float(u & 0xffff0000u); }
DI float dot2(unsigned a, unsigned b, float c) { return __builtin_amdgcn_fdot2_f32_bf16(__builtin_bit_cast(bf16x2_t, a), __builtin_bit_cast(bf16x2_t, b), c, false); }
DI float rbf(float a) { return bf_lo(pk2(a, a)); }
constexpr float kEps = 1e-6f, kL2E = 1.4426950408889634f;
DI float phi_f(float a) {
  float e;
  const float u = __builtin_fmaf(a, kL2E, -kEps * kL2E);
  asm("v_exp_f32_e64 %0, %1 clamp" : "=v"(e) : "v"(u));
  return __builtin_amdgcn_fmed3f(a, kEps, __builtin_inff()) + e;
}
DI bf16x8 pack8(const f32x4& a, const f32x4& b) {
  u32x4 p = {pk2(a[0], a[1]), pk2(a[2], a[3]), pk2(b[0], b[1]), pk2(b[2], b[3])};
  return __builtin_bit_cast(bf16x8, p);
}
DI u32x4 widen(u32x2 a, u32x2 b) {
  auto x = __builtin_amdgcn_permlane16_swap(a[0], b[0], false, false);
  auto y = __builtin_amdgcn_permlane16_swap(a[1], b[1], false, false);
  return (u32x4){x[0], y[0], x[1], y[1]};
}
#define WAIT_V_(n) asm volatile("s_waitcnt vmcnt(" #n ")" ::: "memory")
#define WAIT_V(n) WAIT_V_(n)
#define BC(v) __builtin_bit_cast(bf16x8, (v))
#define WAIT_L0() __builtin_amdgcn_s_waitcnt(0xC07F)
#define BAR() __builtin_amdgcn_s_barrier()
#define SCHED() __builtin_amdgcn_sched_barrier(0)
#define DMA16(rs, dst, vo, so) __builtin_amdgcn_raw_ptr_buffer_load_lds((rs), (LDS_AS void*)(dst), 16, (vo), (so), 0, 0)
#define TRREAD(p) __builtin_amdgcn_ds_read_tr16_b64_v4i16((LDS_AS s16x4*)(p))
#define ST16(v, rs, vo, so) __builtin_amdgcn_raw_buffer_store_b128((v), (rs), (vo), (so), ST_AUX)

#define TS_DECL
#define TS(i)
#define TC(i)
#define TS_FLUSH(kern)
#define TS_FLUSH2()
#define TS_ARG
#define TS_PASS

constexpr int K1_STAGE = 40960;
constexpr int K1_WF = 3 * K1_STAGE;
constexpr int K1_JUNK = K1_WF + 32768;
constexpr int K1_LDS = K1_JUNK + 2048;
#define K1_PF 1

struct Frags { bf16x8 a[2][2]; bf16x8 b[2][5]; };
struct K1Ctx { rsrc_t ra, rb; unsigned va[2], vb[3], ao[3][2], bo[3][5], ldsa, ldsb; int st, rs, wave; };

struct Half { s16x4 lo, hi; };
template <int I0, int I1> DI void k1_read(Frags& F, lptr sm, const unsigned (&ao)[2], const unsigned (&bo)[5]) {
#pragma unroll
  for (int i = I0; i < I1; ++i) {
    const int k = i / 7, j = i % 7;
    if (j < 2) F.a[k][j] = *(LDS_AS bf16x8*)(sm + ao[k] + j * 2048);
    else {
      const s16x4 lo = TRREAD(sm + bo[j - 2] + k * 12288), hi = TRREAD(sm + bo[j - 2] + k * 12288 + 512);
      F.b[k][j - 2] = __builtin_shufflevector(lo, hi, 0, 1, 2, 3, 4, 5, 6, 7);
    }
  }
}
DI void dma16_asm(rsrc_t rs, unsigned lds, unsigned vo, int so) {
  asm volatile("s_mov_b32 m0, %2\n\ts_nop 0\n\tbuffer_load_dwordx4 %0, %1, %3 offen lds" :: "v"(vo), "s"(rs), "s"(lds), "s"(so));
}
DI void pf_asm(rsrc_t rs, unsigned lds, unsigned vo, int so) {
  asm volatile("s_mov_b32 m0, %2\n\ts_nop 0\n\tbuffer_load_dword %0, %1, %3 offen lds" :: "v"(vo), "s"(rs), "s"(lds), "s"(so));
}
template <int BUF, int I> DI void k1_issue1(const K1Ctx& c) {
  if (I < 2) dma16_asm(c.ra, c.ldsa + BUF * K1_STAGE + I * 1024, c.va[I < 2 ? I : 0], c.st * 128);
  else dma16_asm(c.rb, c.ldsb + BUF * K1_STAGE + (I - 2) * 1024, c.vb[I < 2 ? 0 : I - 2], c.st * 64 * c.rs);
}
template <int BUF> DI void k1_issue(K1Ctx& c) {
  k1_issue1<BUF, 0>(c); k1_issue1<BUF, 1>(c); k1_issue1<BUF, 2>(c); k1_issue1<BUF, 3>(c); k1_issue1<BUF, 4>(c);
  c.st = (c.st == 9) ? 0 : c.st + 1;
}
template <bool SWAP, int I0, int I1> DI void k1_mma(f32x4 (&acc)[2][5], const Frags& F) {
#pragma unroll
  for (int i = I0; i < I1; ++i) {
    const int k = i / 10, mt = (i / 5) & 1, nt = i % 5;
    acc[mt][nt] = SWAP ? MFMA16(F.b[k][nt], F.a[k][mt], acc[mt][nt]) : MFMA16(F.a[k][mt], F.b[k][nt], acc[mt][nt]);
  }
}
template <bool SWAP, int N, int SEG> DI void k1_seg(f32x4 (&acc)[2][5], Frags& Fn, const Frags& Fp, lptr sm, K1Ctx& c) {
  constexpr int R0[6] = {0, 4, 7, 10, 12, 14};
  k1_read<R0[SEG], R0[SEG + 1]>(Fn, sm, c.ao[N % 3], c.bo[N % 3]);
  if (N > 0) {
    k1_mma<SWAP, 4 * SEG, 4 * SEG + 4>(acc, Fp);
    if (SEG < 2) {
      __builtin_amdgcn_sched_group_barrier(0x008, 1, 0); __builtin_amdgcn_sched_group_barrier(0x100, 2, 0);
      __builtin_amdgcn_sched_group_barrier(0x008, 1, 0); __builtin_amdgcn_sched_group_barrier(0x100, 2, 0);
      __builtin_amdgcn_sched_group_barrier(0x008, 1, 0); __builtin_amdgcn_sched_group_barrier(0x100, 2, 0);
      __builtin_amdgcn_sched_group_barrier(0x008, 1, 0);
    } else {
#pragma unroll
      for (int i = 0; i < 4; ++i) { __builtin_amdgcn_sched_group_barrier(0x008, 1, 0); __builtin_amdgcn_sched_group_barrier(0x100, 2, 0); }
    }
  }
  SCHED();
  if (N + 2 <= 9) k1_issue1<(N + 2) % 3, SEG>(c);
  SCHED();
}
template <bool SWAP, int N> DI void k1_step(f32x4 (&acc)[2][5], Frags& Fn, const Frags& Fp, lptr sm, K1Ctx& c) {
  WAIT_L0();
  BAR();
  SCHED();
  k1_seg<SWAP, N, 0>(acc, Fn, Fp, sm, c);
  k1_seg<SWAP, N, 1>(acc, Fn, Fp, sm, c);
  k1_seg<SWAP, N, 2>(acc, Fn, Fp, sm, c);
  k1_seg<SWAP, N, 3>(acc, Fn, Fp, sm, c);
  k1_seg<SWAP, N, 4>(acc, Fn, Fp, sm, c);
  c.st = (c.st == 9) ? 0 : c.st + 1;
}

struct DFrags { u32x4 a[2][2]; bf16x8 b[2][3]; };
constexpr int KD_STAGE = 26624;
template <int BUF, int I> DI void kd_issue1(const K1Ctx& c) {
  if (I < 2) dma16_asm(c.ra, c.ldsa + BUF * KD_STAGE + I * 1024, c.va[I & 1], c.st * 128);
  else if (I == 2) dma16_asm(c.rb, c.ldsb + BUF * KD_STAGE, c.vb[0], c.st * 10240);
  else dma16_asm(c.rb, c.wave < 2 ? c.ldsb + BUF * KD_STAGE + 8192 : c.ldsa - 2048 * c.wave + K1_JUNK, c.vb[1], c.st * 10240);
}
template <int BUF> DI void kd_issue(K1Ctx& c) {
  kd_issue1<BUF, 0>(c); kd_issue1<BUF, 2>(c); kd_issue1<BUF, 1>(c); kd_issue1<BUF, 3>(c);
  c.st = (c.st == 9) ? 0 : c.st + 1;
}
template <int NT> DI void kd_mma(f32x4 (&acc)[2][2][3], const DFrags& F) {
#pragma unroll
  for (int mt = 0; mt < 2; ++mt) {
    u32x4 ev, od;
#pragma unroll
    for (int k = 0; k < 2; ++k)
#pragma unroll
      for (int i = 0; i < 2; ++i) {
        ev[2 * k + i] = __builtin_amdgcn_perm(F.a[mt][k][2 * i + 1], F.a[mt][k][2 * i], 0x05040100u);
        od[2 * k + i] = __builtin_amdgcn_perm(F.a[mt][k][2 * i + 1], F.a[mt][k][2 * i], 0x07060302u);
      }
#pragma unroll
    for (int nt = 0; nt < NT; ++nt) {
      acc[0][mt][nt] = MFMA16(F.b[0][nt], BC(ev), acc[0][mt][nt]);
      acc[1][mt][nt] = MFMA16(F.b[1][nt], BC(od), acc[1][mt][nt]);
    }
  }
}
template <int NT, int N> DI void kd_step(f32x4 (&acc)[2][2][3], DFrags& Fn, const DFrags& Fp, lptr sm, K1Ctx& c) {
  WAIT_L0();
  BAR();
  SCHED();
  if (N + 3 <= 9) kd_issue<(N + 3) % 4>(c);
  SCHED();
  constexpr int H = (N % 4) >> 1, O = (N & 1) * KD_STAGE;
#pragma unroll
  for (int mt = 0; mt < 2; ++mt)
#pragma unroll
    for (int k = 0; k < 2; ++k) Fn.a[mt][k] = *(LDS_AS u32x4*)(sm + c.ao[H][k] + O + mt * 2048);
#pragma unroll
  for (int par = 0; par < 2; ++par)
#pragma unroll
    for (int nt = 0; nt < NT; ++nt) Fn.b[par][nt] = *(LDS_AS bf16x8*)(sm + c.bo[H][0] + O + (par * 5 + nt) * 1024);
  if (N > 0) {
    kd_mma<NT>(acc, Fp);
    __builtin_amdgcn_sched_group_barrier(0x100, 2, 0);
    __builtin_amdgcn_sched_group_barrier(0x002, 8, 0);
#pragma unroll
    for (int i = 0; i < 2 * NT; ++i) { __builtin_amdgcn_sched_group_barrier(0x008, 1, 0); __builtin_amdgcn_sched_group_barrier(0x100, 1, 0); __builtin_amdgcn_sched_group_barrier(0x002, 2, 0); }
  }
  SCHED();
}

DI u32x2 make_tag(unsigned e) { return (u32x2){e + 1u, (e + 1u) * 0x9E3779B9u ^ 0x5bd1e995u}; }
DI int p1_rt(int b) { return (b & 1) * 8 + ((b >> 3) & 7); }
DI int p1_ct(int b) { const int x2 = (b & 7) >> 1, j = b >> 6; return j == 0 ? x2 : j == 3 ? 12 + x2 : 3 + 2 * x2 + j; }
DI int flag_slot(int b) { return p1_ct(b) * 16 + p1_rt(b); }
constexpr size_t WS_EPOCH = WS_SYN + 3 * 65536;
DI void flag_store(char* ws, int which, unsigned e, int lane) {
  __builtin_amdgcn_raw_buffer_store_b64(make_tag(e), mk_rsrc(ws + WS_SYN + which * 65536, 65536), lane * 2048 + flag_slot(blockIdx.x) * 8, 0, 16);
}
template <int NT, class FL, class FS> DI void kd_main(char* ws, lptr sm, K1Ctx& c, int rt, int cq, unsigned e, int lane, FL filt_load, FS filt_store TS_ARG) {
  const int wm = c.wave & 3, r = lane & 15, g = lane >> 4;
  constexpr int T0 = NT == 3 ? 0 : 3;
  DFrags D0, D1;
  f32x4 acc[2][2][3];
#pragma unroll
  for (int u = 0; u < 2; ++u) {
    c.ao[u][0] = 2 * u * KD_STAGE + (32 * wm + r) * 128 + ((g ^ (r >> 1)) * 16);
    c.ao[u][1] = 2 * u * KD_STAGE + (32 * wm + r) * 128 + (((4 + g) ^ (r >> 1)) * 16);
    c.bo[u][0] = 2 * u * KD_STAGE + 16384 + T0 * 1024 + lane * 16;
    asm volatile("" : "+v"(c.ao[u][0]), "+v"(c.ao[u][1]), "+v"(c.bo[u][0]));
  }
#pragma unroll
  for (int i = 0; i < 4 * NT; ++i) { acc[i / (2 * NT)][(i / NT) & 1][i % NT] = (f32x4){0.f, 0.f, 0.f, 0.f}; asm volatile("" : "+v"(acc[i / (2 * NT)][(i / NT) & 1][i % NT])); }
  TS(1);
  WAIT_V(8); kd_step<NT, 0>(acc, D0, D1, sm, c);
  TS(2);
  WAIT_V(8); kd_step<NT, 1>(acc, D1, D0, sm, c);
  filt_load();
  WAIT_V(13); kd_step<NT, 2>(acc, D0, D1, sm, c);
  WAIT_V(13); kd_step<NT, 3>(acc, D1, D0, sm, c);
  WAIT_V(13); kd_step<NT, 4>(acc, D0, D1, sm, c);
  WAIT_V(8); kd_step<NT, 5>(acc, D1, D0, sm, c);
  WAIT_V(8); kd_step<NT, 6>(acc, D0, D1, sm, c);
  filt_store();
  WAIT_V(11); kd_step<NT, 7>(acc, D1, D0, sm, c);
  WAIT_V(7); kd_step<NT, 8>(acc, D0, D1, sm, c);
  WAIT_V(3); kd_step<NT, 9>(acc, D1, D0, sm, c);
  kd_mma<NT>(acc, D1);
  TS(3);
  const rsrc_t rx = mk_rsrc(ws + WS_X1 + (wm >> 1) * 4096 + (rt >> 2) * 1024 + (rt & 3) * 256 + (wm & 1) * 128, 321 * 8192);
  const bool up = r >= 8;
  const int fb = 40 * cq + 8 * T0 + 2 * g;
  const bool sp = (fb == 0);
#pragma unroll
  for (int nt = 0; nt < NT; ++nt) {
    unsigned it[4][2];
#pragma unroll
    for (int mt = 0; mt < 2; ++mt) {
      const f32x4 E = acc[0][mt][nt], O = acc[1][mt][nt], P = E + O, M = E - O;
      it[0][mt] = pk2(P[0], P[1]); it[1][mt] = pk2(P[2], P[3]); it[2][mt] = pk2(M[0], -M[1]); it[3][mt] = pk2(M[2], -M[3]);
      if (NT == 3 && nt == 0) {
        it[0][mt] = sp ? (it[0][mt] & 0xffffu) : it[0][mt]; it[2][mt] = sp ? (it[2][mt] & 0xffffu) : it[2][mt];
        __builtin_amdgcn_raw_buffer_store_b32(pk2(E[1], O[1]), rx, sp ? 160 * 8192 + (r & 7) * 16 + (r >> 3) * 8 + mt * 4 : 0x7fffffff, 0, ST_AUX);
      }
    }
#pragma unroll
    for (int h = 0; h < 2; ++h) {
      const unsigned t0 = __builtin_amdgcn_update_dpp(0u, up ? it[2 * h][0] : it[2 * h + 1][0], 0x128, 0xf, 0xf, false);
      const unsigned t1 = __builtin_amdgcn_update_dpp(0u, up ? it[2 * h][1] : it[2 * h + 1][1], 0x128, 0xf, 0xf, false);
      const u32x4 o = {up ? t0 : it[2 * h][0], up ? t1 : it[2 * h][1], up ? it[2 * h + 1][0] : t0, up ? it[2 * h + 1][1] : t1};
      const int f0 = fb + 8 * nt + (r >> 3);
      ST16(o, rx, (h ? 320 - f0 : f0) * 8192 + (r & 7) * 16, 0);
    }
  }
}

struct Args { const u16 *x, *wqkv, *wo, *wr, *wi, *gw; char* ws; u16* out; };

DI void phase1(const Args& A, lptr sm, f32x4 (&pq)[2][5], unsigned e TS_ARG) {
  const u16* x = A.x; const u16* wqkv = A.wqkv; const u16* wr = A.wr; const u16* wi = A.wi; char* ws = A.ws;
  const int tid = threadIdx.x, lane = tid & 63;
  const int wave = __builtin_amdgcn_readfirstlane(tid >> 6);
  const int b = blockIdx.x;
  const int xcd = b & 7, idx = b >> 3;
  const int rt = p1_rt(b), ct = p1_ct(b);
  (void)xcd;
  const bool dft = ct >= 12, iskv = ct >= 4 && ct < 12;
  const int c0 = iskv ? 80 * (ct - 4) : 160 * (ct & 3);
  const int L0 = c0 >> 6, p0 = (c0 & 63) >> 4;
  const int xs = (p0 == 0 || p0 == 3) ? 1 : 2;
  const int bigB = p0 >= 2 ? 1 : 0;

  K1Ctx c;
  c.wave = wave;
  c.ra = mk_rsrc(x, 2048 * 1280);
  c.rb = dft ? mk_rsrc((const char*)&FT + (ct & 3) * 102400, 102400) : mk_rsrc(wqkv, 640 * 3840);
  c.rs = 3840;
  auto grow = [&](int row) { return dft ? 32 * (4 * rt + 2 * ((row >> 3) & 1) + ((row >> 4) & 1)) + 8 * (row >> 5) + (row & 7) : 128 * rt + row; };
#pragma unroll
  for (int i = 0; i < 2; ++i) {
    const int row = 16 * wave + 8 * i + (lane >> 3);
    c.va[i] = grow(row) * 1280 + ((lane & 7) ^ ((row >> 1) & 7)) * 16;
  }
  c.ldsa = (unsigned)(size_t)sm + 2 * wave * 1024;
  c.st = (((((idx & 7) * 5 + (idx >> 3) * 10) >> 2) % 10) * K1_ROT / 10) * (10 / K1_ROT);
  k1_issue1<0, 0>(c); k1_issue1<0, 1>(c);
  SCHED();
  {
    const int row = lane >> 3, hf = lane & 1;
    const int pair = ((lane & 7) >> 1) ^ (((row >> 1) & 1) | ((wave & 1) << 1));
    const unsigned rowb = (8 * wave + row) * c.rs;
    const bool kown = bigB ? (pair >= p0) : (pair <= p0);
    c.vb[0] = rowb + (iskv ? 1280 + (L0 + bigB) * 128 : L0 * 128) + pair * 32 + hf * 16;
    c.vb[1] = rowb + (iskv ? 2560 + (L0 + bigB) * 128 + (hf ^ 1) * 16 : (L0 + 1) * 128 + hf * 16) + pair * 32;
    c.vb[2] = rowb + (iskv ? (L0 + 1 - bigB) * 128 + (kown ? 1280 + pair * 32 + hf * 16 : 2560 + (pair ^ xs) * 32 + (hf ^ 1) * 16)
                           : (L0 + 2) * 128 + pair * 32 + hf * 16);
  }
  c.ldsb = (unsigned)(size_t)sm + 16384 + (dft ? 1 : 3) * wave * 1024;
  if (dft) { c.vb[0] = wave * 1024 + lane * 16; c.vb[1] = wave < 2 ? c.vb[0] + 8192 : 0x7fffffffu; }
  if (dft) { kd_issue1<0, 2>(c); kd_issue1<0, 3>(c); }
  else { k1_issue1<0, 2>(c); k1_issue1<0, 3>(c); k1_issue1<0, 4>(c); }
  c.st = (c.st == 9) ? 0 : c.st + 1;
  BAR();
  if (dft) { kd_issue<1>(c); kd_issue<2>(c); }
  else k1_issue<1>(c);

  const int wm = wave >> 1, wn = wave & 1;
  const int r = lane & 15, g = lane >> 4, q = (lane & 15) >> 2, p = lane & 3;
  const int hsw = ((q >> 1) & 1) | ((g & 1) << 1);
  const unsigned bbase = 16384 + g * 3072 + q * 128;
  c.ao[0][0] = (32 * wm + r) * 128 + ((g ^ (r >> 1)) * 16);
  c.ao[0][1] = (32 * wm + r) * 128 + (((4 + g) ^ (r >> 1)) * 16);
  f32x4 acc[2][5];
  Frags F0, F1;
  const rsrc_t rpf = wave == 0 ? mk_rsrc(A.wo, dft ? 0 : 819200) : wave == 1 ? mk_rsrc(&GT, dft ? 0 : 491520)
                   : wave == 2 ? mk_rsrc(ws, (unsigned)WS_EPOCH + 4) : mk_rsrc(A.out, wave == 3 ? 2621440 : 0);
  const unsigned vpf = wave >= 2 ? (TLB_STRIDE ? (lane * 32 + idx) * TLB_STRIDE : 0x7fffffffu)
                     : (lane < (wave ? 20 : 34)) ? ((ct * 16 + rt) * (wave ? 20 : 34) + lane) * 128 : 0x7fffffffu;

#define K1_HEAD(SWAP, HOOK, W2)                                                                          \
  _Pragma("unroll") for (int u = 1; u < 3; ++u) {                                              \
    _Pragma("unroll") for (int k = 0; k < 2; ++k) { c.ao[u][k] = c.ao[0][k] + u * K1_STAGE; asm volatile("" : "+v"(c.ao[u][k])); }     \
    _Pragma("unroll") for (int k = 0; k < 5; ++k) { c.bo[u][k] = c.bo[0][k] + u * K1_STAGE; asm volatile("" : "+v"(c.bo[u][k])); }     \
  }                                                                                            \
  _Pragma("unroll") for (int mt = 0; mt < 2; ++mt)                                             \
    _Pragma("unroll") for (int nt = 0; nt < 5; ++nt) asm volatile("" : "+v"(acc[mt][nt]));     \
  TS(1);                                                                                       \
  WAIT_V(5); k1_step<SWAP, 0>(acc, F0, F1, sm, c);                                           \
  TS(2);                                                                                       \
  WAIT_V(5); k1_step<SWAP, 1>(acc, F1, F0, sm, c); HOOK;                                             \
  WAIT_V(W2); k1_step<SWAP, 2>(acc, F0, F1, sm, c);                                             \
  pf_asm(rpf, (unsigned)(size_t)sm + K1_JUNK + wave * 256, vpf, 0);

  if (iskv) {
#pragma unroll
    for (int nt = 0; nt < 5; ++nt) {
      const int u = 4 * nt + p;
      const int isv = u >= 10 ? 1 : 0;
      const int pc = 4 * p0 + 10 * wn + u - 10 * isv;
      const int gp = pc >> 2, big = ((gp >> 2) == bigB);
      const int slot = (gp & 3) ^ ((!big && isv) ? xs : 0);
      c.bo[0][nt] = bbase + (big ? isv : 2) * 1024 + ((slot ^ hsw) * 32) + (((pc & 3) ^ (isv << 1)) * 8);
      const float i0 = ((nt < 2) || (nt == 2 && r < 8)) ? kEps : 0.f;
#pragma unroll
      for (int mt = 0; mt < 2; ++mt) acc[mt][nt] = (f32x4){i0, i0, i0, i0};
    }
    unsigned ko[10];
    {
      constexpr int TI[10] = {0, 0, 1, 1, 1, 2, 2, 0, 1, 2}, TJ[10] = {2, 3, 2, 3, 4, 3, 4, -1, -1, -1};
#pragma unroll
      for (int i = 0; i < 10; ++i) {
        const int ti = TI[i], tj = TJ[i];
        const int ur = 16 * ti + r, hk = ur >= 20 ? 1 : 0;
        const int uc = 16 * tj + 4 * g - 40, hv = uc >= 20 ? 1 : 0;
        const bool valid = tj < 0 ? (ur < 40 && g == 0) : (ur < 40 && uc >= 0 && hk == hv);
        const int off = wave * 3840 + hk * 1920 + (ur - 20 * hk) * 96 + (tj < 0 ? 80 : (uc - 20 * hv) * 4);
        ko[i] = K1_STAGE + (valid ? off : 8 * 3840 + lane * 16);
        asm volatile("" : "+v"(ko[i]));
      }
    }
    K1_HEAD(false, , 5)
    WAIT_V(6); k1_step<false, 3>(acc, F1, F0, sm, c);
    WAIT_V(6); k1_step<false, 4>(acc, F0, F1, sm, c);
    WAIT_V(5); k1_step<false, 5>(acc, F1, F0, sm, c);
    WAIT_V(5); k1_step<false, 6>(acc, F0, F1, sm, c);
    WAIT_V(5); k1_step<false, 7>(acc, F1, F0, sm, c);
    WAIT_V(5); k1_step<false, 8>(acc, F0, F1, sm, c);
    WAIT_V(0); k1_step<false, 9>(acc, F1, F0, sm, c);
    if ((ct - 4) * 16 + rt >= 88) {
      const int v = (ct - 4) * 16 + rt - 88, cp = 2 * (v & 1) + (wave >> 2), e0 = 20 * ((32 * (v >> 1)) / 20);
      const int line = ((wave & 2) ? (1120 - 160 * cp) >> 7 : (160 * cp) >> 7) + (wave & 1), row = lane >> 3;
      const rsrc_t rwo = mk_rsrc(A.wo, 640 * 1280);
#pragma unroll
      for (int kb = 0; kb < 8; ++kb) {
        const int pair = ((lane & 7) >> 1) ^ (((row >> 1) & 1) | ((kb & 1) << 1));
        dma16_asm(rwo, (unsigned)(size_t)sm + 2 * K1_STAGE + (wave * 8 + kb) * 1024, min(e0 + 8 * kb + row, 639) * 1280 + line * 128 + pair * 32 + (lane & 1) * 16, 0);
      }
      SCHED();
    }
    k1_mma<false, 0, 20>(acc, F1);
    TS(3);
    bf16x8 fr[5];
#pragma unroll
    for (int nt = 0; nt < 5; ++nt) {
#pragma unroll
      for (int m = 0; m < 2; ++m)
#pragma unroll
        for (int j = 0; j < 4; ++j) {
          if (nt < 2) acc[m][nt][j] = phi_f(acc[m][nt][j]);
          if (nt == 2) { const float ph = phi_f(acc[m][nt][j]); acc[m][nt][j] = (r < 8) ? ph : acc[m][nt][j]; }
        }
      fr[nt] = pack8(acc[0][nt], acc[1][nt]);
    }
    const bf16x8 ones = {0x3f80, 0x3f80, 0x3f80, 0x3f80, 0x3f80, 0x3f80, 0x3f80, 0x3f80};
    {
      constexpr int TI[10] = {0, 0, 1, 1, 1, 2, 2, 0, 1, 2}, TJ[10] = {2, 3, 2, 3, 4, 3, 4, -1, -1, -1};
      const f32x4 zz = {0.f, 0.f, 0.f, 0.f};
      f32x4 d[10];
#pragma unroll
      for (int i = 0; i < 10; ++i) d[i] = MFMA16(TJ[i] < 0 ? ones : fr[TJ[i] < 0 ? 0 : TJ[i]], fr[TI[i]], zz);
#pragma unroll
      for (int i = 0; i < 10; ++i) *(LDS_AS f32x4*)(sm + ko[i]) = d[i];
    }
    WAIT_L0();
    BAR();
    const rsrc_t rk = mk_rsrc(ws + WS_KVP + ((size_t)rt * 32 + 4 * (ct - 4)) * 1920, 7680);
    LDS_AS f32x4* src = (LDS_AS f32x4*)(sm + K1_STAGE);
    const f32x4 s0 = (src[tid] + src[tid + 480]) + (src[tid + 960] + src[tid + 1440]);
    ST16(__builtin_bit_cast(u32x4, s0), rk, tid * 16, 0);
    if (wave >= 6) {
      LDS_AS float* zs = (LDS_AS float*)(sm + K1_STAGE) + min(tid - 384, 79) * 24 + 20;
      const float z = (zs[0] + zs[1920]) + (zs[3840] + zs[5760]);
      __builtin_amdgcn_raw_buffer_store_b32(__float_as_uint(z), mk_rsrc(ws + WS_ZP + rt * 2560 + (ct - 4) * 320, 320), (tid - 384) * 4, 0, ST_AUX);
    }
  } else {
    const int un = (dft ? 64 : 0) + rt * 4 + (ct & 3), mo = un >> 1, fh = un & 1;
    auto filt_load = [&]() {
      const rsrc_t rf = mk_rsrc((wave >> 2) ? wi : wr, 2048 * 642);
      const int kb = 16 * (mo >> 4) + 8 * (mo & 1) + 64 * (16 * ((mo >> 3) & 1) + 4 * ((mo >> 1) & 3) + (wave & 3));
      const unsigned v0 = (lane & 31) < 21 ? (lane >> 5) * 640 + (lane & 31) * 16 : 0x7fffffffu;
#pragma unroll
      for (int i = 0; i < 4; ++i)
        dma16_asm(rf, (unsigned)(size_t)sm + K1_WF + (wave * 4 + i) * 1024, v0, kb * 642 + 320 * fh + i * 1280);
    };
    auto filt_store = [&]() {
      const rsrc_t rw = mk_rsrc(ws + WS_WT + (size_t)fh * 160 * 8192 + mo * 128, (160 + fh) * 8192 - mo * 128);
      const int i8 = lane & 7;
#pragma unroll
      for (int rd = 0; rd < 3; ++rd) {
        const int fl = (rd * 8 + wave) * 8 + (lane >> 3);
        lptr src = sm + K1_WF + i8 * 514 + min(fl, 160) * 2;
        u32x4 o;
#pragma unroll
        for (int j = 0; j < 4; ++j) o[j] = (unsigned)*(LDS_AS u16*)(src + j * 4096) | ((unsigned)*(LDS_AS u16*)(src + 16384 + j * 4096) << 16);
        ST16(o, rw, fl * 8192 + i8 * 16, 0);
      }
      SCHED();
    };
    if (!dft) {
#pragma unroll
      for (int nt = 0; nt < 5; ++nt) {
        const int gp = p0 + 5 * wn + nt;
        c.bo[0][nt] = bbase + (gp >> 2) * 1024 + (((gp & 3) ^ hsw) * 32) + p * 8;
      }
#pragma unroll
      for (int mt = 0; mt < 2; ++mt)
#pragma unroll
        for (int nt = 0; nt < 5; ++nt) acc[mt][nt] = (f32x4){kEps, kEps, kEps, kEps};
      K1_HEAD(true, filt_load(), 9)
      WAIT_V(10); k1_step<true, 3>(acc, F1, F0, sm, c);
      WAIT_V(6); k1_step<true, 4>(acc, F0, F1, sm, c);
      filt_store();
      WAIT_V(8); k1_step<true, 5>(acc, F1, F0, sm, c);
      WAIT_V(8); k1_step<true, 6>(acc, F0, F1, sm, c);
      WAIT_V(5); k1_step<true, 7>(acc, F1, F0, sm, c);
      if (wave == 0) flag_store(ws, 1, e, lane);
      WAIT_V(5); k1_step<true, 8>(acc, F0, F1, sm, c);
      WAIT_V(0); k1_step<true, 9>(acc, F1, F0, sm, c);
      k1_mma<true, 0, 20>(acc, F1);
      TS(3);
#pragma unroll
      for (int mt = 0; mt < 2; ++mt)
#pragma unroll
        for (int nt = 0; nt < 5; ++nt)
#pragma unroll
          for (int j = 0; j < 4; ++j) pq[mt][nt][j] = phi_f(acc[mt][nt][j]);
    } else {
      auto filt_load2 = [&]() { filt_load(); pf_asm(rpf, (unsigned)(size_t)sm + K1_JUNK + 1024 + (wave & 3) * 256, vpf, 0); };
      if (wave < 4) kd_main<3>(ws, sm, c, rt, ct & 3, e, lane, filt_load2, filt_store TS_PASS);
      else kd_main<2>(ws, sm, c, rt, ct & 3, e, lane, filt_load2, filt_store TS_PASS);
    }
  }
  TS(4);
}

DI void publish(char* ws, int which, unsigned e) {
  WAIT_V(0);
  BAR();
  if (threadIdx.x < 64) flag_store(ws, which, e, threadIdx.x);
}
DI bool poll(char* ws, int which, unsigned e, bool n0, bool n1, int lane, unsigned tries = 1u << 17) {
  const rsrc_t rf = mk_rsrc(ws + WS_SYN + which * 65536 + (blockIdx.x & 31) * 2048, 2048);
  const u32x2 t = make_tag(e);
  for (unsigned spin = 0; spin < tries; ++spin) {
    const u32x4 f0 = __builtin_amdgcn_raw_buffer_load_b128(rf, n0 ? lane * 16 : 0x7fffffffu, 0, 16);
    const u32x4 f1 = __builtin_amdgcn_raw_buffer_load_b128(rf, n1 ? lane * 16 + 1024 : 0x7fffffffu, 0, 16);
    n0 = n0 && !(f0[0] == t[0] && f0[1] == t[1] && f0[2] == t[0] && f0[3] == t[1]);
    n1 = n1 && !(f1[0] == t[0] && f1[1] == t[1] && f1[2] == t[0] && f1[3] == t[1]);
    if (!__any(n0 || n1)) return true;
    __builtin_amdgcn_s_sleep(POLL_SLEEP);
  }
  return false;
}
DI void await(char* ws, int which, unsigned e, int need, int wave, int lane) {
  if (wave == 7) poll(ws, which, e, (need >> (lane < 32 ? 0 : 1)) & 1, (need >> (lane < 32 ? 1 : 2)) & 1, lane);
  asm volatile("" ::: "memory");
  BAR();
  asm volatile("" ::: "memory");
}
DI void dma16_sc1(rsrc_t rs, unsigned lds, unsigned vo, int so) {
  asm volatile("s_mov_b32 m0, %2\n\ts_nop 0\n\tbuffer_load_dwordx4 %0, %1, %3 offen sc1 lds" :: "v"(vo), "s"(rs), "s"(lds), "s"(so));
}
#define LD16(rs, vo, so) __builtin_amdgcn_raw_buffer_load_b128((rs), (vo), (so), LD_AUX)

constexpr int P3_HALF = 71680;

struct FftC {
  const u32x4 (&fre)[4]; const u32x4 (&fim)[4]; const u32x4 (&fc)[2]; const u32x4 (&fs)[2]; const u32x4 (&fn)[2];
  const f32x2 (&tc)[2][2]; const f32x2 (&tsn)[2][2]; const unsigned (&uo)[2][4]; const unsigned (&ro)[4]; unsigned vy;
};
DI f32x2 half2(const f32x4& v, int h) { return h ? (f32x2){v[2], v[3]} : (f32x2){v[0], v[1]}; }
constexpr int FSECOND = 304;
template <int NC> DI void fft_run(char* ws, lptr sm, const FftC& T, const u32x4 (&wv)[2][2], int fu0, int wave8, int lane) {
  const f32x4 z4 = {0.f, 0.f, 0.f, 0.f};
  f32x2 wre[NC][2][2], wim[NC][2][2];
#pragma unroll
  for (int c = 0; c < NC; ++c) {
    const int f = min((c ? FSECOND + (fu0 >> 1) : fu0), 320);
    const float sc = ((f == 0 || f == 320) ? 1.f : 2.f) / (2048.f * 640.f);
#pragma unroll
    for (int t = 0; t < 2; ++t)
#pragma unroll
      for (int h = 0; h < 2; ++h) {
        wre[c][t][h] = (f32x2){bf_lo(wv[c][t][2 * h]), bf_lo(wv[c][t][2 * h + 1])} * sc;
        wim[c][t][h] = (f32x2){bf_hi(wv[c][t][2 * h]), bf_hi(wv[c][t][2 * h + 1])} * sc;
      }
  }
#define FOR_C _Pragma("unroll") for (int c = 0; c < NC; ++c)
#define FOR_T _Pragma("unroll") for (int t = 0; t < 2; ++t)
#define FOR_H _Pragma("unroll") for (int h = 0; h < 2; ++h)
#define FOR_K _Pragma("unroll") for (int ks = 0; ks < 4; ++ks)
  f32x4 re[NC][2], im[NC][2];
  u32x4 bre[NC], bim[NC];
  {
    bf16x8 xa[NC][2][4];
    FOR_K FOR_C FOR_T xa[c][t][ks] = *(LDS_AS bf16x8*)(sm + c * 16384 + lane * 16 + (t * 4 + ks) * 1024);
    FOR_K FOR_C FOR_T {
      re[c][t] = MFMA16(xa[c][t][ks], BC(T.fre[ks]), ks ? re[c][t] : z4);
      im[c][t] = MFMA16(xa[c][t][ks], BC(T.fim[ks]), ks ? im[c][t] : z4);
    }
  }
  FOR_C FOR_T FOR_H {
    const f32x2 a = half2(re[c][t], h), d = half2(im[c][t], h);
    const f32x2 x = a * T.tc[t][h] + d * T.tsn[t][h], y = d * T.tc[t][h] - a * T.tsn[t][h];
    bre[c][2 * t + h] = pk2(x[0], x[1]); bim[c][2 * t + h] = pk2(y[0], y[1]);
  }
  FOR_C FOR_T { re[c][t] = MFMA16(BC(T.fc[t]), BC(bre[c]), z4); im[c][t] = MFMA16(BC(T.fc[t]), BC(bim[c]), z4); }
  FOR_C FOR_T { re[c][t] = MFMA16(BC(T.fs[t]), BC(bim[c]), re[c][t]); im[c][t] = MFMA16(BC(T.fn[t]), BC(bre[c]), im[c][t]); }
  FOR_C FOR_T FOR_H {
    const f32x2 a = half2(re[c][t], h), d = half2(im[c][t], h);
    const f32x2 x = a * wre[c][t][h] - d * wim[c][t][h], y = a * wim[c][t][h] + d * wre[c][t][h];
    bre[c][2 * t + h] = pk2(x[0], x[1]); bim[c][2 * t + h] = pk2(y[0], y[1]);
  }
  FOR_C FOR_T { re[c][t] = MFMA16(BC(T.fc[t]), BC(bre[c]), z4); im[c][t] = MFMA16(BC(T.fc[t]), BC(bim[c]), z4); }
  FOR_C FOR_T { re[c][t] = MFMA16(BC(T.fn[t]), BC(bim[c]), re[c][t]); im[c][t] = MFMA16(BC(T.fs[t]), BC(bre[c]), im[c][t]); }
  FOR_C FOR_T FOR_H {
    const f32x2 a = half2(re[c][t], h), d = half2(im[c][t], h);
    const f32x2 x = a * T.tc[t][h] - d * T.tsn[t][h], y = -(d * T.tc[t][h]) - a * T.tsn[t][h];
    *(LDS_AS unsigned*)(sm + c * 16384 + T.uo[t][2 * h]) = pk2(x[0], y[0]);
    *(LDS_AS unsigned*)(sm + c * 16384 + T.uo[t][2 * h + 1]) = pk2(x[1], y[1]);
  }
  WAIT_L0();
  BAR();
  SCHED();
  {
    bf16x8 ua[NC][2][4];
    FOR_K FOR_C FOR_T ua[c][t][ks] = *(LDS_AS bf16x8*)(sm + c * 16384 + T.ro[ks] + t * 4096);
    FOR_K FOR_C FOR_T {
      re[c][t] = MFMA16(ua[c][t][ks], BC(T.fre[ks]), ks ? re[c][t] : z4);
      im[c][t] = MFMA16(ua[c][t][ks], BC(T.fim[ks]), ks ? im[c][t] : z4);
    }
  }
  FOR_C {
    const int fu = (c ? FSECOND + (fu0 >> 1) : fu0), f = min(fu, 320);
    const rsrc_t ry = mk_rsrc(ws + WS_Y + (size_t)f * 4096, fu > 320 ? 0 : ((f == 0 || f == 320) ? 4096 : 321 * 4096));
    ST16(widen((u32x2){pk2(re[c][0][0], re[c][0][1]), pk2(re[c][0][2], re[c][0][3])}, (u32x2){pk2(re[c][1][0], re[c][1][1]), pk2(re[c][1][2], re[c][1][3])}), ry, T.vy, 0);
    ST16(widen((u32x2){pk2(im[c][0][0], im[c][0][1]), pk2(im[c][0][2], im[c][0][3])}, (u32x2){pk2(im[c][1][0], im[c][1][1]), pk2(im[c][1][2], im[c][1][3])}), ry, T.vy, 320 * 4096);
  }
}

template <bool F> DI void phase2(const Args& A, lptr sm0, unsigned e, const f32x4 (&pq)[2][5] TS_ARG) {
  char* ws = A.ws;
  const int lane = threadIdx.x & 63, tid = threadIdx.x & 255;
  const int wave8 = __builtin_amdgcn_readfirstlane(threadIdx.x >> 6);
  const int wave = wave8 & 3, team = wave8 >> 2;
  lptr sm = sm0 + team * P3_HALF;
  const unsigned smu = (unsigned)(size_t)sm;
  const int b = blockIdx.x;
  const int rt1 = p1_rt(b), ct1 = p1_ct(b);
  const bool iskv = ct1 >= 4 && ct1 < 12;
  const int v = (ct1 - 4) * 16 + rt1;
  const int r = lane & 15, g = lane >> 4;
  const f32x4 z4 = {0.f, 0.f, 0.f, 0.f};
  const bool isfft = ct1 >= 12 || (iskv && v < 88);
  publish(ws, 1, e);
  TS(5);
  if (isfft) {
    const int fu0 = (iskv ? 128 + 2 * v : 2 * (rt1 * 4 + (ct1 & 3))) + team;
    const bool two = fu0 < 34 && team == 0;
    u32x4 wv[2][2];
    auto loads = [&]() {
#pragma unroll
      for (int c = 0; c < 2; ++c) {
        const int f = min(c ? FSECOND + (fu0 >> 1) : fu0, 320);
        const rsrc_t rx = mk_rsrc(ws + WS_X1 + (size_t)f * 8192, (c && !two) ? 0 : 8192), rw = mk_rsrc(ws + WS_WT + (size_t)f * 8192, (c && !two) ? 0 : 8192);
        dma16_sc1(rx, smu + c * 16384 + wave * 2048, lane * 16, wave * 2048);
        dma16_sc1(rx, smu + c * 16384 + wave * 2048 + 1024, lane * 16, wave * 2048 + 1024);
        wv[c][0] = LD16(rw, lane * 16, wave * 2048);
        wv[c][1] = LD16(rw, lane * 16 + 1024, wave * 2048);
      }
      SCHED();
    };
    await(ws, 1, e, 5, wave8, lane);
    TS(7);
    loads();
    u32x4 fre[4], fim[4], fc[2], fs[2], fn[2];
    f32x2 tc[2][2], tsn[2][2];
    unsigned uo[2][4], ro[4];
    {
      const float a64 = (float)(16 * wave + r) * (1.f / 64.f);
      const unsigned m2 = (r & 2) ? 0x80008000u : 0u, m1 = (r & 1) ? 0x80008000u : 0u;
      const unsigned m3 = ((r + 1) & 2) ? 0x80008000u : 0u;
#pragma unroll
      for (int j = 0; j < 4; ++j) {
        const float ang = a64 * (float)(4 * g + j);
        const float c = __builtin_amdgcn_cosf(ang), s = __builtin_amdgcn_sinf(ang);
        const unsigned p = pk2(c, s), q = pk2(-s, c);
        fre[0][j] = p; fim[0][j] = q;
        fre[1][j] = ((r & 1) ? q : p) ^ m2;
        fim[1][j] = ((r & 1) ? p : q) ^ m3;
        fre[2][j] = p ^ m1; fim[2][j] = q ^ m1;
        fre[3][j] = fre[1][j] ^ m1; fim[3][j] = fim[1][j] ^ m1;
      }
      const float a32 = (float)r * (1.f / 32.f);
#pragma unroll
      for (int j = 0; j < 2; ++j) {
        const float a0 = a32 * (float)(4 * g + 2 * j), a1 = a0 + a32;
        const unsigned c = pk2(__builtin_amdgcn_cosf(a0), __builtin_amdgcn_cosf(a1)), s = pk2(__builtin_amdgcn_sinf(a0), __builtin_amdgcn_sinf(a1));
        fc[0][j] = c; fc[0][2 + j] = c ^ m1; fs[0][j] = s; fs[0][2 + j] = s ^ m1;
      }
#pragma unroll
      for (int j = 0; j < 4; ++j) {
        fc[1][j] = fc[0][j] ^ 0x80000000u; fs[1][j] = fs[0][j] ^ 0x80000000u;
        fn[0][j] = fs[0][j] ^ 0x80008000u; fn[1][j] = fs[0][j] ^ 0x00008000u;
        ro[j] = 8192 + r * 256 + (((4 * j + g) ^ r) * 16);
      }
#pragma unroll
      for (int t = 0; t < 2; ++t)
#pragma unroll
        for (int j = 0; j < 4; ++j) {
          const int s2 = 16 * t + 4 * g + j;
          const float a = a64 * (1.f / 32.f) * (float)s2;
          tc[t][j >> 1][j & 1] = __builtin_amdgcn_cosf(a);
          tsn[t][j >> 1][j & 1] = __builtin_amdgcn_sinf(a);
          uo[t][j] = 8192 + s2 * 256 + (((4 * wave + (r >> 2)) ^ (s2 & 15)) * 16) + (r & 3) * 4;
        }
    }
    unsigned vy = wave * 1024 + r * 64 + (g & 1) * 32 + (g >> 1) * 16;
#pragma unroll
    for (int ks = 0; ks < 4; ++ks) { asm volatile("" : "+v"(fre[ks]), "+v"(fim[ks]), "+v"(ro[ks])); }
#pragma unroll
    for (int t = 0; t < 2; ++t) {
      asm volatile("" : "+v"(fc[t]), "+v"(fs[t]), "+v"(fn[t]), "+v"(tc[t][0]), "+v"(tc[t][1]), "+v"(tsn[t][0]), "+v"(tsn[t][1]));
      asm volatile("" : "+v"(uo[t][0]), "+v"(uo[t][1]), "+v"(uo[t][2]), "+v"(uo[t][3]), "+v"(vy));
    }
    SCHED();
    TS(6);
    FftC T = {fre, fim, fc, fs, fn, tc, tsn, uo, ro, vy};
    WAIT_V(0);
    asm volatile("" : "+v"(wv[0][0]), "+v"(wv[0][1]), "+v"(wv[1][0]), "+v"(wv[1][1]));
    BAR();
    SCHED();
    TS(8);
    if (two) fft_run<2>(ws, sm, T, wv, fu0, wave8, lane);
    else fft_run<1>(ws, sm, T, wv, fu0, wave8, lane);
  } else if (iskv) {
    const int ks = (v - 88) >> 1, cp = 2 * ((v - 88) & 1) + team;
    const int hf = (32 * ks) / 20, e0 = 20 * hf;
    const int q = r >> 2, p = lane & 3;
    const int hsw = ((q >> 1) & 1) | ((g & 1) << 1);
    const int L1 = (160 * cp) >> 7, L2 = (1120 - 160 * cp) >> 7;
    lptr wb = sm0 + 2 * K1_STAGE + team * 32768;
    lptr ab = sm0 + 32768;
    if (threadIdx.x < 288) *(LDS_AS u32x4*)(ab + threadIdx.x * 16) = (u32x4){0u, 0u, 0u, 0u};
    const int t5 = min((int)threadIdx.x >> 1, 159), kl = t5 / 5, qd = t5 - kl * 5;
    const int k = 32 * ks + kl, h = k / 20, d = k - 20 * h;
    unsigned ko = h * 1920 + d * 96 + qd * 16 + (threadIdx.x & 1) * 8 * 61440, ao = kl * 144 + (20 * (h - hf) + 4 * qd) * 2;
    unsigned bo[3];
#pragma unroll
    for (int n = 0; n < 3; ++n) {
      const int tl = min(wave + 4 * n, 9);
      const int ctl = tl >= 5 ? 1 : 0, nt = tl - 5 * ctl, ctg = 2 * cp + ctl;
      const int i = 16 * nt + 4 * p;
      const int byte = 2 * (i < 40 ? 40 * ctg + i : 560 - 40 * ctg + i);
      const int pl = i < 40 ? (byte >> 7) - L1 : 2 + (byte >> 7) - L2;
      bo[n] = (pl * 8 + g) * 1024 + q * 128 + ((((byte >> 5) & 3) ^ hsw) * 32) + (byte & 31);
      asm volatile("" : "+v"(bo[n]));
    }
    asm volatile("" : "+v"(ko), "+v"(ao));
    WAIT_L0();
    TS(6);
    await(ws, 1, e, 2, wave8, lane);
    TS(7);
    f32x4 pv[8];
    const rsrc_t rk = mk_rsrc(ws + WS_KVP, wave8 < 5 ? 983040 : 0);
#pragma unroll
    for (int i = 0; i < 8; ++i) pv[i] = __builtin_bit_cast(f32x4, LD16(rk, ko, i * 61440));
    SCHED();
    bf16x8 bw[3][2];
#pragma unroll
    for (int n = 0; n < 3; ++n)
#pragma unroll
      for (int s = 0; s < 2; ++s) {
        const s16x4 lo = TRREAD(wb + bo[n] + s * 4096), hi = TRREAD(wb + bo[n] + s * 4096 + 512);
        bw[n][s] = __builtin_shufflevector(lo, hi, 0, 1, 2, 3, 4, 5, 6, 7);
      }
    SCHED();
    f32x4 sum = ((pv[0] + pv[1]) + (pv[2] + pv[3])) + ((pv[4] + pv[5]) + (pv[6] + pv[7]));
#pragma unroll
    for (int j = 0; j < 4; ++j) sum[j] += __uint_as_float(__builtin_amdgcn_update_dpp(0u, __float_as_uint(sum[j]), 0xB1, 0xf, 0xf, false));
    if (threadIdx.x < 320 && !(threadIdx.x & 1)) *(LDS_AS u32x2*)(ab + ao) = (u32x2){pk2(sum[0], sum[1]), pk2(sum[2], sum[3])};
    WAIT_L0();
    BAR();
    TS(8);
    bf16x8 a[2][2];
#pragma unroll
    for (int t = 0; t < 2; ++t)
#pragma unroll
      for (int s = 0; s < 2; ++s) a[t][s] = *(LDS_AS bf16x8*)(ab + (8 * (r >> 2) + (r & 3) + 4 * t) * 144 + s * 64 + g * 16);
    const rsrc_t rm = mk_rsrc(ws + WS_M + (size_t)(10 * cp) * 20480 + ks * 1024, 9 * 20480 + 1024);
#pragma unroll
    for (int n = 0; n < 3; ++n) {
      f32x4 d1 = MFMA16(a[0][0], bw[n][0], z4), d2 = MFMA16(a[1][0], bw[n][0], z4);
      d1 = MFMA16(a[0][1], bw[n][1], d1); d2 = MFMA16(a[1][1], bw[n][1], d2);
      ST16(__builtin_bit_cast(u32x4, pack8(d1, d2)), rm, lane * 16, (wave + 4 * n) * 20480);
    }
  } else if (ct1 < 4) {
    const int t8 = threadIdx.x, wm = wave8 >> 1, wn = wave8 & 1;
    const int gb = rt1 * 4 + ct1;
      const rsrc_t rx = mk_rsrc(A.x + (size_t)gb * 32 * 640, 32 * 1280), rg = mk_rsrc(A.gw, 1280);
    u32x4 gx[5], gg[5];
#pragma unroll
      for (int i = 0; i < 5; ++i) {
        gx[i] = __builtin_amdgcn_raw_buffer_load_b128(rx, t8 * 80 + i * 16, 0, 0);
        gg[i] = __builtin_amdgcn_raw_buffer_load_b128(rg, (t8 & 15) * 80 + i * 16, 0, 0);
      }
    u32x2 pk[2][5];
    unsigned zm[5];
#pragma unroll
    for (int nt = 0; nt < 5; ++nt) {
#pragma unroll
      for (int mt = 0; mt < 2; ++mt) {
        pk[mt][nt] = (u32x2){pk2(pq[mt][nt][0], pq[mt][nt][1]), pk2(pq[mt][nt][2], pq[mt][nt][3])};
        asm volatile("" : "+v"(pk[mt][nt]));
      }
      zm[nt] = ((4 * nt + g) / 5 == (r & 3)) ? 0xffffffffu : 0u;
      asm volatile("" : "+v"(zm[nt]));
    }
    const rsrc_t rz = mk_rsrc(ws + WS_ZP + ct1 * 640, 40960);
    const rsrc_t rp = mk_rsrc(ws + WS_P + (size_t)(8 * rt1 + 2 * wm) * 20480 + (20 * ct1 + 10 * wn) * 256, 2 * 20480);
    const unsigned vo = (g & 1) * 20480 + (g >> 1) * 256 + r * 16;
    SCHED();
    TS(6);
    await(ws, 1, e, 2, wave8, lane);
    TS(7);
    if (wave8 < 3) {
      float zv[16];
#pragma unroll
      for (int i = 0; i < 16; ++i) zv[i] = __uint_as_float(__builtin_amdgcn_raw_buffer_load_b32(rz, min(t8, 159) * 4, i * 2560, LD_AUX));
#pragma unroll
      for (int i = 0; i < 8; ++i) zv[i] += zv[i + 8];
      const float zs = ((zv[0] + zv[1]) + (zv[2] + zv[3])) + ((zv[4] + zv[5]) + (zv[6] + zv[7]));
      *(LDS_AS u16*)(sm0 + t8 * 2) = (u16)pk2(zs, zs);
    }
    WAIT_L0();
    BAR();
    TS(8);
    u32x2 zq[6];
#pragma unroll
    for (int nt = 0; nt < 5; ++nt) { zq[nt] = *(LDS_AS u32x2*)(sm0 + wn * 160 + nt * 32 + g * 8); zq[nt][0] &= zm[nt]; zq[nt][1] &= zm[nt]; }
    zq[5] = (u32x2){0u, 0u};
    u32x2 po[2][5];
#pragma unroll
    for (int mt = 0; mt < 2; ++mt) {
      f32x4 den = {kEps, kEps, kEps, kEps};
#pragma unroll
      for (int m = 0; m < 3; ++m) {
        const u32x4 za = {zq[2 * m][0], zq[2 * m][1], zq[2 * m + 1][0], zq[2 * m + 1][1]};
        const u32x2 p1 = m < 2 ? pk[mt][2 * m + 1] : (u32x2){0u, 0u};
        const u32x4 pb = {pk[mt][2 * m][0], pk[mt][2 * m][1], p1[0], p1[1]};
        den = MFMA16(BC(za), BC(pb), den);
      }
      f32x4 inv;
#pragma unroll
      for (int j = 0; j < 4; ++j) inv[j] = __builtin_amdgcn_rcpf(den[j]);
#pragma unroll
      for (int nt = 0; nt < 5; ++nt) {
        const float iv = nt == 0 ? inv[0] : nt == 4 ? inv[3] : (g < nt ? inv[nt - 1] : inv[nt]);
        const f32x4 w = pq[mt][nt] * iv;
        po[mt][nt] = (u32x2){pk2(w[0], w[1]), pk2(w[2], w[3])};
      }
    }
#pragma unroll
    for (int nt = 0; nt < 5; ++nt) ST16(widen(po[0][nt], po[1][nt]), rp, vo + nt * 512, 0);
    SCHED();
    {
      float s0 = 0.f, s1 = 0.f;
#pragma unroll
      for (int i = 0; i < 5; ++i) { s0 = dot2(gx[i][0], gg[i][0], s0); s1 = dot2(gx[i][1], gg[i][1], s1); s0 = dot2(gx[i][2], gg[i][2], s0); s1 = dot2(gx[i][3], gg[i][3], s1); }
      float s = s0 + s1;
#pragma unroll
      for (int o = 8; o > 0; o >>= 1) s += __shfl_xor(s, o);
      if ((t8 & 15) == 0) __builtin_amdgcn_raw_buffer_store_b32(__float_as_uint(1.f / (1.f + __expf(-s))), mk_rsrc(ws + WS_G, 8192), (gb * 32 + (t8 >> 4)) * 4, 0, 16);
    }
  }
  TS(9);
}

constexpr int LDS_ALL = K1_LDS;
static_assert(81920 + (2 * 64 * 84 + 2 * 64 * 52 + 64) * 4 <= LDS_ALL, "lds");

template <bool F> DI void phase3(const Args& A, lptr sm, unsigned e TS_ARG) {
  char* ws = A.ws;
  const int tid = threadIdx.x, lane = tid & 63;
  const int wave = __builtin_amdgcn_readfirstlane(tid >> 6);
  const int b = blockIdx.x, xcd = b & 7, idx = b >> 3;
  const int rt = xcd * 4 + (idx & 3);
  const int ct = idx >> 2;
  const int r = lane & 15, g = lane >> 4;
  LDS_AS float* red = (LDS_AS float*)(sm + 81920);
  constexpr int RA = 64 * 84, RE = 64 * 52, RG = 2 * RA + 2 * RE;
  const bool rev = P3_REV && (((idx >> 3) ^ idx) & 1);
  const int k0 = rev ? 9 : 0, kd = rev ? -1 : 1;
  u32x4 fg[10][3];
  if (wave == 2 || wave == 3) {
    const rsrc_t rg = mk_rsrc((const char*)&GT + (size_t)(2 * ct + wave - 2) * 30720, 30720);
#pragma unroll
    for (int i = 0; i < 30; ++i) fg[i / 3][i % 3] = __builtin_amdgcn_raw_buffer_load_b128(rg, lane * 16 + (i % 3) * 1024, (k0 + kd * (i / 3)) * 3072, 0);
  }
  await(ws, 2, e, 7, wave, lane);
  TS(11);
  if (wave < 2) {
    const rsrc_t ra = mk_rsrc(ws + WS_P + (size_t)(4 * rt) * 20480 + wave * 10240, 4 * 20480);
    const rsrc_t rb = mk_rsrc(ws + WS_M + (size_t)(5 * ct) * 20480 + wave * 10240, 5 * 20480);
    const unsigned vo = lane * 16;
    f32x4 acc[4][5];
#pragma unroll
    for (int mt = 0; mt < 4; ++mt)
#pragma unroll
      for (int nt = 0; nt < 5; ++nt) acc[mt][nt] = (f32x4){0.f, 0.f, 0.f, 0.f};
    u32x4 fa[10][4], fb[10][5];
    constexpr int PF = P3_PF;
    auto ld = [&](int ks) {
#pragma unroll
      for (int nt = 0; nt < 5; ++nt) fb[ks][nt] = LD16(rb, vo, nt * 20480 + (k0 + kd * ks) * 1024);
#pragma unroll
      for (int mt = 0; mt < 4; ++mt) fa[ks][mt] = LD16(ra, vo, mt * 20480 + (k0 + kd * ks) * 1024);
    };
#pragma unroll
    for (int ks = 0; ks < PF; ++ks) { ld(ks); SCHED(); }
#pragma unroll
    for (int ks = 0; ks < 10; ++ks) {
#pragma unroll
      for (int mt = 0; mt < 4; ++mt)
#pragma unroll
        for (int nt = 0; nt < 5; ++nt) acc[mt][nt] = MFMA16(BC(fb[ks][nt]), BC(fa[ks][mt]), acc[mt][nt]);
      SCHED();
      if (ks + PF < 10) ld(ks + PF);
      SCHED();
    }
    TS(12);
#pragma unroll
    for (int mt = 0; mt < 4; ++mt)
#pragma unroll
      for (int nt = 0; nt < 5; ++nt) *(LDS_AS f32x4*)(red + wave * RA + (16 * mt + r) * 84 + 16 * nt + 4 * g) = acc[mt][nt];
  } else if (wave < 4) {
    const int od = wave - 2;
    lptr yb = sm + od * 40960;
    const unsigned ybu = (unsigned)(size_t)yb;
    const rsrc_t ry = mk_rsrc(ws + WS_Y + (size_t)od * 320 * 4096 + rt * 128, 320 * 4096);
    const int row = lane >> 3;
    const unsigned vy0 = row * 4096 + ((((lane & 7) >> 1) ^ ((row >> 1) & 1)) * 32) + (lane & 1) * 16;
    const unsigned vy1 = vy0 ^ 64;
    auto issue = [&](int ks) {
#pragma unroll
      for (int i = 0; i < 4; ++i) dma16_sc1(ry, ybu + (ks * 4 + i) * 1024, (i & 1) ? vy1 : vy0, ((k0 + kd * ks) * 4 + i) * 32768);
    };
#pragma unroll
    for (int i = 0; i < 30; ++i) asm volatile("" : "+v"(fg[i / 3][i % 3]));
    issue(0); issue(1); issue(2);
    if (P3_YW == 5) { issue(3); issue(4); }
    const int q = r >> 2, p = lane & 3;
    const int hsw = ((q >> 1) & 1) | ((g & 1) << 1);
    unsigned yo[4];
#pragma unroll
    for (int mt = 0; mt < 4; ++mt) yo[mt] = g * 1024 + q * 128 + ((mt ^ hsw) * 32) + p * 8;
    f32x4 acc[4][3];
#pragma unroll
    for (int mt = 0; mt < 4; ++mt)
#pragma unroll
      for (int nt = 0; nt < 3; ++nt) acc[mt][nt] = (f32x4){0.f, 0.f, 0.f, 0.f};
#define K3_STEP(ks, n)                                                                                         \
    {                                                                                                          \
      WAIT_V(n);                                                                                               \
      SCHED();                                                                                                 \
      bf16x8 fy[4];                                                                                            \
      _Pragma("unroll") for (int mt = 0; mt < 4; ++mt) {                                                       \
        const s16x4 lo = TRREAD(yb + yo[mt] + ks * 4096), hi = TRREAD(yb + yo[mt] + ks * 4096 + 512);          \
        fy[mt] = __builtin_shufflevector(lo, hi, 0, 1, 2, 3, 4, 5, 6, 7);                                      \
      }                                                                                                        \
      SCHED();                                                                                                 \
      if (ks + P3_YW < 10) issue(ks + P3_YW);                                                                          \
      SCHED();                                                                                                 \
      _Pragma("unroll") for (int mt = 0; mt < 4; ++mt)                                                         \
        _Pragma("unroll") for (int nt = 0; nt < 3; ++nt) acc[mt][nt] = MFMA16(BC(fg[ks][nt]), fy[mt], acc[mt][nt]);   \
      SCHED();                                                                                                 \
    }
    K3_STEP(0, 8) K3_STEP(1, 8) K3_STEP(2, 8) K3_STEP(3, 8) K3_STEP(4, 8)
    K3_STEP(5, 8) K3_STEP(6, 8) K3_STEP(7, 8) K3_STEP(8, 4) K3_STEP(9, 0)
    TS(12);
#pragma unroll
    for (int mt = 0; mt < 4; ++mt)
#pragma unroll
      for (int nt = 0; nt < 3; ++nt) *(LDS_AS f32x4*)(red + 2 * RA + od * RE + (16 * mt + r) * 52 + 16 * nt + 4 * g) = acc[mt][nt];
  } else {
    if (wave == 4) red[RG + lane] = __uint_as_float(__builtin_amdgcn_raw_buffer_load_b32(mk_rsrc(ws + WS_G + rt * 256, 256), lane * 4, 0, LD_AUX));
  }
  const rsrc_t ro = mk_rsrc(A.out + (size_t)(64 * rt) * 640, 64 * 1280);
  unsigned oa[2], oe[2], oq[2], og[2], or_[2];
#pragma unroll
  for (int i = 0; i < 2; ++i) {
    const int it = i ? 512 + wave * 16 + (lane & 15) : tid;
    const int row = it / 10, gr = it - row * 10;
    oa[i] = (row * 84 + 8 * gr) * 4;
    oe[i] = (2 * RA + row * 52) * 4;
    oq[i] = oe[i] + (gr < 5 ? 8 * gr : 72 - 8 * gr) * 4;
    og[i] = (i && lane >= 16) ? 0x7fffffffu : row * 1280 + (gr < 5 ? 80 * ct : 1120 - 80 * ct) + 16 * gr;
    or_[i] = (RG + row) * 4;
    oa[i] |= (gr < 5) ? 0u : 0x80000000u;
    asm volatile("" : "+v"(oa[i]), "+v"(oe[i]), "+v"(oq[i]), "+v"(og[i]), "+v"(or_[i]));
  }
  WAIT_L0();
  BAR();
  TS(13);
#pragma unroll
  for (int i = 0; i < 2; ++i) {
    const bool lo = (int)oa[i] >= 0;
    lptr rb = sm + 81920, pa = rb + (oa[i] & 0x7fffffffu), pq = rb + oq[i];
    const float gq = *(LDS_AS float*)(rb + or_[i]);
    const f32x4 a0 = *(LDS_AS f32x4*)pa + *(LDS_AS f32x4*)(pa + RA * 4);
    const f32x4 a1 = *(LDS_AS f32x4*)(pa + 16) + *(LDS_AS f32x4*)(pa + RA * 4 + 16);
    const float yn = *(LDS_AS float*)(rb + oe[i] + (RE + 41) * 4);
    const f32x4 e0 = *(LDS_AS f32x4*)pq, e1 = *(LDS_AS f32x4*)(pq + 16), o0 = *(LDS_AS f32x4*)(pq + RE * 4), o1 = *(LDS_AS f32x4*)(pq + RE * 4 + 16);
    const float p8 = *(LDS_AS float*)(pq + 32) + *(LDS_AS float*)(pq + RE * 4 + 32);
    const f32x4 m0 = e0 - o0, m1 = e1 - o1, p0 = e0 + o0, p1 = e1 + o1;
    const f32x4 yy = {yn, -yn, yn, -yn};
    const f32x4 b0 = (lo ? m0 : (f32x4){p8, p1[3], p1[2], p1[1]}) + yy;
    const f32x4 b1 = (lo ? m1 : (f32x4){p1[0], p0[3], p0[2], p0[1]}) + yy;
    const f32x4 r0 = b0 + gq * (a0 - b0), r1 = b1 + gq * (a1 - b1);
    __builtin_amdgcn_raw_buffer_store_b128(__builtin_bit_cast(u32x4, pack8(r0, r1)), ro, og[i], 0, OUT_AUX);
  }
  TS(14);
}

#define TS_END()

__global__ __launch_bounds__(512, 2) void fused_kernel(Args A) {
  extern __shared__ __attribute__((aligned(16))) char smem[];
  lptr sm = (lptr)smem;
  TS_DECL;
  TS(0);
  {
    unsigned long long pc; unsigned len;
    asm volatile("s_getpc_b64 %0\n.Lfk_start:\n\ts_mov_b32 %1, .Lfk_end-.Lfk_start" : "=s"(pc), "=s"(len));
    pf_asm(mk_rsrc((const void*)pc, len), (unsigned)(size_t)sm + K1_JUNK + 1792, (threadIdx.x * 32 + (blockIdx.x >> 3)) * 128, 0);
  }
  const rsrc_t re = mk_rsrc(A.ws + WS_EPOCH, 4);
  const unsigned e = *(const __attribute__((address_space(4))) unsigned*)(A.ws + WS_EPOCH);
  f32x4 pq[2][5];
  phase1(A, sm, pq, e TS_PASS);
  phase2<true>(A, sm, e, pq TS_PASS);
  publish(A.ws, 2, e);
  TS(10);
  phase3<true>(A, sm, e TS_PASS);
  if (blockIdx.x == 0 && threadIdx.x == 0) __builtin_amdgcn_raw_buffer_store_b32(e + 1u, re, 0, 0, 16);
  TS_END();
  asm volatile(".Lfk_end:");
}

extern "C" void kernel_launch(void* const* d_in, const int* in_sizes, int n_in, void* d_out, int out_size, void* d_ws,
                              size_t ws_size, hipStream_t stream) {
  Args A;
  A.x = (const u16*)d_in[0]; A.wqkv = (const u16*)d_in[1]; A.wo = (const u16*)d_in[2];
  A.wr = (const u16*)d_in[4]; A.wi = (const u16*)d_in[5]; A.gw = (const u16*)d_in[6];
  A.ws = (char*)d_ws; A.out = (u16*)d_out;
  (void)hipFuncSetAttribute((const void*)fused_kernel, hipFuncAttributeMaxDynamicSharedMemorySize, LDS_ALL);
  fused_kernel<<<256, 512, LDS_ALL, stream>>>(A);
}
```

```cpp
#include <hip/hip_runtime.h>
#include <hip/hip_bf16.h>
#include <utility>

typedef unsigned short u16;
typedef __attribute__((ext_vector_type(8))) short bf16x8;
typedef __attribute__((ext_vector_type(4))) short s16x4;
typedef __attribute__((ext_vector_type(4))) float f32x4;
typedef __attribute__((ext_vector_type(2))) float f32x2;
typedef __attribute__((ext_vector_type(2))) __bf16 bf16x2_t;
typedef __attribute__((ext_vector_type(4))) unsigned u32x4;
typedef __attribute__((ext_vector_type(2))) unsigned u32x2;
#define LDS_AS __attribute__((address_space(3)))
#define DI __device__ __forceinline__
#define MFMA16(a, b, c) __builtin_amdgcn_mfma_f32_16x16x32_bf16((a), (b), (c), 0, 0, 0)
typedef LDS_AS char* lptr;
typedef __amdgpu_buffer_rsrc_t rsrc_t;

#define ST_AUX 16
#define OUT_AUX 0
#define K1_ROT 5
#define POLL_SLEEP 2
#define CODE_PF 1
#define P3_REV 0
#define P3_PF 3
#define P3_YW 3
#define P3_MAP 1
#define LD_AUX 16

constexpr double kPi = 3.14159265358979323846;
constexpr double ccos_s(double x) { double x2 = x * x, t = 1, s = 1; for (int i = 1; i < 12; ++i) { t *= -x2 / ((2 * i - 1) * (2 * i)); s += t; } return s; }
constexpr double csin_s(double x) { double x2 = x * x, t = x, s = x; for (int i = 1; i < 12; ++i) { t *= -x2 / ((2 * i) * (2 * i + 1)); s += t; } return s; }
constexpr double cos640(int m) {
  m %= 640; if (m > 320) m = 640 - m;
  bool neg = false; if (m > 160) { m = 320 - m; neg = true; }
  double v = (m <= 80) ? ccos_s(2 * kPi * m / 640) : csin_s(2 * kPi * (160 - m) / 640);
  return neg ? -v : v;
}
constexpr u16 to_bf16c(double v) { float f = (float)v; unsigned u = __builtin_bit_cast(unsigned, f); u += 0x7fffu + ((u >> 16) & 1u); return (u16)(u >> 16); }
struct C640 { u16 v[640]; };
constexpr C640 make_c640() { C640 c{}; for (int i = 0; i < 640; ++i) c.v[i] = to_bf16c(cos640(i)); return c; }
constexpr C640 COS640 = make_c640();

struct alignas(128) FRow { u16 v[10 * 512]; };
constexpr FRow make_frow(int id) {
  FRow t{};
  const int cq = id / 10, st = id % 10;
  for (int par = 0; par < 2; ++par) for (int nt = 0; nt < 5; ++nt) for (int l = 0; l < 64; ++l) {
    const int kk = 80 * cq + 16 * nt + (l & 15);
    for (int j = 0; j < 8; ++j) {
      const int d = 64 * st + 32 * (j >> 2) + 8 * (l >> 4) + 2 * (j & 3) + par;
      const int m = (kk == 1) ? (d * 160 + 160 * par) % 640 : (d * (kk >> 1) + 160 * (kk & 1)) % 640;
      t.v[((par * 5 + nt) * 64 + l) * 8 + j] = COS640.v[m];
    }
  }
  return t;
}
template <int I> constexpr FRow FROW = make_frow(I);
struct alignas(128) FTab { FRow r[40]; };
template <int... I> constexpr FTab make_ftab(std::integer_sequence<int, I...>) { return FTab{{FROW<I>...}}; }
__device__ const FTab FT = make_ftab(std::make_integer_sequence<int, 40>{});

struct alignas(128) GRow { u16 v[10 * 3 * 512]; };
constexpr GRow make_grow(int id) {
  GRow t{};
  const int ct = id >> 1, od = id & 1;
  for (int ks = 0; ks < 10; ++ks) for (int nt = 0; nt < 3; ++nt) for (int l = 0; l < 64; ++l) {
    const int ci = 16 * nt + (l & 15), d = 40 * ct + ci;
    for (int j = 0; j < 8; ++j) {
      const int f = 32 * ks + 8 * (l >> 4) + j;
      t.v[((ks * 3 + nt) * 64 + l) * 8 + j] = (ci <= 40) ? COS640.v[(f * d + (od ? 160 : 0)) % 640] : (u16)((od && ci == 41 && f == 0) ? 0x3f80 : 0);
    }
  }
  return t;
}
template <int I> constexpr GRow GROW = make_grow(I);
struct alignas(128) GTab { GRow r[16]; };
template <int... I> constexpr GTab make_gtab(std::integer_sequence<int, I...>) { return GTab{{GROW<I>...}}; }
__device__ const GTab GT = make_gtab(std::make_integer_sequence<int, 16>{});

constexpr size_t WS_P   = 0;
constexpr size_t WS_Y   = WS_P + 2621440;
constexpr size_t WS_X1  = WS_Y + 2621440;
constexpr size_t WS_WT  = WS_X1 + 321 * 8192;
constexpr size_t WS_KVP = WS_WT + 352 * 8192;
constexpr size_t WS_M   = WS_KVP + 983040;
constexpr size_t WS_G   = WS_M + 819200;
constexpr size_t WS_ZP  = WS_G + 8192;
constexpr size_t WS_SYN = WS_ZP + 40960;

DI rsrc_t mk_rsrc(const void* p, unsigned bytes) { return __builtin_amdgcn_make_buffer_rsrc((void*)p, 0, bytes, 0x00020000); }
DI unsigned pk2(float a, float b) { f32x2 v = {a, b}; return __builtin_bit_cast(unsigned, __builtin_convertvector(v, bf16x2_t)); }
DI float bf_lo(unsigned u) { return __uint_as_float(u << 16); }
DI float bf_hi(unsigned u) { return __uint_as_float(u & 0xffff0000u); }
DI float dot2(unsigned a, unsigned b, float c) { return __builtin_amdgcn_fdot2_f32_bf16(__builtin_bit_cast(bf16x2_t, a), __builtin_bit_cast(bf16x2_t, b), c, false); }
constexpr float kEps = 1e-6f, kL2E = 1.4426950408889634f;
DI float phi_f(float a) {
  float e;
  const float u = __builtin_fmaf(a, kL2E, -kEps * kL2E);
  asm("v_exp_f32_e64 %0, %1 clamp" : "=v"(e) : "v"(u));
  return __builtin_amdgcn_fmed3f(a, kEps, __builtin_inff()) + e;
}
DI bf16x8 pack8(const f32x4& a, const f32x4& b) {
  u32x4 p = {pk2(a[0], a[1]), pk2(a[2], a[3]), pk2(b[0], b[1]), pk2(b[2], b[3])};
  return __builtin_bit_cast(bf16x8, p);
}
DI u32x4 widen(u32x2 a, u32x2 b) {
  auto x = __builtin_amdgcn_permlane16_swap(a[0], b[0], false, false);
  auto y = __builtin_amdgcn_permlane16_swap(a[1], b[1], false, false);
  return (u32x4){x[0], y[0], x[1], y[1]};
}
#define WAIT_V_(n) asm volatile("s_waitcnt vmcnt(" #n ")" ::: "memory")
#define WAIT_V(n) WAIT_V_(n)
#define BC(v) __builtin_bit_cast(bf16x8, (v))
#define WAIT_L0() __builtin_amdgcn_s_waitcnt(0xC07F)
#define BAR() __builtin_amdgcn_s_barrier()
#define SCHED() __builtin_amdgcn_sched_barrier(0)
#define TRREAD(p) __builtin_amdgcn_ds_read_tr16_b64_v4i16((LDS_AS s16x4*)(p))
#define ST16(v, rs, vo, so) __builtin_amdgcn_raw_buffer_store_b128((v), (rs), (vo), (so), ST_AUX)

#define TS_DECL
#define TS(i)
#define TC(i)
#define TS_FLUSH(kern)
#define TS_FLUSH2()
#define TS_ARG
#define TS_PASS

constexpr int K1_STAGE = 40960;
constexpr int K1_WF = 3 * K1_STAGE;
constexpr int K1_JUNK = K1_WF + 33792;
constexpr int K1_LDS = K1_JUNK + 2048;

struct Frags { bf16x8 a[2][2]; bf16x8 b[2][5]; };
struct K1Ctx { rsrc_t ra, rb; unsigned va[2], vb[3], ao[3][2], bo[3][5], ldsa, ldsb; int st, rs, wave; };

template <int I0, int I1> DI void k1_read(Frags& F, lptr sm, const unsigned (&ao)[2], const unsigned (&bo)[5]) {
#pragma unroll
  for (int i = I0; i < I1; ++i) {
    const int k = i / 7, j = i % 7;
    if (j < 2) F.a[k][j] = *(LDS_AS bf16x8*)(sm + ao[k] + j * 2048);
    else {
      const s16x4 lo = TRREAD(sm + bo[j - 2] + k * 12288), hi = TRREAD(sm + bo[j - 2] + k * 12288 + 512);
      F.b[k][j - 2] = __builtin_shufflevector(lo, hi, 0, 1, 2, 3, 4, 5, 6, 7);
    }
  }
}
DI void dma16_asm(rsrc_t rs, unsigned lds, unsigned vo, int so) {
  asm volatile("s_mov_b32 m0, %2\n\ts_nop 0\n\tbuffer_load_dwordx4 %0, %1, %3 offen lds" :: "v"(vo), "s"(rs), "s"(lds), "s"(so));
}
DI void pf_asm(rsrc_t rs, unsigned lds, unsigned vo, int so) {
  asm volatile("s_mov_b32 m0, %2\n\ts_nop 0\n\tbuffer_load_dword %0, %1, %3 offen lds" :: "v"(vo), "s"(rs), "s"(lds), "s"(so));
}
template <int BUF, int I> DI void k1_issue1(const K1Ctx& c) {
  if (I < 2) dma16_asm(c.ra, c.ldsa + BUF * K1_STAGE + I * 1024, c.va[I < 2 ? I : 0], c.st * 128);
  else dma16_asm(c.rb, c.ldsb + BUF * K1_STAGE + (I - 2) * 1024, c.vb[I < 2 ? 0 : I - 2], c.st * 64 * c.rs);
}
template <int BUF> DI void k1_issue(K1Ctx& c) {
  k1_issue1<BUF, 0>(c); k1_issue1<BUF, 1>(c); k1_issue1<BUF, 2>(c); k1_issue1<BUF, 3>(c); k1_issue1<BUF, 4>(c);
  c.st = (c.st == 9) ? 0 : c.st + 1;
}
template <bool SWAP, int I0, int I1> DI void k1_mma(f32x4 (&acc)[2][5], const Frags& F) {
#pragma unroll
  for (int i = I0; i < I1; ++i) {
    const int k = i / 10, mt = (i / 5) & 1, nt = i % 5;
    acc[mt][nt] = SWAP ? MFMA16(F.b[k][nt], F.a[k][mt], acc[mt][nt]) : MFMA16(F.a[k][mt], F.b[k][nt], acc[mt][nt]);
  }
}
template <bool SWAP, int N, int SEG> DI void k1_seg(f32x4 (&acc)[2][5], Frags& Fn, const Frags& Fp, lptr sm, K1Ctx& c) {
  constexpr int R0[6] = {0, 4, 7, 10, 12, 14};
  k1_read<R0[SEG], R0[SEG + 1]>(Fn, sm, c.ao[N % 3], c.bo[N % 3]);
  if (N > 0) {
    k1_mma<SWAP, 4 * SEG, 4 * SEG + 4>(acc, Fp);
    if (SEG < 2) {
      __builtin_amdgcn_sched_group_barrier(0x008, 1, 0); __builtin_amdgcn_sched_group_barrier(0x100, 2, 0);
      __builtin_amdgcn_sched_group_barrier(0x008, 1, 0); __builtin_amdgcn_sched_group_barrier(0x100, 2, 0);
      __builtin_amdgcn_sched_group_barrier(0x008, 1, 0); __builtin_amdgcn_sched_group_barrier(0x100, 2, 0);
      __builtin_amdgcn_sched_group_barrier(0x008, 1, 0);
    } else {
#pragma unroll
      for (int i = 0; i < 4; ++i) { __builtin_amdgcn_sched_group_barrier(0x008, 1, 0); __builtin_amdgcn_sched_group_barrier(0x100, 2, 0); }
    }
  }
  SCHED();
  if (N + 2 <= 9) k1_issue1<(N + 2) % 3, SEG>(c);
  SCHED();
}
template <bool SWAP, int N> DI void k1_step(f32x4 (&acc)[2][5], Frags& Fn, const Frags& Fp, lptr sm, K1Ctx& c) {
  WAIT_L0();
  BAR();
  SCHED();
  k1_seg<SWAP, N, 0>(acc, Fn, Fp, sm, c);
  k1_seg<SWAP, N, 1>(acc, Fn, Fp, sm, c);
  k1_seg<SWAP, N, 2>(acc, Fn, Fp, sm, c);
  k1_seg<SWAP, N, 3>(acc, Fn, Fp, sm, c);
  k1_seg<SWAP, N, 4>(acc, Fn, Fp, sm, c);
  c.st = (c.st == 9) ? 0 : c.st + 1;
}

struct DFrags { u32x4 a[2][2]; bf16x8 b[2][3]; };
constexpr int KD_STAGE = 26624;
template <int BUF, int I> DI void kd_issue1(const K1Ctx& c) {
  if (I < 2) dma16_asm(c.ra, c.ldsa + BUF * KD_STAGE + I * 1024, c.va[I & 1], c.st * 128);
  else if (I == 2) dma16_asm(c.rb, c.ldsb + BUF * KD_STAGE, c.vb[0], c.st * 10240);
  else dma16_asm(c.rb, c.wave < 2 ? c.ldsb + BUF * KD_STAGE + 8192 : c.ldsa - 2048 * c.wave + K1_JUNK, c.vb[1], c.st * 10240);
}
template <int BUF> DI void kd_issue(K1Ctx& c) {
  kd_issue1<BUF, 0>(c); kd_issue1<BUF, 2>(c); kd_issue1<BUF, 1>(c); kd_issue1<BUF, 3>(c);
  c.st = (c.st == 9) ? 0 : c.st + 1;
}
template <int NT> DI void kd_mma(f32x4 (&acc)[2][2][3], const DFrags& F) {
#pragma unroll
  for (int mt = 0; mt < 2; ++mt) {
    u32x4 ev, od;
#pragma unroll
    for (int k = 0; k < 2; ++k)
#pragma unroll
      for (int i = 0; i < 2; ++i) {
        ev[2 * k + i] = __builtin_amdgcn_perm(F.a[mt][k][2 * i + 1], F.a[mt][k][2 * i], 0x05040100u);
        od[2 * k + i] = __builtin_amdgcn_perm(F.a[mt][k][2 * i + 1], F.a[mt][k][2 * i], 0x07060302u);
      }
#pragma unroll
    for (int nt = 0; nt < NT; ++nt) {
      acc[0][mt][nt] = MFMA16(F.b[0][nt], BC(ev), acc[0][mt][nt]);
      acc[1][mt][nt] = MFMA16(F.b[1][nt], BC(od), acc[1][mt][nt]);
    }
  }
}
template <int NT, int N> DI void kd_step(f32x4 (&acc)[2][2][3], DFrags& Fn, const DFrags& Fp, lptr sm, K1Ctx& c) {
  WAIT_L0();
  BAR();
  SCHED();
  if (N + 3 <= 9) kd_issue<(N + 3) % 4>(c);
  SCHED();
  constexpr int H = (N % 4) >> 1, O = (N & 1) * KD_STAGE;
#pragma unroll
  for (int mt = 0; mt < 2; ++mt)
#pragma unroll
    for (int k = 0; k < 2; ++k) Fn.a[mt][k] = *(LDS_AS u32x4*)(sm + c.ao[H][k] + O + mt * 2048);
#pragma unroll
  for (int par = 0; par < 2; ++par)
#pragma unroll
    for (int nt = 0; nt < NT; ++nt) Fn.b[par][nt] = *(LDS_AS bf16x8*)(sm + c.bo[H][0] + O + (par * 5 + nt) * 1024);
  if (N > 0) {
    kd_mma<NT>(acc, Fp);
    __builtin_amdgcn_sched_group_barrier(0x100, 2, 0);
    __builtin_amdgcn_sched_group_barrier(0x002, 8, 0);
#pragma unroll
    for (int i = 0; i < 2 * NT; ++i) { __builtin_amdgcn_sched_group_barrier(0x008, 1, 0); __builtin_amdgcn_sched_group_barrier(0x100, 1, 0); __builtin_amdgcn_sched_group_barrier(0x002, 2, 0); }
  }
  SCHED();
}

DI u32x2 make_tag(unsigned e) { return (u32x2){e + 1u, (e + 1u) * 0x9E3779B9u ^ 0x5bd1e995u}; }
DI int p1_rt(int b) { return (b & 1) * 8 + ((b >> 3) & 7); }
DI int p1_ct(int b) { const int x2 = (b & 7) >> 1, j = b >> 6; return j == 0 ? x2 : j == 3 ? 12 + x2 : 3 + 2 * x2 + j; }
DI int flag_slot(int b) { return p1_ct(b) * 16 + p1_rt(b); }
constexpr size_t WS_EPOCH = WS_SYN + 3 * 65536;
DI void flag_store(char* ws, int which, unsigned e, int lane) {
  __builtin_amdgcn_raw_buffer_store_b64(make_tag(e), mk_rsrc(ws + WS_SYN + which * 65536, 65536), lane * 2048 + flag_slot(blockIdx.x) * 8, 0, 16);
}
template <int NT, class FL, class FS> DI void kd_main(char* ws, lptr sm, K1Ctx& c, int rt, int cq, unsigned e, int lane, FL filt_load, FS filt_store TS_ARG) {
  const int wm = c.wave & 3, r = lane & 15, g = lane >> 4;
  constexpr int T0 = NT == 3 ? 0 : 3;
  DFrags D0, D1;
  f32x4 acc[2][2][3];
#pragma unroll
  for (int u = 0; u < 2; ++u) {
    c.ao[u][0] = 2 * u * KD_STAGE + (32 * wm + r) * 128 + ((g ^ (r >> 1)) * 16);
    c.ao[u][1] = 2 * u * KD_STAGE + (32 * wm + r) * 128 + (((4 + g) ^ (r >> 1)) * 16);
    c.bo[u][0] = 2 * u * KD_STAGE + 16384 + T0 * 1024 + lane * 16;
    asm volatile("" : "+v"(c.ao[u][0]), "+v"(c.ao[u][1]), "+v"(c.bo[u][0]));
  }
#pragma unroll
  for (int i = 0; i < 4 * NT; ++i) { acc[i / (2 * NT)][(i / NT) & 1][i % NT] = (f32x4){0.f, 0.f, 0.f, 0.f}; asm volatile("" : "+v"(acc[i / (2 * NT)][(i / NT) & 1][i % NT])); }
  TS(1);
  WAIT_V(8); kd_step<NT, 0>(acc, D0, D1, sm, c);
  TS(2);
  WAIT_V(8); kd_step<NT, 1>(acc, D1, D0, sm, c);
  filt_load();
  WAIT_V(12); kd_step<NT, 2>(acc, D0, D1, sm, c);
  WAIT_V(12); kd_step<NT, 3>(acc, D1, D0, sm, c);
  WAIT_V(12); kd_step<NT, 4>(acc, D0, D1, sm, c);
  WAIT_V(8); kd_step<NT, 5>(acc, D1, D0, sm, c);
  WAIT_V(8); kd_step<NT, 6>(acc, D0, D1, sm, c);
  filt_store();
  WAIT_V(11); kd_step<NT, 7>(acc, D1, D0, sm, c);
  WAIT_V(7); kd_step<NT, 8>(acc, D0, D1, sm, c);
  WAIT_V(3); kd_step<NT, 9>(acc, D1, D0, sm, c);
  kd_mma<NT>(acc, D1);
  TS(3);
  const rsrc_t rx = mk_rsrc(ws + WS_X1 + (wm >> 1) * 4096 + (rt >> 2) * 1024 + (rt & 3) * 256 + (wm & 1) * 128, 321 * 8192);
  const bool up = r >= 8;
  const int fb = 40 * cq + 8 * T0 + 2 * g;
  const bool sp = (fb == 0);
#pragma unroll
  for (int nt = 0; nt < NT; ++nt) {
    unsigned it[4][2];
#pragma unroll
    for (int mt = 0; mt < 2; ++mt) {
      const f32x4 E = acc[0][mt][nt], O = acc[1][mt][nt], P = E + O, M = E - O;
      it[0][mt] = pk2(P[0], P[1]); it[1][mt] = pk2(P[2], P[3]); it[2][mt] = pk2(M[0], -M[1]); it[3][mt] = pk2(M[2], -M[3]);
      if (NT == 3 && nt == 0) {
        it[0][mt] = sp ? (it[0][mt] & 0xffffu) : it[0][mt]; it[2][mt] = sp ? (it[2][mt] & 0xffffu) : it[2][mt];
        __builtin_amdgcn_raw_buffer_store_b32(pk2(E[1], O[1]), rx, sp ? 160 * 8192 + (r & 7) * 16 + (r >> 3) * 8 + mt * 4 : 0x7fffffff, 0, ST_AUX);
      }
    }
#pragma unroll
    for (int h = 0; h < 2; ++h) {
      const unsigned t0 = __builtin_amdgcn_update_dpp(0u, up ? it[2 * h][0] : it[2 * h + 1][0], 0x128, 0xf, 0xf, false);
      const unsigned t1 = __builtin_amdgcn_update_dpp(0u, up ? it[2 * h][1] : it[2 * h + 1][1], 0x128, 0xf, 0xf, false);
      const u32x4 o = {up ? t0 : it[2 * h][0], up ? t1 : it[2 * h][1], up ? it[2 * h + 1][0] : t0, up ? it[2 * h + 1][1] : t1};
      const int f0 = fb + 8 * nt + (r >> 3);
      ST16(o, rx, (h ? 320 - f0 : f0) * 8192 + (r & 7) * 16, 0);
    }
  }
}

struct Args { const u16 *x, *wqkv, *wo, *wr, *wi, *gw; char* ws; u16* out; };

DI void phase1(const Args& A, lptr sm, f32x4 (&pq)[2][5], unsigned e TS_ARG) {
  const u16* x = A.x; const u16* wqkv = A.wqkv; const u16* wr = A.wr; const u16* wi = A.wi; char* ws = A.ws;
  const int tid = threadIdx.x, lane = tid & 63;
  const int wave = __builtin_amdgcn_readfirstlane(tid >> 6);
  const int b = blockIdx.x;
  const int xcd = b & 7, idx = b >> 3;
  const int rt = p1_rt(b), ct = p1_ct(b);
  (void)xcd;
  const bool dft = ct >= 12, iskv = ct >= 4 && ct < 12;
  const int c0 = iskv ? 80 * (ct - 4) : 160 * (ct & 3);
  const int L0 = c0 >> 6, p0 = (c0 & 63) >> 4;
  const int xs = (p0 == 0 || p0 == 3) ? 1 : 2;
  const int bigB = p0 >= 2 ? 1 : 0;

  K1Ctx c;
  c.wave = wave;
  c.ra = mk_rsrc(x, 2048 * 1280);
  c.rb = dft ? mk_rsrc((const char*)&FT + (ct & 3) * 102400, 102400) : mk_rsrc(wqkv, 640 * 3840);
  c.rs = 3840;
  auto grow = [&](int row) { return dft ? 32 * (4 * rt + 2 * ((row >> 3) & 1) + ((row >> 4) & 1)) + 8 * (row >> 5) + (row & 7) : 128 * rt + row; };
#pragma unroll
  for (int i = 0; i < 2; ++i) {
    const int row = 16 * wave + 8 * i + (lane >> 3);
    c.va[i] = grow(row) * 1280 + ((lane & 7) ^ ((row >> 1) & 7)) * 16;
  }
  c.ldsa = (unsigned)(size_t)sm + 2 * wave * 1024;
  c.st = (((((idx & 7) * 5 + (idx >> 3) * 10) >> 2) % 10) * K1_ROT / 10) * (10 / K1_ROT);
  k1_issue1<0, 0>(c); k1_issue1<0, 1>(c);
  SCHED();
  {
    const int row = lane >> 3, hf = lane & 1;
    const int pair = ((lane & 7) >> 1) ^ (((row >> 1) & 1) | ((wave & 1) << 1));
    const unsigned rowb = (8 * wave + row) * c.rs;
    const bool kown = bigB ? (pair >= p0) : (pair <= p0);
    c.vb[0] = rowb + (iskv ? 1280 + (L0 + bigB) * 128 : L0 * 128) + pair * 32 + hf * 16;
    c.vb[1] = rowb + (iskv ? 2560 + (L0 + bigB) * 128 + (hf ^ 1) * 16 : (L0 + 1) * 128 + hf * 16) + pair * 32;
    c.vb[2] = rowb + (iskv ? (L0 + 1 - bigB) * 128 + (kown ? 1280 + pair * 32 + hf * 16 : 2560 + (pair ^ xs) * 32 + (hf ^ 1) * 16)
                           : (L0 + 2) * 128 + pair * 32 + hf * 16);
  }
  c.ldsb = (unsigned)(size_t)sm + 16384 + (dft ? 1 : 3) * wave * 1024;
  if (dft) { c.vb[0] = wave * 1024 + lane * 16; c.vb[1] = wave < 2 ? c.vb[0] + 8192 : 0x7fffffffu; }
  if (dft) { kd_issue1<0, 2>(c); kd_issue1<0, 3>(c); }
  else { k1_issue1<0, 2>(c); k1_issue1<0, 3>(c); k1_issue1<0, 4>(c); }
  c.st = (c.st == 9) ? 0 : c.st + 1;
  BAR();
  if (dft) { kd_issue<1>(c); kd_issue<2>(c); }
  else k1_issue<1>(c);

  const int wm = wave >> 1, wn = wave & 1;
  const int r = lane & 15, g = lane >> 4, q = (lane & 15) >> 2, p = lane & 3;
  const int hsw = ((q >> 1) & 1) | ((g & 1) << 1);
  const unsigned bbase = 16384 + g * 3072 + q * 128;
  c.ao[0][0] = (32 * wm + r) * 128 + ((g ^ (r >> 1)) * 16);
  c.ao[0][1] = (32 * wm + r) * 128 + (((4 + g) ^ (r >> 1)) * 16);
  f32x4 acc[2][5];
  Frags F0, F1;

#define K1_HEAD(SWAP, HOOK, W2)                                                                          \
  _Pragma("unroll") for (int u = 1; u < 3; ++u) {                                              \
    _Pragma("unroll") for (int k = 0; k < 2; ++k) { c.ao[u][k] = c.ao[0][k] + u * K1_STAGE; asm volatile("" : "+v"(c.ao[u][k])); }     \
    _Pragma("unroll") for (int k = 0; k < 5; ++k) { c.bo[u][k] = c.bo[0][k] + u * K1_STAGE; asm volatile("" : "+v"(c.bo[u][k])); }     \
  }                                                                                            \
  _Pragma("unroll") for (int mt = 0; mt < 2; ++mt)                                             \
    _Pragma("unroll") for (int nt = 0; nt < 5; ++nt) asm volatile("" : "+v"(acc[mt][nt]));     \
  TS(1);                                                                                       \
  WAIT_V(5); k1_step<SWAP, 0>(acc, F0, F1, sm, c);                                           \
  TS(2);                                                                                       \
  WAIT_V(5); k1_step<SWAP, 1>(acc, F1, F0, sm, c); HOOK;                                             \
  WAIT_V(W2); k1_step<SWAP, 2>(acc, F0, F1, sm, c);

  if (iskv) {
#pragma unroll
    for (int nt = 0; nt < 5; ++nt) {
      const int u = 4 * nt + p;
      const int isv = u >= 10 ? 1 : 0;
      const int pc = 4 * p0 + 10 * wn + u - 10 * isv;
      const int gp = pc >> 2, big = ((gp >> 2) == bigB);
      const int slot = (gp & 3) ^ ((!big && isv) ? xs : 0);
      c.bo[0][nt] = bbase + (big ? isv : 2) * 1024 + ((slot ^ hsw) * 32) + (((pc & 3) ^ (isv << 1)) * 8);
      const float i0 = ((nt < 2) || (nt == 2 && r < 8)) ? kEps : 0.f;
#pragma unroll
      for (int mt = 0; mt < 2; ++mt) acc[mt][nt] = (f32x4){i0, i0, i0, i0};
    }
    unsigned ko[8];
    constexpr int TI[8] = {0, 0, 1, 1, 1, 2, 2, 2}, TJ[8] = {2, 3, 2, 3, 4, 3, 4, 2};
#pragma unroll
    for (int i = 0; i < 8; ++i) {
      const int ti = TI[i], tj = TJ[i];
      const int ur = 16 * ti + r, hk = ur >= 20 ? 1 : 0;
      const int uc = 16 * tj + 4 * g - 40, hv = uc >= 20 ? 1 : 0;
      const bool isz = tj == 2 && g == 0;
      const bool valid = ur < 40 && (isz || (uc >= 0 && hk == hv));
      const int off = wave * 3840 + hk * 1920 + (ur - 20 * hk) * 96 + (isz ? 80 : (uc - 20 * hv) * 4);
      ko[i] = K1_STAGE + (valid ? off : 8 * 3840 + lane * 16);
      asm volatile("" : "+v"(ko[i]));
    }
    K1_HEAD(false, , 5)
    WAIT_V(5); k1_step<false, 3>(acc, F1, F0, sm, c);
    WAIT_V(5); k1_step<false, 4>(acc, F0, F1, sm, c);
    WAIT_V(5); k1_step<false, 5>(acc, F1, F0, sm, c);
    WAIT_V(5); k1_step<false, 6>(acc, F0, F1, sm, c);
    WAIT_V(5); k1_step<false, 7>(acc, F1, F0, sm, c);
    WAIT_V(5); k1_step<false, 8>(acc, F0, F1, sm, c);
    WAIT_V(0); k1_step<false, 9>(acc, F1, F0, sm, c);
    k1_mma<false, 0, 20>(acc, F1);
    TS(3);
    bf16x8 fr[5];
#pragma unroll
    for (int nt = 0; nt < 5; ++nt) {
#pragma unroll
      for (int m = 0; m < 2; ++m)
#pragma unroll
        for (int j = 0; j < 4; ++j) {
          if (nt < 2) acc[m][nt][j] = phi_f(acc[m][nt][j]);
          if (nt == 2) { const float ph = phi_f(acc[m][nt][j]); acc[m][nt][j] = (r < 8) ? ph : acc[m][nt][j]; }
        }
      fr[nt] = pack8(acc[0][nt], acc[1][nt]);
    }
    const bf16x8 ones = {0x3f80, 0x3f80, 0x3f80, 0x3f80, 0x3f80, 0x3f80, 0x3f80, 0x3f80};
    {
      const f32x4 zz = {0.f, 0.f, 0.f, 0.f};
      const bf16x8 v2 = (r < 8) ? ones : fr[2];
      f32x4 d[8];
#pragma unroll
      for (int i = 0; i < 8; ++i) d[i] = MFMA16(TJ[i] == 2 ? v2 : fr[TJ[i]], fr[TI[i]], zz);
#pragma unroll
      for (int i = 0; i < 8; ++i) *(LDS_AS f32x4*)(sm + ko[i]) = d[i];
    }
    WAIT_L0();
    BAR();
    const rsrc_t rk = mk_rsrc(ws + WS_KVP + ((size_t)rt * 32 + 4 * (ct - 4)) * 1920, 7680);
    LDS_AS f32x4* src = (LDS_AS f32x4*)(sm + K1_STAGE);
    const f32x4 s0 = (src[tid] + src[tid + 480]) + (src[tid + 960] + src[tid + 1440]);
    ST16(__builtin_bit_cast(u32x4, s0), rk, tid * 16, 0);
    if (wave >= 6) {
      LDS_AS float* zs = (LDS_AS float*)(sm + K1_STAGE) + min(tid - 384, 79) * 24 + 20;
      const float z = (zs[0] + zs[1920]) + (zs[3840] + zs[5760]);
      __builtin_amdgcn_raw_buffer_store_b32(__float_as_uint(z), mk_rsrc(ws + WS_ZP + rt * 2560 + (ct - 4) * 320, 320), (tid - 384) * 4, 0, ST_AUX);
    }
  } else {
    const int un = (dft ? 64 : 0) + rt * 4 + (ct & 3), mo = un >> 1, fh = un & 1;
    auto filt_load = [&]() {
      const rsrc_t rf = mk_rsrc((wave >> 2) ? wi : wr, 2048 * 642);
      const int kb = 16 * (mo >> 4) + 8 * (mo & 1) + 64 * (16 * ((mo >> 3) & 1) + 4 * ((mo >> 1) & 3) + (wave & 3));
      const unsigned v0 = lane < 21 ? lane * 16 : (lane >= 25 && lane < 46) ? 640 + (lane - 25) * 16 : 0x7fffffffu;
#pragma unroll
      for (int i = 0; i < 4; ++i)
        dma16_asm(rf, (unsigned)(size_t)sm + K1_WF + (wave * 4 + i) * 1056, v0, kb * 642 + 320 * fh + i * 1280);
    };
    auto filt_store = [&]() {
      const rsrc_t rw = mk_rsrc(ws + WS_WT + (size_t)fh * 160 * 8192 + mo * 128, (160 + fh) * 8192 - mo * 128);
      const int i8 = lane & 7;
#pragma unroll
      for (int rd = 0; rd < 3; ++rd) {
        const int fl = (rd * 8 + wave) * 8 + (lane >> 3);
        lptr src = sm + K1_WF + (i8 >> 1) * 1056 + (i8 & 1) * 400 + i8 * 2 + min(fl, 160) * 2;
        u32x4 o;
#pragma unroll
        for (int j = 0; j < 4; ++j) o[j] = (unsigned)*(LDS_AS u16*)(src + j * 4224) | ((unsigned)*(LDS_AS u16*)(src + 16896 + j * 4224) << 16);
        ST16(o, rw, fl * 8192 + i8 * 16, 0);
      }
      SCHED();
    };
    if (!dft) {
#pragma unroll
      for (int nt = 0; nt < 5; ++nt) {
        const int gp = p0 + 5 * wn + nt;
        c.bo[0][nt] = bbase + (gp >> 2) * 1024 + (((gp & 3) ^ hsw) * 32) + p * 8;
      }
#pragma unroll
      for (int mt = 0; mt < 2; ++mt)
#pragma unroll
        for (int nt = 0; nt < 5; ++nt) acc[mt][nt] = (f32x4){kEps, kEps, kEps, kEps};
      K1_HEAD(true, filt_load(), 9)
      WAIT_V(9); k1_step<true, 3>(acc, F1, F0, sm, c);
      WAIT_V(5); k1_step<true, 4>(acc, F0, F1, sm, c);
      filt_store();
      WAIT_V(8); k1_step<true, 5>(acc, F1, F0, sm, c);
      WAIT_V(8); k1_step<true, 6>(acc, F0, F1, sm, c);
      WAIT_V(5); k1_step<true, 7>(acc, F1, F0, sm, c);
      if (wave == 0) flag_store(ws, 1, e, lane);
      WAIT_V(5); k1_step<true, 8>(acc, F0, F1, sm, c);
      WAIT_V(0); k1_step<true, 9>(acc, F1, F0, sm, c);
      k1_mma<true, 0, 20>(acc, F1);
      TS(3);
#pragma unroll
      for (int mt = 0; mt < 2; ++mt)
#pragma unroll
        for (int nt = 0; nt < 5; ++nt)
#pragma unroll
          for (int j = 0; j < 4; ++j) pq[mt][nt][j] = phi_f(acc[mt][nt][j]);
    } else {
      if (wave < 4) kd_main<3>(ws, sm, c, rt, ct & 3, e, lane, filt_load, filt_store TS_PASS);
      else kd_main<2>(ws, sm, c, rt, ct & 3, e, lane, filt_load, filt_store TS_PASS);
    }
  }
  TS(4);
}

DI void publish(char* ws, int which, unsigned e) {
  WAIT_V(0);
  BAR();
  if (threadIdx.x < 64) flag_store(ws, which, e, threadIdx.x);
}
DI bool poll(char* ws, int which, unsigned e, bool n0, bool n1, int lane, unsigned tries = 1u << 17) {
  const rsrc_t rf = mk_rsrc(ws + WS_SYN + which * 65536 + (blockIdx.x & 31) * 2048, 2048);
  const u32x2 t = make_tag(e);
  for (unsigned spin = 0; spin < tries; ++spin) {
    const u32x4 f0 = __builtin_amdgcn_raw_buffer_load_b128(rf, n0 ? lane * 16 : 0x7fffffffu, 0, 16);
    const u32x4 f1 = __builtin_amdgcn_raw_buffer_load_b128(rf, n1 ? lane * 16 + 1024 : 0x7fffffffu, 0, 16);
    n0 = n0 && !(f0[0] == t[0] && f0[1] == t[1] && f0[2] == t[0] && f0[3] == t[1]);
    n1 = n1 && !(f1[0] == t[0] && f1[1] == t[1] && f1[2] == t[0] && f1[3] == t[1]);
    if (!__any(n0 || n1)) return true;
    __builtin_amdgcn_s_sleep(POLL_SLEEP);
  }
  return false;
}
DI void await(char* ws, int which, unsigned e, int need, int wave, int lane) {
  if (wave == 7) poll(ws, which, e, (need >> (lane < 32 ? 0 : 1)) & 1, (need >> (lane < 32 ? 1 : 2)) & 1, lane);
  asm volatile("" ::: "memory");
  BAR();
  asm volatile("" ::: "memory");
}
DI void dma16_sc1(rsrc_t rs, unsigned lds, unsigned vo, int so) {
  asm volatile("s_mov_b32 m0, %2\n\ts_nop 0\n\tbuffer_load_dwordx4 %0, %1, %3 offen sc1 lds" :: "v"(vo), "s"(rs), "s"(lds), "s"(so));
}
#define LD16(rs, vo, so) __builtin_amdgcn_raw_buffer_load_b128((rs), (vo), (so), LD_AUX)

constexpr int P3_HALF = 71680;

struct FftC {
  const u32x4 (&fre)[4]; const u32x4 (&fim)[4]; const u32x4 (&fc)[2]; const u32x4 (&fs)[2]; const u32x4 (&fn)[2];
  const f32x2 (&tc)[2][2]; const f32x2 (&tsn)[2][2]; const unsigned (&uo)[2][4]; const unsigned (&ro)[4]; unsigned vy;
};
DI f32x2 half2(const f32x4& v, int h) { return h ? (f32x2){v[2], v[3]} : (f32x2){v[0], v[1]}; }
constexpr int FSECOND = 304;
template <int NC> DI void fft_run(char* ws, lptr sm, const FftC& T, const u32x4 (&wv)[2][2], int fu0, int wave8, int lane) {
  const f32x4 z4 = {0.f, 0.f, 0.f, 0.f};
  f32x2 wre[NC][2][2], wim[NC][2][2];
#pragma unroll
  for (int c = 0; c < NC; ++c) {
    const int f = min((c ? FSECOND + (fu0 >> 1) : fu0), 320);
    const float sc = ((f == 0 || f == 320) ? 1.f : 2.f) / (2048.f * 640.f);
#pragma unroll
    for (int t = 0; t < 2; ++t)
#pragma unroll
      for (int h = 0; h < 2; ++h) {
        wre[c][t][h] = (f32x2){bf_lo(wv[c][t][2 * h]), bf_lo(wv[c][t][2 * h + 1])} * sc;
        wim[c][t][h] = (f32x2){bf_hi(wv[c][t][2 * h]), bf_hi(wv[c][t][2 * h + 1])} * sc;
      }
  }
#define FOR_C _Pragma("unroll") for (int c = 0; c < NC; ++c)
#define FOR_T _Pragma("unroll") for (int t = 0; t < 2; ++t)
#define FOR_H _Pragma("unroll") for (int h = 0; h < 2; ++h)
#define FOR_K _Pragma("unroll") for (int ks = 0; ks < 4; ++ks)
  f32x4 re[NC][2], im[NC][2];
  u32x4 bre[NC], bim[NC];
  {
    bf16x8 xa[NC][2][4];
    FOR_K FOR_C FOR_T xa[c][t][ks] = *(LDS_AS bf16x8*)(sm + c * 16384 + lane * 16 + (t * 4 + ks) * 1024);
    FOR_K FOR_C FOR_T {
      re[c][t] = MFMA16(xa[c][t][ks], BC(T.fre[ks]), ks ? re[c][t] : z4);
      im[c][t] = MFMA16(xa[c][t][ks], BC(T.fim[ks]), ks ? im[c][t] : z4);
    }
  }
  FOR_C FOR_T FOR_H {
    const f32x2 a = half2(re[c][t], h), d = half2(im[c][t], h);
    const f32x2 x = a * T.tc[t][h] + d * T.tsn[t][h], y = d * T.tc[t][h] - a * T.tsn[t][h];
    bre[c][2 * t + h] = pk2(x[0], x[1]); bim[c][2 * t + h] = pk2(y[0], y[1]);
  }
  FOR_C FOR_T { re[c][t] = MFMA16(BC(T.fc[t]), BC(bre[c]), z4); im[c][t] = MFMA16(BC(T.fc[t]), BC(bim[c]), z4); }
  FOR_C FOR_T { re[c][t] = MFMA16(BC(T.fs[t]), BC(bim[c]), re[c][t]); im[c][t] = MFMA16(BC(T.fn[t]), BC(bre[c]), im[c][t]); }
  FOR_C FOR_T FOR_H {
    const f32x2 a = half2(re[c][t], h), d = half2(im[c][t], h);
    const f32x2 x = a * wre[c][t][h] - d * wim[c][t][h], y = a * wim[c][t][h] + d * wre[c][t][h];
    bre[c][2 * t + h] = pk2(x[0], x[1]); bim[c][2 * t + h] = pk2(y[0], y[1]);
  }
  FOR_C FOR_T { re[c][t] = MFMA16(BC(T.fc[t]), BC(bre[c]), z4); im[c][t] = MFMA16(BC(T.fc[t]), BC(bim[c]), z4); }
  FOR_C FOR_T { re[c][t] = MFMA16(BC(T.fn[t]), BC(bim[c]), re[c][t]); im[c][t] = MFMA16(BC(T.fs[t]), BC(bre[c]), im[c][t]); }
  FOR_C FOR_T FOR_H {
    const f32x2 a = half2(re[c][t], h), d = half2(im[c][t], h);
    const f32x2 x = a * T.tc[t][h] - d * T.tsn[t][h], y = -(d * T.tc[t][h]) - a * T.tsn[t][h];
    *(LDS_AS unsigned*)(sm + c * 16384 + T.uo[t][2 * h]) = pk2(x[0], y[0]);
    *(LDS_AS unsigned*)(sm + c * 16384 + T.uo[t][2 * h + 1]) = pk2(x[1], y[1]);
  }
  WAIT_L0();
  BAR();
  SCHED();
  {
    bf16x8 ua[NC][2][4];
    FOR_K FOR_C FOR_T ua[c][t][ks] = *(LDS_AS bf16x8*)(sm + c * 16384 + T.ro[ks] + t * 4096);
    FOR_K FOR_C FOR_T {
      re[c][t] = MFMA16(ua[c][t][ks], BC(T.fre[ks]), ks ? re[c][t] : z4);
      im[c][t] = MFMA16(ua[c][t][ks], BC(T.fim[ks]), ks ? im[c][t] : z4);
    }
  }
  FOR_C {
    const int fu = (c ? FSECOND + (fu0 >> 1) : fu0), f = min(fu, 320);
    const rsrc_t ry = mk_rsrc(ws + WS_Y + (size_t)f * 4096, fu > 320 ? 0 : ((f == 0 || f == 320) ? 4096 : 321 * 4096));
    ST16(widen((u32x2){pk2(re[c][0][0], re[c][0][1]), pk2(re[c][0][2], re[c][0][3])}, (u32x2){pk2(re[c][1][0], re[c][1][1]), pk2(re[c][1][2], re[c][1][3])}), ry, T.vy, 0);
    ST16(widen((u32x2){pk2(im[c][0][0], im[c][0][1]), pk2(im[c][0][2], im[c][0][3])}, (u32x2){pk2(im[c][1][0], im[c][1][1]), pk2(im[c][1][2], im[c][1][3])}), ry, T.vy, 320 * 4096);
  }
}

DI void phase2(const Args& A, lptr sm0, unsigned e, const f32x4 (&pq)[2][5] TS_ARG) {
  char* ws = A.ws;
  const int lane = threadIdx.x & 63, tid = threadIdx.x & 255;
  const int wave8 = __builtin_amdgcn_readfirstlane(threadIdx.x >> 6);
  const int wave = wave8 & 3, team = wave8 >> 2;
  lptr sm = sm0 + team * P3_HALF;
  const unsigned smu = (unsigned)(size_t)sm;
  const int b = blockIdx.x;
  const int rt1 = p1_rt(b), ct1 = p1_ct(b);
  const bool iskv = ct1 >= 4 && ct1 < 12;
  const int v = (ct1 - 4) * 16 + rt1;
  const int r = lane & 15, g = lane >> 4;
  const f32x4 z4 = {0.f, 0.f, 0.f, 0.f};
  const bool isfft = ct1 >= 12 || (iskv && v < 88);
  publish(ws, 1, e);
  TS(5);
  if (isfft) {
    const int fu0 = (iskv ? 128 + 2 * v : 2 * (rt1 * 4 + (ct1 & 3))) + team;
    const bool two = fu0 < 34 && team == 0;
    u32x4 wv[2][2];
    auto loads = [&]() {
#pragma unroll
      for (int c = 0; c < 2; ++c) {
        const int f = min(c ? FSECOND + (fu0 >> 1) : fu0, 320);
        const rsrc_t rx = mk_rsrc(ws + WS_X1 + (size_t)f * 8192, (c && !two) ? 0 : 8192), rw = mk_rsrc(ws + WS_WT + (size_t)f * 8192, (c && !two) ? 0 : 8192);
        dma16_sc1(rx, smu + c * 16384 + wave * 2048, lane * 16, wave * 2048);
        dma16_sc1(rx, smu + c * 16384 + wave * 2048 + 1024, lane * 16, wave * 2048 + 1024);
        wv[c][0] = LD16(rw, lane * 16, wave * 2048);
        wv[c][1] = LD16(rw, lane * 16 + 1024, wave * 2048);
      }
      SCHED();
    };
    await(ws, 1, e, 5, wave8, lane);
    TS(7);
    loads();
    u32x4 fre[4], fim[4], fc[2], fs[2], fn[2];
    f32x2 tc[2][2], tsn[2][2];
    unsigned uo[2][4], ro[4];
    {
      const float a64 = (float)(16 * wave + r) * (1.f / 64.f);
      const unsigned m2 = (r & 2) ? 0x80008000u : 0u, m1 = (r & 1) ? 0x80008000u : 0u;
      const unsigned m3 = ((r + 1) & 2) ? 0x80008000u : 0u;
#pragma unroll
      for (int j = 0; j < 4; ++j) {
        const float ang = a64 * (float)(4 * g + j);
        const float c = __builtin_amdgcn_cosf(ang), s = __builtin_amdgcn_sinf(ang);
        const unsigned p = pk2(c, s), q = pk2(-s, c);
        fre[0][j] = p; fim[0][j] = q;
        fre[1][j] = ((r & 1) ? q : p) ^ m2;
        fim[1][j] = ((r & 1) ? p : q) ^ m3;
        fre[2][j] = p ^ m1; fim[2][j] = q ^ m1;
        fre[3][j] = fre[1][j] ^ m1; fim[3][j] = fim[1][j] ^ m1;
      }
      const float a32 = (float)r * (1.f / 32.f);
#pragma unroll
      for (int j = 0; j < 2; ++j) {
        const float a0 = a32 * (float)(4 * g + 2 * j), a1 = a0 + a32;
        const unsigned c = pk2(__builtin_amdgcn_cosf(a0), __builtin_amdgcn_cosf(a1)), s = pk2(__builtin_amdgcn_sinf(a0), __builtin_amdgcn_sinf(a1));
        fc[0][j] = c; fc[0][2 + j] = c ^ m1; fs[0][j] = s; fs[0][2 + j] = s ^ m1;
      }
#pragma unroll
      for (int j = 0; j < 4; ++j) {
        fc[1][j] = fc[0][j] ^ 0x80000000u; fs[1][j] = fs[0][j] ^ 0x80000000u;
        fn[0][j] = fs[0][j] ^ 0x80008000u; fn[1][j] = fs[0][j] ^ 0x00008000u;
        ro[j] = 8192 + r * 256 + (((4 * j + g) ^ r) * 16);
      }
#pragma unroll
      for (int t = 0; t < 2; ++t)
#pragma unroll
        for (int j = 0; j < 4; ++j) {
          const int s2 = 16 * t + 4 * g + j;
          const float a = a64 * (1.f / 32.f) * (float)s2;
          tc[t][j >> 1][j & 1] = __builtin_amdgcn_cosf(a);
          tsn[t][j >> 1][j & 1] = __builtin_amdgcn_sinf(a);
          uo[t][j] = 8192 + s2 * 256 + (((4 * wave + (r >> 2)) ^ (s2 & 15)) * 16) + (r & 3) * 4;
        }
    }
    unsigned vy = wave * 1024 + r * 64 + (g & 1) * 32 + (g >> 1) * 16;
#pragma unroll
    for (int ks = 0; ks < 4; ++ks) { asm volatile("" : "+v"(fre[ks]), "+v"(fim[ks]), "+v"(ro[ks])); }
#pragma unroll
    for (int t = 0; t < 2; ++t) {
      asm volatile("" : "+v"(fc[t]), "+v"(fs[t]), "+v"(fn[t]), "+v"(tc[t][0]), "+v"(tc[t][1]), "+v"(tsn[t][0]), "+v"(tsn[t][1]));
      asm volatile("" : "+v"(uo[t][0]), "+v"(uo[t][1]), "+v"(uo[t][2]), "+v"(uo[t][3]), "+v"(vy));
    }
    SCHED();
    TS(6);
    FftC T = {fre, fim, fc, fs, fn, tc, tsn, uo, ro, vy};
    WAIT_V(0);
    asm volatile("" : "+v"(wv[0][0]), "+v"(wv[0][1]), "+v"(wv[1][0]), "+v"(wv[1][1]));
    BAR();
    SCHED();
    TS(8);
    if (two) fft_run<2>(ws, sm, T, wv, fu0, wave8, lane);
    else fft_run<1>(ws, sm, T, wv, fu0, wave8, lane);
  } else if (iskv) {
    const int ks = (v - 88) >> 1, cp = 2 * ((v - 88) & 1) + team;
    const int hf = (32 * ks) / 20, e0 = 20 * hf;
    const int q = r >> 2, p = lane & 3;
    const int hsw = ((q >> 1) & 1) | ((g & 1) << 1);
    const int L1 = (160 * cp) >> 7, L2 = (1120 - 160 * cp) >> 7;
    lptr wb = sm;
    {
      const rsrc_t rwo = mk_rsrc(A.wo, 640 * 1280);
      const int line = (wave < 2 ? L1 : L2) + (wave & 1), row = lane >> 3;
#pragma unroll
      for (int kb = 0; kb < 8; ++kb) {
        const int pair = ((lane & 7) >> 1) ^ (((row >> 1) & 1) | ((kb & 1) << 1));
        dma16_asm(rwo, smu + (wave * 8 + kb) * 1024, min(e0 + 8 * kb + row, 639) * 1280 + line * 128 + pair * 32 + (lane & 1) * 16, 0);
      }
    }
    lptr ab = sm0 + 32768;
    if (threadIdx.x < 288) *(LDS_AS u32x4*)(ab + threadIdx.x * 16) = (u32x4){0u, 0u, 0u, 0u};
    const int t5 = min((int)threadIdx.x >> 1, 159), kl = t5 / 5, qd = t5 - kl * 5;
    const int k = 32 * ks + kl, h = k / 20, d = k - 20 * h;
    unsigned ko = h * 1920 + d * 96 + qd * 16 + (threadIdx.x & 1) * 8 * 61440, ao = kl * 144 + (20 * (h - hf) + 4 * qd) * 2;
    unsigned bo[3];
#pragma unroll
    for (int n = 0; n < 3; ++n) {
      const int tl = min(wave + 4 * n, 9);
      const int ctl = tl >= 5 ? 1 : 0, nt = tl - 5 * ctl, ctg = 2 * cp + ctl;
      const int i = 16 * nt + 4 * p;
      const int byte = 2 * (i < 40 ? 40 * ctg + i : 560 - 40 * ctg + i);
      const int pl = i < 40 ? (byte >> 7) - L1 : 2 + (byte >> 7) - L2;
      bo[n] = (pl * 8 + g) * 1024 + q * 128 + ((((byte >> 5) & 3) ^ hsw) * 32) + (byte & 31);
      asm volatile("" : "+v"(bo[n]));
    }
    asm volatile("" : "+v"(ko), "+v"(ao));
    WAIT_V(0);
    WAIT_L0();
    TS(6);
    await(ws, 1, e, 2, wave8, lane);
    TS(7);
    f32x4 pv[8];
    const rsrc_t rk = mk_rsrc(ws + WS_KVP, wave8 < 5 ? 983040 : 0);
#pragma unroll
    for (int i = 0; i < 8; ++i) pv[i] = __builtin_bit_cast(f32x4, LD16(rk, ko, i * 61440));
    SCHED();
    bf16x8 bw[3][2];
#pragma unroll
    for (int n = 0; n < 3; ++n)
#pragma unroll
      for (int s = 0; s < 2; ++s) {
        const s16x4 lo = TRREAD(wb + bo[n] + s * 4096), hi = TRREAD(wb + bo[n] + s * 4096 + 512);
        bw[n][s] = __builtin_shufflevector(lo, hi, 0, 1, 2, 3, 4, 5, 6, 7);
      }
    SCHED();
    f32x4 sum = ((pv[0] + pv[1]) + (pv[2] + pv[3])) + ((pv[4] + pv[5]) + (pv[6] + pv[7]));
#pragma unroll
    for (int j = 0; j < 4; ++j) sum[j] += __uint_as_float(__builtin_amdgcn_update_dpp(0u, __float_as_uint(sum[j]), 0xB1, 0xf, 0xf, false));
    if (threadIdx.x < 320 && !(threadIdx.x & 1)) *(LDS_AS u32x2*)(ab + ao) = (u32x2){pk2(sum[0], sum[1]), pk2(sum[2], sum[3])};
    WAIT_L0();
    BAR();
    TS(8);
    bf16x8 a[2][2];
#pragma unroll
    for (int t = 0; t < 2; ++t)
#pragma unroll
      for (int s = 0; s < 2; ++s) a[t][s] = *(LDS_AS bf16x8*)(ab + (8 * (r >> 2) + (r & 3) + 4 * t) * 144 + s * 64 + g * 16);
    const rsrc_t rm = mk_rsrc(ws + WS_M + (size_t)(10 * cp) * 20480 + ks * 1024, 9 * 20480 + 1024);
#pragma unroll
    for (int n = 0; n < 3; ++n) {
      f32x4 d1 = MFMA16(a[0][0], bw[n][0], z4), d2 = MFMA16(a[1][0], bw[n][0], z4);
      d1 = MFMA16(a[0][1], bw[n][1], d1); d2 = MFMA16(a[1][1], bw[n][1], d2);
      ST16(__builtin_bit_cast(u32x4, pack8(d1, d2)), rm, lane * 16, (wave + 4 * n) * 20480);
    }
  } else if (ct1 < 4) {
    const int t8 = threadIdx.x, wm = wave8 >> 1, wn = wave8 & 1;
    const int gb = rt1 * 4 + ct1;
    pf_asm(mk_rsrc(&GT, 491520), (unsigned)(size_t)sm0 + K1_JUNK, t8 < 60 ? (gb * 60 + t8) * 128 : 0x7fffffffu, 0);
      const rsrc_t rx = mk_rsrc(A.x + (size_t)gb * 32 * 640, 32 * 1280), rg = mk_rsrc(A.gw, 1280);
    u32x4 gx[5], gg[5];
#pragma unroll
      for (int i = 0; i < 5; ++i) {
        gx[i] = __builtin_amdgcn_raw_buffer_load_b128(rx, t8 * 80 + i * 16, 0, 0);
        gg[i] = __builtin_amdgcn_raw_buffer_load_b128(rg, (t8 & 15) * 80 + i * 16, 0, 0);
      }
    u32x2 pk[2][5];
    unsigned zm[5];
#pragma unroll
    for (int nt = 0; nt < 5; ++nt) {
#pragma unroll
      for (int mt = 0; mt < 2; ++mt) {
        pk[mt][nt] = (u32x2){pk2(pq[mt][nt][0], pq[mt][nt][1]), pk2(pq[mt][nt][2], pq[mt][nt][3])};
        asm volatile("" : "+v"(pk[mt][nt]));
      }
      zm[nt] = ((4 * nt + g) / 5 == (r & 3)) ? 0xffffffffu : 0u;
      asm volatile("" : "+v"(zm[nt]));
    }
    const rsrc_t rz = mk_rsrc(ws + WS_ZP + ct1 * 640, 40960);
    const rsrc_t rp = mk_rsrc(ws + WS_P + (size_t)(8 * rt1 + 2 * wm) * 20480 + (20 * ct1 + 10 * wn) * 256, 2 * 20480);
    const unsigned vo = (g & 1) * 20480 + (g >> 1) * 256 + r * 16;
    SCHED();
    TS(6);
    await(ws, 1, e, 2, wave8, lane);
    TS(7);
    if (wave8 < 3) {
      float zv[16];
#pragma unroll
      for (int i = 0; i < 16; ++i) zv[i] = __uint_as_float(__builtin_amdgcn_raw_buffer_load_b32(rz, min(t8, 159) * 4, i * 2560, LD_AUX));
#pragma unroll
      for (int i = 0; i < 8; ++i) zv[i] += zv[i + 8];
      const float zs = ((zv[0] + zv[1]) + (zv[2] + zv[3])) + ((zv[4] + zv[5]) + (zv[6] + zv[7]));
      *(LDS_AS u16*)(sm0 + t8 * 2) = (u16)pk2(zs, zs);
    }
    WAIT_L0();
    BAR();
    TS(8);
    u32x2 zq[6];
#pragma unroll
    for (int nt = 0; nt < 5; ++nt) { zq[nt] = *(LDS_AS u32x2*)(sm0 + wn * 160 + nt * 32 + g * 8); zq[nt][0] &= zm[nt]; zq[nt][1] &= zm[nt]; }
    zq[5] = (u32x2){0u, 0u};
    u32x2 po[2][5];
#pragma unroll
    for (int mt = 0; mt < 2; ++mt) {
      f32x4 den = {kEps, kEps, kEps, kEps};
#pragma unroll
      for (int m = 0; m < 3; ++m) {
        const u32x4 za = {zq[2 * m][0], zq[2 * m][1], zq[2 * m + 1][0], zq[2 * m + 1][1]};
        const u32x2 p1 = m < 2 ? pk[mt][2 * m + 1] : (u32x2){0u, 0u};
        const u32x4 pb = {pk[mt][2 * m][0], pk[mt][2 * m][1], p1[0], p1[1]};
        den = MFMA16(BC(za), BC(pb), den);
      }
      f32x4 inv;
#pragma unroll
      for (int j = 0; j < 4; ++j) inv[j] = __builtin_amdgcn_rcpf(den[j]);
#pragma unroll
      for (int nt = 0; nt < 5; ++nt) {
        const float iv = nt == 0 ? inv[0] : nt == 4 ? inv[3] : (g < nt ? inv[nt - 1] : inv[nt]);
        const f32x4 w = pq[mt][nt] * iv;
        po[mt][nt] = (u32x2){pk2(w[0], w[1]), pk2(w[2], w[3])};
      }
    }
#pragma unroll
    for (int nt = 0; nt < 5; ++nt) ST16(widen(po[0][nt], po[1][nt]), rp, vo + nt * 512, 0);
    SCHED();
    {
      float s0 = 0.f, s1 = 0.f;
#pragma unroll
      for (int i = 0; i < 5; ++i) { s0 = dot2(gx[i][0], gg[i][0], s0); s1 = dot2(gx[i][1], gg[i][1], s1); s0 = dot2(gx[i][2], gg[i][2], s0); s1 = dot2(gx[i][3], gg[i][3], s1); }
      float s = s0 + s1;
#pragma unroll
      for (int o = 8; o > 0; o >>= 1) s += __shfl_xor(s, o);
      if ((t8 & 15) == 0) __builtin_amdgcn_raw_buffer_store_b32(__float_as_uint(1.f / (1.f + __expf(-s))), mk_rsrc(ws + WS_G, 8192), (gb * 32 + (t8 >> 4)) * 4, 0, 16);
    }
  }
  TS(9);
}

constexpr int LDS_ALL = K1_LDS;
static_assert(81920 + (2 * 64 * 84 + 2 * 64 * 52 + 64) * 4 <= LDS_ALL, "lds");

DI void phase3(const Args& A, lptr sm, unsigned e TS_ARG) {
  char* ws = A.ws;
  const int tid = threadIdx.x, lane = tid & 63;
  const int wave = __builtin_amdgcn_readfirstlane(tid >> 6);
  const int b = blockIdx.x, xcd = b & 7, idx = b >> 3;
  const int rt = xcd * 4 + (idx & 3);
  const int ct = idx >> 2;
  const int r = lane & 15, g = lane >> 4;
  LDS_AS float* red = (LDS_AS float*)(sm + 81920);
  constexpr int RA = 64 * 84, RE = 64 * 52, RG = 2 * RA + 2 * RE;
  const bool rev = P3_REV && (((idx >> 3) ^ idx) & 1);
  const int k0 = rev ? 9 : 0, kd = rev ? -1 : 1;
  u32x4 fg[10][3];
  if (wave == 2 || wave == 3) {
    const rsrc_t rg = mk_rsrc((const char*)&GT + (size_t)(2 * ct + wave - 2) * 30720, 30720);
#pragma unroll
    for (int i = 0; i < 30; ++i) fg[i / 3][i % 3] = __builtin_amdgcn_raw_buffer_load_b128(rg, lane * 16 + (i % 3) * 1024, (k0 + kd * (i / 3)) * 3072, 0);
  }
  await(ws, 2, e, 7, wave, lane);
  TS(11);
  if (wave < 2) {
    const rsrc_t ra = mk_rsrc(ws + WS_P + (size_t)(4 * rt) * 20480 + wave * 10240, 4 * 20480);
    const rsrc_t rb = mk_rsrc(ws + WS_M + (size_t)(5 * ct) * 20480 + wave * 10240, 5 * 20480);
    const unsigned vo = lane * 16;
    f32x4 acc[4][5];
#pragma unroll
    for (int mt = 0; mt < 4; ++mt)
#pragma unroll
      for (int nt = 0; nt < 5; ++nt) acc[mt][nt] = (f32x4){0.f, 0.f, 0.f, 0.f};
    u32x4 fa[10][4], fb[10][5];
    constexpr int PF = P3_PF;
    auto ld = [&](int ks) {
#pragma unroll
      for (int nt = 0; nt < 5; ++nt) fb[ks][nt] = LD16(rb, vo, nt * 20480 + (k0 + kd * ks) * 1024);
#pragma unroll
      for (int mt = 0; mt < 4; ++mt) fa[ks][mt] = LD16(ra, vo, mt * 20480 + (k0 + kd * ks) * 1024);
    };
#pragma unroll
    for (int ks = 0; ks < PF; ++ks) { ld(ks); SCHED(); }
#pragma unroll
    for (int ks = 0; ks < 10; ++ks) {
#pragma unroll
      for (int mt = 0; mt < 4; ++mt)
#pragma unroll
        for (int nt = 0; nt < 5; ++nt) acc[mt][nt] = MFMA16(BC(fb[ks][nt]), BC(fa[ks][mt]), acc[mt][nt]);
      if (ks < 9) SCHED();
      if (ks + PF < 10) ld(ks + PF);
      if (ks < 9) SCHED();
    }
    TS(12);
#pragma unroll
    for (int mt = 0; mt < 4; ++mt)
#pragma unroll
      for (int nt = 0; nt < 5; ++nt) *(LDS_AS f32x4*)(red + wave * RA + (16 * mt + r) * 84 + 16 * nt + 4 * g) = acc[mt][nt];
  } else if (wave < 4) {
    const int od = wave - 2;
    lptr yb = sm + od * 40960;
    const unsigned ybu = (unsigned)(size_t)yb;
    const rsrc_t ry = mk_rsrc(ws + WS_Y + (size_t)od * 320 * 4096 + rt * 128, 320 * 4096);
    const int row = lane >> 3;
    const unsigned vy0 = row * 4096 + ((((lane & 7) >> 1) ^ ((row >> 1) & 1)) * 32) + (lane & 1) * 16;
    const unsigned vy1 = vy0 ^ 64;
    auto issue = [&](int ks) {
#pragma unroll
      for (int i = 0; i < 4; ++i) dma16_sc1(ry, ybu + (ks * 4 + i) * 1024, (i & 1) ? vy1 : vy0, ((k0 + kd * ks) * 4 + i) * 32768);
    };
#pragma unroll
    for (int i = 0; i < 30; ++i) asm volatile("" : "+v"(fg[i / 3][i % 3]));
    issue(0); issue(1); issue(2);
    if (P3_YW == 5) { issue(3); issue(4); }
    const int q = r >> 2, p = lane & 3;
    const int hsw = ((q >> 1) & 1) | ((g & 1) << 1);
    unsigned yo[4];
#pragma unroll
    for (int mt = 0; mt < 4; ++mt) yo[mt] = g * 1024 + q * 128 + ((mt ^ hsw) * 32) + p * 8;
    f32x4 acc[4][3];
#pragma unroll
    for (int mt = 0; mt < 4; ++mt)
#pragma unroll
      for (int nt = 0; nt < 3; ++nt) acc[mt][nt] = (f32x4){0.f, 0.f, 0.f, 0.f};
#define K3_STEP(ks, n)                                                                                         \
    {                                                                                                          \
      WAIT_V(n);                                                                                               \
      SCHED();                                                                                                 \
      bf16x8 fy[4];                                                                                            \
      _Pragma("unroll") for (int mt = 0; mt < 4; ++mt) {                                                       \
        const s16x4 lo = TRREAD(yb + yo[mt] + ks * 4096), hi = TRREAD(yb + yo[mt] + ks * 4096 + 512);          \
        fy[mt] = __builtin_shufflevector(lo, hi, 0, 1, 2, 3, 4, 5, 6, 7);                                      \
      }                                                                                                        \
      SCHED();                                                                                                 \
      if (ks + P3_YW < 10) issue(ks + P3_YW);                                                                          \
      SCHED();                                                                                                 \
      _Pragma("unroll") for (int mt = 0; mt < 4; ++mt)                                                         \
        _Pragma("unroll") for (int nt = 0; nt < 3; ++nt) acc[mt][nt] = MFMA16(BC(fg[ks][nt]), fy[mt], acc[mt][nt]);   \
      SCHED();                                                                                                 \
    }
    K3_STEP(0, 8) K3_STEP(1, 8) K3_STEP(2, 8) K3_STEP(3, 8) K3_STEP(4, 8)
    K3_STEP(5, 8) K3_STEP(6, 8) K3_STEP(7, 8) K3_STEP(8, 4) K3_STEP(9, 0)
    TS(12);
#pragma unroll
    for (int mt = 0; mt < 4; ++mt)
#pragma unroll
      for (int nt = 0; nt < 3; ++nt) *(LDS_AS f32x4*)(red + 2 * RA + od * RE + (16 * mt + r) * 52 + 16 * nt + 4 * g) = acc[mt][nt];
  } else {
    if (wave == 4) red[RG + lane] = __uint_as_float(__builtin_amdgcn_raw_buffer_load_b32(mk_rsrc(ws + WS_G + rt * 256, 256), lane * 4, 0, LD_AUX));
  }
  const rsrc_t ro = mk_rsrc(A.out + (size_t)(64 * rt) * 640, 64 * 1280);
  unsigned oa[2], oe[2], oq[2], og[2], or_[2];
#pragma unroll
  for (int i = 0; i < 2; ++i) {
    const int it = i ? 512 + wave * 16 + (lane & 15) : tid;
    const int row = it / 10, gr = it - row * 10;
    oa[i] = (row * 84 + 8 * gr) * 4;
    oe[i] = (2 * RA + row * 52) * 4;
    oq[i] = oe[i] + (gr < 5 ? 8 * gr : 72 - 8 * gr) * 4;
    og[i] = (i && lane >= 16) ? 0x7fffffffu : row * 1280 + (gr < 5 ? 80 * ct : 1120 - 80 * ct) + 16 * gr;
    or_[i] = (RG + row) * 4;
    oa[i] |= (gr < 5) ? 0u : 0x80000000u;
    asm volatile("" : "+v"(oa[i]), "+v"(oe[i]), "+v"(oq[i]), "+v"(og[i]), "+v"(or_[i]));
  }
  WAIT_L0();
  BAR();
  TS(13);
#pragma unroll
  for (int i = 0; i < 2; ++i) {
    const bool lo = (int)oa[i] >= 0;
    lptr rb = sm + 81920, pa = rb + (oa[i] & 0x7fffffffu), pq = rb + oq[i];
    const float gq = *(LDS_AS float*)(rb + or_[i]);
    const f32x4 a0 = *(LDS_AS f32x4*)pa + *(LDS_AS f32x4*)(pa + RA * 4);
    const f32x4 a1 = *(LDS_AS f32x4*)(pa + 16) + *(LDS_AS f32x4*)(pa + RA * 4 + 16);
    const float yn = *(LDS_AS float*)(rb + oe[i] + (RE + 41) * 4);
    const f32x4 e0 = *(LDS_AS f32x4*)pq, e1 = *(LDS_AS f32x4*)(pq + 16), o0 = *(LDS_AS f32x4*)(pq + RE * 4), o1 = *(LDS_AS f32x4*)(pq + RE * 4 + 16);
    const float p8 = *(LDS_AS float*)(pq + 32) + *(LDS_AS float*)(pq + RE * 4 + 32);
    const f32x4 m0 = e0 - o0, m1 = e1 - o1, p0 = e0 + o0, p1 = e1 + o1;
    const f32x4 yy = {yn, -yn, yn, -yn};
    const f32x4 b0 = (lo ? m0 : (f32x4){p8, p1[3], p1[2], p1[1]}) + yy;
    const f32x4 b1 = (lo ? m1 : (f32x4){p1[0], p0[3], p0[2], p0[1]}) + yy;
    const f32x4 r0 = b0 + gq * (a0 - b0), r1 = b1 + gq * (a1 - b1);
    __builtin_amdgcn_raw_buffer_store_b128(__builtin_bit_cast(u32x4, pack8(r0, r1)), ro, og[i], 0, OUT_AUX);
  }
  TS(14);
}

#define TS_END()

__global__ __launch_bounds__(512, 2) void fused_kernel(Args A) {
  extern __shared__ __attribute__((aligned(16))) char smem[];
  lptr sm = (lptr)smem;
  TS_DECL;
  TS(0);
  {
    unsigned long long pc; unsigned len;
    asm volatile("s_getpc_b64 %0\n.Lfk_start:\n\ts_mov_b32 %1, .Lfk_end-.Lfk_start" : "=s"(pc), "=s"(len));
    pf_asm(mk_rsrc((const void*)pc, len), (unsigned)(size_t)sm + K1_JUNK + 1792, (threadIdx.x * 32 + (blockIdx.x >> 3)) * 128, 0);
  }
  const rsrc_t re = mk_rsrc(A.ws + WS_EPOCH, 4);
  const unsigned e = *(const __attribute__((address_space(4))) unsigned*)(A.ws + WS_EPOCH);
  f32x4 pq[2][5];
  phase1(A, sm, pq, e TS_PASS);
  phase2(A, sm, e, pq TS_PASS);
  publish(A.ws, 2, e);
  TS(10);
  phase3(A, sm, e TS_PASS);
  if (blockIdx.x == 0 && threadIdx.x == 0) __builtin_amdgcn_raw_buffer_store_b32(e + 1u, re, 0, 0, 16);
  TS_END();
  asm volatile(".Lfk_end:");
}

extern "C" void kernel_launch(void* const* d_in, const int* in_sizes, int n_in, void* d_out, int out_size, void* d_ws,
                              size_t ws_size, hipStream_t stream) {
  Args A;
  A.x = (const u16*)d_in[0]; A.wqkv = (const u16*)d_in[1]; A.wo = (const u16*)d_in[2];
  A.wr = (const u16*)d_in[4]; A.wi = (const u16*)d_in[5]; A.gw = (const u16*)d_in[6];
  A.ws = (char*)d_ws; A.out = (u16*)d_out;
  (void)hipFuncSetAttribute((const void*)fused_kernel, hipFuncAttributeMaxDynamicSharedMemorySize, LDS_ALL);
  fused_kernel<<<256, 512, LDS_ALL, stream>>>(A);
}
```

```cpp
#include <hip/hip_runtime.h>
#include <hip/hip_bf16.h>
#include <utility>

typedef unsigned short u16;
typedef __attribute__((ext_vector_type(8))) short bf16x8;
typedef __attribute__((ext_vector_type(4))) short s16x4;
typedef __attribute__((ext_vector_type(4))) float f32x4;
typedef __attribute__((ext_vector_type(2))) float f32x2;
typedef __attribute__((ext_vector_type(2))) __bf16 bf16x2_t;
typedef __attribute__((ext_vector_type(4))) unsigned u32x4;
typedef __attribute__((ext_vector_type(2))) unsigned u32x2;
#define LDS_AS __attribute__((address_space(3)))
#define DI __device__ __forceinline__
#define MFMA16(a, b, c) __builtin_amdgcn_mfma_f32_16x16x32_bf16((a), (b), (c), 0, 0, 0)
typedef LDS_AS char* lptr;
typedef __amdgpu_buffer_rsrc_t rsrc_t;

#define ST_AUX 16
#define OUT_AUX 0
#define K1_ROT 5
#define POLL_SLEEP 2
#define CODE_PF 1
#define P3_REV 0
#define P3_PF 3
#define P3_YW 3
#define P3_MAP 1
#define LD_AUX 16

constexpr double kPi = 3.14159265358979323846;
constexpr double ccos_s(double x) { double x2 = x * x, t = 1, s = 1; for (int i = 1; i < 12; ++i) { t *= -x2 / ((2 * i - 1) * (2 * i)); s += t; } return s; }
constexpr double csin_s(double x) { double x2 = x * x, t = x, s = x; for (int i = 1; i < 12; ++i) { t *= -x2 / ((2 * i) * (2 * i + 1)); s += t; } return s; }
constexpr double cos640(int m) {
  m %= 640; if (m > 320) m = 640 - m;
  bool neg = false; if (m > 160) { m = 320 - m; neg = true; }
  double v = (m <= 80) ? ccos_s(2 * kPi * m / 640) : csin_s(2 * kPi * (160 - m) / 640);
  return neg ? -v : v;
}
constexpr u16 to_bf16c(double v) { float f = (float)v; unsigned u = __builtin_bit_cast(unsigned, f); u += 0x7fffu + ((u >> 16) & 1u); return (u16)(u >> 16); }
struct C640 { u16 v[640]; };
constexpr C640 make_c640() { C640 c{}; for (int i = 0; i < 640; ++i) c.v[i] = to_bf16c(cos640(i)); return c; }
constexpr C640 COS640 = make_c640();

struct alignas(128) FRow { u16 v[10 * 512]; };
constexpr FRow make_frow(int id) {
  FRow t{};
  const int cq = id / 10, st = id % 10;
  for (int par = 0; par < 2; ++par) for (int nt = 0; nt < 5; ++nt) for (int l = 0; l < 64; ++l) {
    const int kk = 80 * cq + 16 * nt + (l & 15);
    for (int j = 0; j < 8; ++j) {
      const int d = 64 * st + 32 * (j >> 2) + 8 * (l >> 4) + 2 * (j & 3) + par;
      const int m = (kk == 1) ? (d * 160 + 160 * par) % 640 : (d * (kk >> 1) + 160 * (kk & 1)) % 640;
      t.v[((par * 5 + nt) * 64 + l) * 8 + j] = COS640.v[m];
    }
  }
  return t;
}
template <int I> constexpr FRow FROW = make_frow(I);
struct alignas(128) FTab { FRow r[40]; };
template <int... I> constexpr FTab make_ftab(std::integer_sequence<int, I...>) { return FTab{{FROW<I>...}}; }
__device__ const FTab FT = make_ftab(std::make_integer_sequence<int, 40>{});

struct alignas(128) GRow { u16 v[10 * 3 * 512]; };
constexpr GRow make_grow(int id) {
  GRow t{};
  const int ct = id >> 1, od = id & 1;
  for (int ks = 0; ks < 10; ++ks) for (int nt = 0; nt < 3; ++nt) for (int l = 0; l < 64; ++l) {
    const int ci = 16 * nt + (l & 15), d = 40 * ct + ci;
    for (int j = 0; j < 8; ++j) {
      const int f = 32 * ks + 8 * (l >> 4) + j;
      t.v[((ks * 3 + nt) * 64 + l) * 8 + j] = (ci <= 40) ? COS640.v[(f * d + (od ? 160 : 0)) % 640] : (u16)((od && ci == 41 && f == 0) ? 0x3f80 : 0);
    }
  }
  return t;
}
template <int I> constexpr GRow GROW = make_grow(I);
struct alignas(128) GTab { GRow r[16]; };
template <int... I> constexpr GTab make_gtab(std::integer_sequence<int, I...>) { return GTab{{GROW<I>...}}; }
__device__ const GTab GT = make_gtab(std::make_integer_sequence<int, 16>{});

constexpr size_t WS_P   = 0;
constexpr size_t WS_Y   = WS_P + 2621440;
constexpr size_t WS_X1  = WS_Y + 2621440;
constexpr size_t WS_WT  = WS_X1 + 321 * 8192;
constexpr size_t WS_KVP = WS_WT + 352 * 8192;
constexpr size_t WS_M   = WS_KVP + 983040;
constexpr size_t WS_G   = WS_M + 819200;
constexpr size_t WS_ZP  = WS_G + 8192;
constexpr size_t WS_SYN = WS_ZP + 40960;

DI rsrc_t mk_rsrc(const void* p, unsigned bytes) { return __builtin_amdgcn_make_buffer_rsrc((void*)p, 0, bytes, 0x00020000); }
DI unsigned pk2(float a, float b) { f32x2 v = {a, b}; return __builtin_bit_cast(unsigned, __builtin_convertvector(v, bf16x2_t)); }
DI float bf_lo(unsigned u) { return __uint_as_float(u << 16); }
DI float bf_hi(unsigned u) { return __uint_as_float(u & 0xffff0000u); }
DI float dot2(unsigned a, unsigned b, float c) { return __builtin_amdgcn_fdot2_f32_bf16(__builtin_bit_cast(bf16x2_t, a), __builtin_bit_cast(bf16x2_t, b), c, false); }
constexpr float kEps = 1e-6f, kL2E = 1.4426950408889634f;
DI float phi_f(float a) {
  float e;
  const float u = __builtin_fmaf(a, kL2E, -kEps * kL2E);
  asm("v_exp_f32_e64 %0, %1 clamp" : "=v"(e) : "v"(u));
  return __builtin_amdgcn_fmed3f(a, kEps, __builtin_inff()) + e;
}
DI bf16x8 pack8(const f32x4& a, const f32x4& b) {
  u32x4 p = {pk2(a[0], a[1]), pk2(a[2], a[3]), pk2(b[0], b[1]), pk2(b[2], b[3])};
  return __builtin_bit_cast(bf16x8, p);
}
DI u32x4 widen(u32x2 a, u32x2 b) {
  auto x = __builtin_amdgcn_permlane16_swap(a[0], b[0], false, false);
  auto y = __builtin_amdgcn_permlane16_swap(a[1], b[1], false, false);
  return (u32x4){x[0], y[0], x[1], y[1]};
}
#define WAIT_V_(n) asm volatile("s_waitcnt vmcnt(" #n ")" ::: "memory")
#define WAIT_V(n) WAIT_V_(n)
#define BC(v) __builtin_bit_cast(bf16x8, (v))
#define WAIT_L0() __builtin_amdgcn_s_waitcnt(0xC07F)
#define BAR() __builtin_amdgcn_s_barrier()
#define SCHED() __builtin_amdgcn_sched_barrier(0)
#define TRREAD(p) __builtin_amdgcn_ds_read_tr16_b64_v4i16((LDS_AS s16x4*)(p))
#define ST16(v, rs, vo, so) __builtin_amdgcn_raw_buffer_store_b128((v), (rs), (vo), (so), ST_AUX)

#define TS_DECL
#define TS(i)
#define TC(i)
#define TS_FLUSH(kern)
#define TS_FLUSH2()
#define TS_ARG
#define TS_PASS

constexpr int K1_STAGE = 40960;
constexpr int K1_WF = 3 * K1_STAGE;
constexpr int K1_JUNK = K1_WF + 33792;
constexpr int K1_LDS = K1_JUNK + 2048;

struct Frags { bf16x8 a[2][2]; bf16x8 b[2][5]; };
struct K1Ctx { rsrc_t ra, rb; unsigned va[2], vb[3], ao[3][2], bo[3][5], ldsa, ldsb; int st, rs, wave; };

template <int I0, int I1> DI void k1_read(Frags& F, lptr sm, const unsigned (&ao)[2], const unsigned (&bo)[5]) {
#pragma unroll
  for (int i = I0; i < I1; ++i) {
    const int k = i / 7, j = i % 7;
    if (j < 2) F.a[k][j] = *(LDS_AS bf16x8*)(sm + ao[k] + j * 2048);
    else {
      const s16x4 lo = TRREAD(sm + bo[j - 2] + k * 12288), hi = TRREAD(sm + bo[j - 2] + k * 12288 + 512);
      F.b[k][j - 2] = __builtin_shufflevector(lo, hi, 0, 1, 2, 3, 4, 5, 6, 7);
    }
  }
}
DI void dma16_asm(rsrc_t rs, unsigned lds, unsigned vo, int so) {
  asm volatile("s_mov_b32 m0, %2\n\ts_nop 0\n\tbuffer_load_dwordx4 %0, %1, %3 offen lds" :: "v"(vo), "s"(rs), "s"(lds), "s"(so));
}
DI void pf_asm(rsrc_t rs, unsigned lds, unsigned vo, int so) {
  asm volatile("s_mov_b32 m0, %2\n\ts_nop 0\n\tbuffer_load_dword %0, %1, %3 offen lds" :: "v"(vo), "s"(rs), "s"(lds), "s"(so));
}
template <int BUF, int I> DI void k1_issue1(const K1Ctx& c) {
  if (I < 2) dma16_asm(c.ra, c.ldsa + BUF * K1_STAGE + I * 1024, c.va[I < 2 ? I : 0], c.st * 128);
  else dma16_asm(c.rb, c.ldsb + BUF * K1_STAGE + (I - 2) * 1024, c.vb[I < 2 ? 0 : I - 2], c.st * 64 * c.rs);
}
template <int BUF> DI void k1_issue(K1Ctx& c) {
  k1_issue1<BUF, 0>(c); k1_issue1<BUF, 1>(c); k1_issue1<BUF, 2>(c); k1_issue1<BUF, 3>(c); k1_issue1<BUF, 4>(c);
  c.st = (c.st == 9) ? 0 : c.st + 1;
}
template <bool SWAP, int I0, int I1> DI void k1_mma(f32x4 (&acc)[2][5], const Frags& F) {
#pragma unroll
  for (int i = I0; i < I1; ++i) {
    const int k = i / 10, mt = (i / 5) & 1, nt = i % 5;
    acc[mt][nt] = SWAP ? MFMA16(F.b[k][nt], F.a[k][mt], acc[mt][nt]) : MFMA16(F.a[k][mt], F.b[k][nt], acc[mt][nt]);
  }
}
template <bool SWAP, int N, int SEG> DI void k1_seg(f32x4 (&acc)[2][5], Frags& Fn, const Frags& Fp, lptr sm, K1Ctx& c) {
  constexpr int R0[6] = {0, 4, 7, 10, 12, 14};
  k1_read<R0[SEG], R0[SEG + 1]>(Fn, sm, c.ao[N % 3], c.bo[N % 3]);
  if (N > 0) {
    k1_mma<SWAP, 4 * SEG, 4 * SEG + 4>(acc, Fp);
    if (SEG < 2) {
      __builtin_amdgcn_sched_group_barrier(0x008, 1, 0); __builtin_amdgcn_sched_group_barrier(0x100, 2, 0);
      __builtin_amdgcn_sched_group_barrier(0x008, 1, 0); __builtin_amdgcn_sched_group_barrier(0x100, 2, 0);
      __builtin_amdgcn_sched_group_barrier(0x008, 1, 0); __builtin_amdgcn_sched_group_barrier(0x100, 2, 0);
      __builtin_amdgcn_sched_group_barrier(0x008, 1, 0);
    } else {
#pragma unroll
      for (int i = 0; i < 4; ++i) { __builtin_amdgcn_sched_group_barrier(0x008, 1, 0); __builtin_amdgcn_sched_group_barrier(0x100, 2, 0); }
    }
  }
  SCHED();
  if (N + 2 <= 9) k1_issue1<(N + 2) % 3, SEG>(c);
  SCHED();
}
template <bool SWAP, int N> DI void k1_step(f32x4 (&acc)[2][5], Frags& Fn, const Frags& Fp, lptr sm, K1Ctx& c) {
  WAIT_L0();
  BAR();
  SCHED();
  k1_seg<SWAP, N, 0>(acc, Fn, Fp, sm, c);
  k1_seg<SWAP, N, 1>(acc, Fn, Fp, sm, c);
  k1_seg<SWAP, N, 2>(acc, Fn, Fp, sm, c);
  k1_seg<SWAP, N, 3>(acc, Fn, Fp, sm, c);
  k1_seg<SWAP, N, 4>(acc, Fn, Fp, sm, c);
  c.st = (c.st == 9) ? 0 : c.st + 1;
}

struct DFrags { u32x4 a[2][2]; bf16x8 b[2][3]; };
constexpr int KD_STAGE = 26624;
template <int BUF, int I> DI void kd_issue1(const K1Ctx& c) {
  if (I < 2) dma16_asm(c.ra, c.ldsa + BUF * KD_STAGE + I * 1024, c.va[I & 1], c.st * 128);
  else if (I == 2) dma16_asm(c.rb, c.ldsb + BUF * KD_STAGE, c.vb[0], c.st * 10240);
  else dma16_asm(c.rb, c.wave < 2 ? c.ldsb + BUF * KD_STAGE + 8192 : c.ldsa - 2048 * c.wave + K1_JUNK, c.vb[1], c.st * 10240);
}
template <int BUF> DI void kd_issue(K1Ctx& c) {
  kd_issue1<BUF, 0>(c); kd_issue1<BUF, 2>(c); kd_issue1<BUF, 1>(c); kd_issue1<BUF, 3>(c);
  c.st = (c.st == 9) ? 0 : c.st + 1;
}
template <int NT> DI void kd_mma(f32x4 (&acc)[2][2][3], const DFrags& F) {
#pragma unroll
  for (int mt = 0; mt < 2; ++mt) {
    u32x4 ev, od;
#pragma unroll
    for (int k = 0; k < 2; ++k)
#pragma unroll
      for (int i = 0; i < 2; ++i) {
        ev[2 * k + i] = __builtin_amdgcn_perm(F.a[mt][k][2 * i + 1], F.a[mt][k][2 * i], 0x05040100u);
        od[2 * k + i] = __builtin_amdgcn_perm(F.a[mt][k][2 * i + 1], F.a[mt][k][2 * i], 0x07060302u);
      }
#pragma unroll
    for (int nt = 0; nt < NT; ++nt) {
      acc[0][mt][nt] = MFMA16(F.b[0][nt], BC(ev), acc[0][mt][nt]);
      acc[1][mt][nt] = MFMA16(F.b[1][nt], BC(od), acc[1][mt][nt]);
    }
  }
}
template <int NT, int N> DI void kd_step(f32x4 (&acc)[2][2][3], DFrags& Fn, const DFrags& Fp, lptr sm, K1Ctx& c) {
  WAIT_L0();
  BAR();
  SCHED();
  if (N + 3 <= 9) kd_issue<(N + 3) % 4>(c);
  SCHED();
  constexpr int H = (N % 4) >> 1, O = (N & 1) * KD_STAGE;
#pragma unroll
  for (int mt = 0; mt < 2; ++mt)
#pragma unroll
    for (int k = 0; k < 2; ++k) Fn.a[mt][k] = *(LDS_AS u32x4*)(sm + c.ao[H][k] + O + mt * 2048);
#pragma unroll
  for (int par = 0; par < 2; ++par)
#pragma unroll
    for (int nt = 0; nt < NT; ++nt) Fn.b[par][nt] = *(LDS_AS bf16x8*)(sm + c.bo[H][0] + O + (par * 5 + nt) * 1024);
  if (N > 0) {
    kd_mma<NT>(acc, Fp);
    __builtin_amdgcn_sched_group_barrier(0x100, 2, 0);
    __builtin_amdgcn_sched_group_barrier(0x002, 8, 0);
#pragma unroll
    for (int i = 0; i < 2 * NT; ++i) { __builtin_amdgcn_sched_group_barrier(0x008, 1, 0); __builtin_amdgcn_sched_group_barrier(0x100, 1, 0); __builtin_amdgcn_sched_group_barrier(0x002, 2, 0); }
  }
  SCHED();
}

DI u32x2 make_tag(unsigned e) { return (u32x2){e + 1u, (e + 1u) * 0x9E3779B9u ^ 0x5bd1e995u}; }
DI int p1_rt(int b) { return (b & 1) * 8 + ((b >> 3) & 7); }
DI int p1_ct(int b) { const int x2 = (b & 7) >> 1, j = b >> 6; return j == 0 ? x2 : j == 3 ? 12 + x2 : 3 + 2 * x2 + j; }
DI int flag_slot(int b) { return p1_ct(b) * 16 + p1_rt(b); }
constexpr size_t WS_EPOCH = WS_SYN + 3 * 65536;
DI void flag_store(char* ws, int which, unsigned e, int lane) {
  __builtin_amdgcn_raw_buffer_store_b64(make_tag(e), mk_rsrc(ws + WS_SYN + which * 65536, 65536), lane * 2048 + flag_slot(blockIdx.x) * 8, 0, 16);
}
template <int NT, class FL, class FS> DI void kd_main(char* ws, lptr sm, K1Ctx& c, int rt, int cq, unsigned e, int lane, FL filt_load, FS filt_store TS_ARG) {
  const int wm = c.wave & 3, r = lane & 15, g = lane >> 4;
  constexpr int T0 = NT == 3 ? 0 : 3;
  DFrags D0, D1;
  f32x4 acc[2][2][3];
#pragma unroll
  for (int u = 0; u < 2; ++u) {
    c.ao[u][0] = 2 * u * KD_STAGE + (32 * wm + r) * 128 + ((g ^ (r >> 1)) * 16);
    c.ao[u][1] = 2 * u * KD_STAGE + (32 * wm + r) * 128 + (((4 + g) ^ (r >> 1)) * 16);
    c.bo[u][0] = 2 * u * KD_STAGE + 16384 + T0 * 1024 + lane * 16;
    asm volatile("" : "+v"(c.ao[u][0]), "+v"(c.ao[u][1]), "+v"(c.bo[u][0]));
  }
#pragma unroll
  for (int i = 0; i < 4 * NT; ++i) { acc[i / (2 * NT)][(i / NT) & 1][i % NT] = (f32x4){0.f, 0.f, 0.f, 0.f}; asm volatile("" : "+v"(acc[i / (2 * NT)][(i / NT) & 1][i % NT])); }
  TS(1);
  WAIT_V(8); kd_step<NT, 0>(acc, D0, D1, sm, c);
  TS(2);
  WAIT_V(8); kd_step<NT, 1>(acc, D1, D0, sm, c);
  WAIT_V(8); kd_step<NT, 2>(acc, D0, D1, sm, c);
  WAIT_V(8); kd_step<NT, 3>(acc, D1, D0, sm, c);
  filt_load();
  WAIT_V(12); kd_step<NT, 4>(acc, D0, D1, sm, c);
  WAIT_V(12); kd_step<NT, 5>(acc, D1, D0, sm, c);
  WAIT_V(12); kd_step<NT, 6>(acc, D0, D1, sm, c);
  WAIT_V(8); kd_step<NT, 7>(acc, D1, D0, sm, c);
  WAIT_V(4); kd_step<NT, 8>(acc, D0, D1, sm, c);
  filt_store();
  WAIT_V(3); kd_step<NT, 9>(acc, D1, D0, sm, c);
  kd_mma<NT>(acc, D1);
  TS(3);
  const rsrc_t rx = mk_rsrc(ws + WS_X1 + (wm >> 1) * 4096 + (rt >> 2) * 1024 + (rt & 3) * 256 + (wm & 1) * 128, 321 * 8192);
  const bool up = r >= 8;
  const int fb = 40 * cq + 8 * T0 + 2 * g;
  const bool sp = (fb == 0);
#pragma unroll
  for (int nt = 0; nt < NT; ++nt) {
    unsigned it[4][2];
#pragma unroll
    for (int mt = 0; mt < 2; ++mt) {
      const f32x4 E = acc[0][mt][nt], O = acc[1][mt][nt], P = E + O, M = E - O;
      it[0][mt] = pk2(P[0], P[1]); it[1][mt] = pk2(P[2], P[3]); it[2][mt] = pk2(M[0], -M[1]); it[3][mt] = pk2(M[2], -M[3]);
      if (NT == 3 && nt == 0) {
        it[0][mt] = sp ? (it[0][mt] & 0xffffu) : it[0][mt]; it[2][mt] = sp ? (it[2][mt] & 0xffffu) : it[2][mt];
        __builtin_amdgcn_raw_buffer_store_b32(pk2(E[1], O[1]), rx, sp ? 160 * 8192 + (r & 7) * 16 + (r >> 3) * 8 + mt * 4 : 0x7fffffff, 0, ST_AUX);
      }
    }
#pragma unroll
    for (int h = 0; h < 2; ++h) {
      const unsigned t0 = __builtin_amdgcn_update_dpp(0u, up ? it[2 * h][0] : it[2 * h + 1][0], 0x128, 0xf, 0xf, false);
      const unsigned t1 = __builtin_amdgcn_update_dpp(0u, up ? it[2 * h][1] : it[2 * h + 1][1], 0x128, 0xf, 0xf, false);
      const u32x4 o = {up ? t0 : it[2 * h][0], up ? t1 : it[2 * h][1], up ? it[2 * h + 1][0] : t0, up ? it[2 * h + 1][1] : t1};
      const int f0 = fb + 8 * nt + (r >> 3);
      ST16(o, rx, (h ? 320 - f0 : f0) * 8192 + (r & 7) * 16, 0);
    }
  }
}

struct Args { const u16 *x, *wqkv, *wo, *wr, *wi, *gw; char* ws; u16* out; };

DI void phase1(const Args& A, lptr sm, f32x4 (&pq)[2][5], unsigned e TS_ARG) {
  const u16* x = A.x; const u16* wqkv = A.wqkv; const u16* wr = A.wr; const u16* wi = A.wi; char* ws = A.ws;
  const int tid = threadIdx.x, lane = tid & 63;
  const int wave = __builtin_amdgcn_readfirstlane(tid >> 6);
  const int b = blockIdx.x;
  const int xcd = b & 7, idx = b >> 3;
  const int rt = p1_rt(b), ct = p1_ct(b);
  (void)xcd;
  const bool dft = ct >= 12, iskv = ct >= 4 && ct < 12;
  const int c0 = iskv ? 80 * (ct - 4) : 160 * (ct & 3);
  const int L0 = c0 >> 6, p0 = (c0 & 63) >> 4;
  const int xs = (p0 == 0 || p0 == 3) ? 1 : 2;
  const int bigB = p0 >= 2 ? 1 : 0;

  K1Ctx c;
  c.wave = wave;
  c.ra = mk_rsrc(x, 2048 * 1280);
  c.rb = dft ? mk_rsrc((const char*)&FT + (ct & 3) * 102400, 102400) : mk_rsrc(wqkv, 640 * 3840);
  c.rs = 3840;
  auto grow = [&](int row) { return dft ? 32 * (4 * rt + 2 * ((row >> 3) & 1) + ((row >> 4) & 1)) + 8 * (row >> 5) + (row & 7) : 128 * rt + row; };
#pragma unroll
  for (int i = 0; i < 2; ++i) {
    const int row = 16 * wave + 8 * i + (lane >> 3);
    c.va[i] = grow(row) * 1280 + ((lane & 7) ^ ((row >> 1) & 7)) * 16;
  }
  c.ldsa = (unsigned)(size_t)sm + 2 * wave * 1024;
  c.st = (((((idx & 7) * 5 + (idx >> 3) * 10) >> 2) % 10) * K1_ROT / 10) * (10 / K1_ROT);
  k1_issue1<0, 0>(c); k1_issue1<0, 1>(c);
  SCHED();
  {
    const int row = lane >> 3, hf = lane & 1;
    const int pair = ((lane & 7) >> 1) ^ (((row >> 1) & 1) | ((wave & 1) << 1));
    const unsigned rowb = (8 * wave + row) * c.rs;
    const bool kown = bigB ? (pair >= p0) : (pair <= p0);
    c.vb[0] = rowb + (iskv ? 1280 + (L0 + bigB) * 128 : L0 * 128) + pair * 32 + hf * 16;
    c.vb[1] = rowb + (iskv ? 2560 + (L0 + bigB) * 128 + (hf ^ 1) * 16 : (L0 + 1) * 128 + hf * 16) + pair * 32;
    c.vb[2] = rowb + (iskv ? (L0 + 1 - bigB) * 128 + (kown ? 1280 + pair * 32 + hf * 16 : 2560 + (pair ^ xs) * 32 + (hf ^ 1) * 16)
                           : (L0 + 2) * 128 + pair * 32 + hf * 16);
  }
  c.ldsb = (unsigned)(size_t)sm + 16384 + (dft ? 1 : 3) * wave * 1024;
  if (dft) { c.vb[0] = wave * 1024 + lane * 16; c.vb[1] = wave < 2 ? c.vb[0] + 8192 : 0x7fffffffu; }
  if (dft) { kd_issue1<0, 2>(c); kd_issue1<0, 3>(c); }
  else { k1_issue1<0, 2>(c); k1_issue1<0, 3>(c); k1_issue1<0, 4>(c); }
  c.st = (c.st == 9) ? 0 : c.st + 1;
  BAR();
  if (dft) { kd_issue<1>(c); kd_issue<2>(c); }
  else k1_issue<1>(c);

  const int wm = wave >> 1, wn = wave & 1;
  const int r = lane & 15, g = lane >> 4, q = (lane & 15) >> 2, p = lane & 3;
  const int hsw = ((q >> 1) & 1) | ((g & 1) << 1);
  const unsigned bbase = 16384 + g * 3072 + q * 128;
  c.ao[0][0] = (32 * wm + r) * 128 + ((g ^ (r >> 1)) * 16);
  c.ao[0][1] = (32 * wm + r) * 128 + (((4 + g) ^ (r >> 1)) * 16);
  f32x4 acc[2][5];
  Frags F0, F1;

#define K1_HEAD(SWAP, HOOK, W2)                                                                          \
  _Pragma("unroll") for (int u = 1; u < 3; ++u) {                                              \
    _Pragma("unroll") for (int k = 0; k < 2; ++k) { c.ao[u][k] = c.ao[0][k] + u * K1_STAGE; asm volatile("" : "+v"(c.ao[u][k])); }     \
    _Pragma("unroll") for (int k = 0; k < 5; ++k) { c.bo[u][k] = c.bo[0][k] + u * K1_STAGE; asm volatile("" : "+v"(c.bo[u][k])); }     \
  }                                                                                            \
  _Pragma("unroll") for (int mt = 0; mt < 2; ++mt)                                             \
    _Pragma("unroll") for (int nt = 0; nt < 5; ++nt) asm volatile("" : "+v"(acc[mt][nt]));     \
  TS(1);                                                                                       \
  WAIT_V(5); k1_step<SWAP, 0>(acc, F0, F1, sm, c);                                           \
  TS(2);                                                                                       \
  WAIT_V(5); k1_step<SWAP, 1>(acc, F1, F0, sm, c); HOOK;                                             \
  WAIT_V(W2); k1_step<SWAP, 2>(acc, F0, F1, sm, c);

  if (iskv) {
#pragma unroll
    for (int nt = 0; nt < 5; ++nt) {
      const int u = 4 * nt + p;
      const int isv = u >= 10 ? 1 : 0;
      const int pc = 4 * p0 + 10 * wn + u - 10 * isv;
      const int gp = pc >> 2, big = ((gp >> 2) == bigB);
      const int slot = (gp & 3) ^ ((!big && isv) ? xs : 0);
      c.bo[0][nt] = bbase + (big ? isv : 2) * 1024 + ((slot ^ hsw) * 32) + (((pc & 3) ^ (isv << 1)) * 8);
      const float i0 = ((nt < 2) || (nt == 2 && r < 8)) ? kEps : 0.f;
#pragma unroll
      for (int mt = 0; mt < 2; ++mt) acc[mt][nt] = (f32x4){i0, i0, i0, i0};
    }
    unsigned ko[8];
    constexpr int TI[8] = {0, 0, 1, 1, 1, 2, 2, 2}, TJ[8] = {2, 3, 2, 3, 4, 3, 4, 2};
#pragma unroll
    for (int i = 0; i < 8; ++i) {
      const int ti = TI[i], tj = TJ[i];
      const int ur = 16 * ti + r, hk = ur >= 20 ? 1 : 0;
      const int uc = 16 * tj + 4 * g - 40, hv = uc >= 20 ? 1 : 0;
      const bool isz = tj == 2 && g == 0;
      const bool valid = ur < 40 && (isz || (uc >= 0 && hk == hv));
      const int off = wave * 3840 + hk * 1920 + (ur - 20 * hk) * 96 + (isz ? 80 : (uc - 20 * hv) * 4);
      ko[i] = K1_STAGE + (valid ? off : 8 * 3840 + lane * 16);
      asm volatile("" : "+v"(ko[i]));
    }
    K1_HEAD(false, , 5)
    WAIT_V(5); k1_step<false, 3>(acc, F1, F0, sm, c);
    WAIT_V(5); k1_step<false, 4>(acc, F0, F1, sm, c);
    WAIT_V(5); k1_step<false, 5>(acc, F1, F0, sm, c);
    WAIT_V(5); k1_step<false, 6>(acc, F0, F1, sm, c);
    WAIT_V(5); k1_step<false, 7>(acc, F1, F0, sm, c);
    WAIT_V(5); k1_step<false, 8>(acc, F0, F1, sm, c);
    WAIT_V(0); k1_step<false, 9>(acc, F1, F0, sm, c);
    k1_mma<false, 0, 20>(acc, F1);
    TS(3);
    bf16x8 fr[5];
#pragma unroll
    for (int nt = 0; nt < 5; ++nt) {
#pragma unroll
      for (int m = 0; m < 2; ++m)
#pragma unroll
        for (int j = 0; j < 4; ++j) {
          if (nt < 2) acc[m][nt][j] = phi_f(acc[m][nt][j]);
          if (nt == 2) { const float ph = phi_f(acc[m][nt][j]); acc[m][nt][j] = (r < 8) ? ph : acc[m][nt][j]; }
        }
      fr[nt] = pack8(acc[0][nt], acc[1][nt]);
    }
    const bf16x8 ones = {0x3f80, 0x3f80, 0x3f80, 0x3f80, 0x3f80, 0x3f80, 0x3f80, 0x3f80};
    {
      const f32x4 zz = {0.f, 0.f, 0.f, 0.f};
      const bf16x8 v2 = (r < 8) ? ones : fr[2];
      f32x4 d[8];
#pragma unroll
      for (int i = 0; i < 8; ++i) d[i] = MFMA16(TJ[i] == 2 ? v2 : fr[TJ[i]], fr[TI[i]], zz);
#pragma unroll
      for (int i = 0; i < 8; ++i) *(LDS_AS f32x4*)(sm + ko[i]) = d[i];
    }
    WAIT_L0();
    BAR();
    const rsrc_t rk = mk_rsrc(ws + WS_KVP + ((size_t)rt * 32 + 4 * (ct - 4)) * 1920, 7680);
    LDS_AS f32x4* src = (LDS_AS f32x4*)(sm + K1_STAGE);
    const f32x4 s0 = (src[tid] + src[tid + 480]) + (src[tid + 960] + src[tid + 1440]);
    ST16(__builtin_bit_cast(u32x4, s0), rk, tid * 16, 0);
    if (wave >= 6) {
      LDS_AS float* zs = (LDS_AS float*)(sm + K1_STAGE) + min(tid - 384, 79) * 24 + 20;
      const float z = (zs[0] + zs[1920]) + (zs[3840] + zs[5760]);
      __builtin_amdgcn_raw_buffer_store_b32(__float_as_uint(z), mk_rsrc(ws + WS_ZP + rt * 2560 + (ct - 4) * 320, 320), (tid - 384) * 4, 0, ST_AUX);
    }
  } else {
    const int un = (dft ? 64 : 0) + rt * 4 + (ct & 3), mo = un >> 1, fh = un & 1;
    auto filt_load = [&]() {
      const rsrc_t rf = mk_rsrc((wave >> 2) ? wi : wr, 2048 * 642);
      const int kb = 16 * (mo >> 4) + 8 * (mo & 1) + 64 * (16 * ((mo >> 3) & 1) + 4 * ((mo >> 1) & 3) + (wave & 3));
      const unsigned v0 = lane < 21 ? lane * 16 : (lane >= 25 && lane < 46) ? 640 + (lane - 25) * 16 : 0x7fffffffu;
#pragma unroll
      for (int i = 0; i < 4; ++i)
        dma16_asm(rf, (unsigned)(size_t)sm + K1_WF + (wave * 4 + i) * 1056, v0, kb * 642 + 320 * fh + i * 1280);
    };
    auto filt_store = [&]() {
      const rsrc_t rw = mk_rsrc(ws + WS_WT + (size_t)fh * 160 * 8192 + mo * 128, (160 + fh) * 8192 - mo * 128);
      const int i8 = lane & 7;
#pragma unroll
      for (int rd = 0; rd < 3; ++rd) {
        const int fl = (rd * 8 + wave) * 8 + (lane >> 3);
        lptr src = sm + K1_WF + (i8 >> 1) * 1056 + (i8 & 1) * 400 + i8 * 2 + min(fl, 160) * 2;
        u32x4 o;
#pragma unroll
        for (int j = 0; j < 4; ++j) o[j] = (unsigned)*(LDS_AS u16*)(src + j * 4224) | ((unsigned)*(LDS_AS u16*)(src + 16896 + j * 4224) << 16);
        ST16(o, rw, fl * 8192 + i8 * 16, 0);
      }
      SCHED();
    };
    if (!dft) {
#pragma unroll
      for (int nt = 0; nt < 5; ++nt) {
        const int gp = p0 + 5 * wn + nt;
        c.bo[0][nt] = bbase + (gp >> 2) * 1024 + (((gp & 3) ^ hsw) * 32) + p * 8;
      }
#pragma unroll
      for (int mt = 0; mt < 2; ++mt)
#pragma unroll
        for (int nt = 0; nt < 5; ++nt) acc[mt][nt] = (f32x4){kEps, kEps, kEps, kEps};
      K1_HEAD(true, , 5)
      WAIT_V(5); k1_step<true, 3>(acc, F1, F0, sm, c);
      filt_load();
      WAIT_V(9); k1_step<true, 4>(acc, F0, F1, sm, c);
      WAIT_V(9); k1_step<true, 5>(acc, F1, F0, sm, c);
      WAIT_V(5); k1_step<true, 6>(acc, F0, F1, sm, c);
      filt_store();
      WAIT_V(8); k1_step<true, 7>(acc, F1, F0, sm, c);
      WAIT_V(8); k1_step<true, 8>(acc, F0, F1, sm, c);
      WAIT_V(0); k1_step<true, 9>(acc, F1, F0, sm, c);
      if (wave == 0) flag_store(ws, 1, e, lane);
      k1_mma<true, 0, 20>(acc, F1);
      TS(3);
#pragma unroll
      for (int mt = 0; mt < 2; ++mt)
#pragma unroll
        for (int nt = 0; nt < 5; ++nt)
#pragma unroll
          for (int j = 0; j < 4; ++j) pq[mt][nt][j] = phi_f(acc[mt][nt][j]);
    } else {
      if (wave < 4) kd_main<3>(ws, sm, c, rt, ct & 3, e, lane, filt_load, filt_store TS_PASS);
      else kd_main<2>(ws, sm, c, rt, ct & 3, e, lane, filt_load, filt_store TS_PASS);
    }
  }
  TS(4);
}

DI void publish(char* ws, int which, unsigned e) {
  WAIT_V(0);
  BAR();
  if (threadIdx.x < 64) flag_store(ws, which, e, threadIdx.x);
}
DI bool poll(char* ws, int which, unsigned e, bool n0, bool n1, int lane, unsigned tries = 1u << 17) {
  const rsrc_t rf = mk_rsrc(ws + WS_SYN + which * 65536 + (blockIdx.x & 31) * 2048, 2048);
  const u32x2 t = make_tag(e);
  for (unsigned spin = 0; spin < tries; ++spin) {
    const u32x4 f0 = __builtin_amdgcn_raw_buffer_load_b128(rf, n0 ? lane * 16 : 0x7fffffffu, 0, 16);
    const u32x4 f1 = __builtin_amdgcn_raw_buffer_load_b128(rf, n1 ? lane * 16 + 1024 : 0x7fffffffu, 0, 16);
    n0 = n0 && !(f0[0] == t[0] && f0[1] == t[1] && f0[2] == t[0] && f0[3] == t[1]);
    n1 = n1 && !(f1[0] == t[0] && f1[1] == t[1] && f1[2] == t[0] && f1[3] == t[1]);
    if (!__any(n0 || n1)) return true;
    __builtin_amdgcn_s_sleep(POLL_SLEEP);
  }
  return false;
}
DI void await(char* ws, int which, unsigned e, int need, int wave, int lane) {
  if (wave == 7) poll(ws, which, e, (need >> (lane < 32 ? 0 : 1)) & 1, (need >> (lane < 32 ? 1 : 2)) & 1, lane);
  asm volatile("" ::: "memory");
  BAR();
  asm volatile("" ::: "memory");
}
DI void dma16_sc1(rsrc_t rs, unsigned lds, unsigned vo, int so) {
  asm volatile("s_mov_b32 m0, %2\n\ts_nop 0\n\tbuffer_load_dwordx4 %0, %1, %3 offen sc1 lds" :: "v"(vo), "s"(rs), "s"(lds), "s"(so));
}
#define LD16(rs, vo, so) __builtin_amdgcn_raw_buffer_load_b128((rs), (vo), (so), LD_AUX)

constexpr int P3_HALF = 71680;

struct FftC {
  const u32x4 (&fre)[4]; const u32x4 (&fim)[4]; const u32x4 (&fc)[2]; const u32x4 (&fs)[2]; const u32x4 (&fn)[2];
  const f32x2 (&tc)[2][2]; const f32x2 (&tsn)[2][2]; const unsigned (&uo)[2][4]; const unsigned (&ro)[4]; unsigned vy;
};
DI f32x2 half2(const f32x4& v, int h) { return h ? (f32x2){v[2], v[3]} : (f32x2){v[0], v[1]}; }
constexpr int FSECOND = 304;
template <int NC> DI void fft_run(char* ws, lptr sm, const FftC& T, const u32x4 (&wv)[2][2], int fu0, int wave8, int lane) {
  const f32x4 z4 = {0.f, 0.f, 0.f, 0.f};
  f32x2 wre[NC][2][2], wim[NC][2][2];
#pragma unroll
  for (int c = 0; c < NC; ++c) {
    const int f = min((c ? FSECOND + (fu0 >> 1) : fu0), 320);
    const float sc = ((f == 0 || f == 320) ? 1.f : 2.f) / (2048.f * 640.f);
#pragma unroll
    for (int t = 0; t < 2; ++t)
#pragma unroll
      for (int h = 0; h < 2; ++h) {
        wre[c][t][h] = (f32x2){bf_lo(wv[c][t][2 * h]), bf_lo(wv[c][t][2 * h + 1])} * sc;
        wim[c][t][h] = (f32x2){bf_hi(wv[c][t][2 * h]), bf_hi(wv[c][t][2 * h + 1])} * sc;
      }
  }
#define FOR_C _Pragma("unroll") for (int c = 0; c < NC; ++c)
#define FOR_T _Pragma("unroll") for (int t = 0; t < 2; ++t)
#define FOR_H _Pragma("unroll") for (int h = 0; h < 2; ++h)
#define FOR_K _Pragma("unroll") for (int ks = 0; ks < 4; ++ks)
  f32x4 re[NC][2], im[NC][2];
  u32x4 bre[NC], bim[NC];
  {
    bf16x8 xa[NC][2][4];
    FOR_K FOR_C FOR_T xa[c][t][ks] = *(LDS_AS bf16x8*)(sm + c * 16384 + lane * 16 + (t * 4 + ks) * 1024);
    FOR_K FOR_C FOR_T {
      re[c][t] = MFMA16(xa[c][t][ks], BC(T.fre[ks]), ks ? re[c][t] : z4);
      im[c][t] = MFMA16(xa[c][t][ks], BC(T.fim[ks]), ks ? im[c][t] : z4);
    }
  }
  FOR_C FOR_T FOR_H {
    const f32x2 a = half2(re[c][t], h), d = half2(im[c][t], h);
    const f32x2 x = a * T.tc[t][h] + d * T.tsn[t][h], y = d * T.tc[t][h] - a * T.tsn[t][h];
    bre[c][2 * t + h] = pk2(x[0], x[1]); bim[c][2 * t + h] = pk2(y[0], y[1]);
  }
  FOR_C FOR_T { re[c][t] = MFMA16(BC(T.fc[t]), BC(bre[c]), z4); im[c][t] = MFMA16(BC(T.fc[t]), BC(bim[c]), z4); }
  FOR_C FOR_T { re[c][t] = MFMA16(BC(T.fs[t]), BC(bim[c]), re[c][t]); im[c][t] = MFMA16(BC(T.fn[t]), BC(bre[c]), im[c][t]); }
  FOR_C FOR_T FOR_H {
    const f32x2 a = half2(re[c][t], h), d = half2(im[c][t], h);
    const f32x2 x = a * wre[c][t][h] - d * wim[c][t][h], y = a * wim[c][t][h] + d * wre[c][t][h];
    bre[c][2 * t + h] = pk2(x[0], x[1]); bim[c][2 * t + h] = pk2(y[0], y[1]);
  }
  FOR_C FOR_T { re[c][t] = MFMA16(BC(T.fc[t]), BC(bre[c]), z4); im[c][t] = MFMA16(BC(T.fc[t]), BC(bim[c]), z4); }
  FOR_C FOR_T { re[c][t] = MFMA16(BC(T.fn[t]), BC(bim[c]), re[c][t]); im[c][t] = MFMA16(BC(T.fs[t]), BC(bre[c]), im[c][t]); }
  FOR_C FOR_T FOR_H {
    const f32x2 a = half2(re[c][t], h), d = half2(im[c][t], h);
    const f32x2 x = a * T.tc[t][h] - d * T.tsn[t][h], y = -(d * T.tc[t][h]) - a * T.tsn[t][h];
    *(LDS_AS unsigned*)(sm + c * 16384 + T.uo[t][2 * h]) = pk2(x[0], y[0]);
    *(LDS_AS unsigned*)(sm + c * 16384 + T.uo[t][2 * h + 1]) = pk2(x[1], y[1]);
  }
  WAIT_L0();
  BAR();
  SCHED();
  {
    bf16x8 ua[NC][2][4];
    FOR_K FOR_C FOR_T ua[c][t][ks] = *(LDS_AS bf16x8*)(sm + c * 16384 + T.ro[ks] + t * 4096);
    FOR_K FOR_C FOR_T {
      re[c][t] = MFMA16(ua[c][t][ks], BC(T.fre[ks]), ks ? re[c][t] : z4);
      im[c][t] = MFMA16(ua[c][t][ks], BC(T.fim[ks]), ks ? im[c][t] : z4);
    }
  }
  FOR_C {
    const int fu = (c ? FSECOND + (fu0 >> 1) : fu0), f = min(fu, 320);
    const rsrc_t ry = mk_rsrc(ws + WS_Y + (size_t)f * 4096, fu > 320 ? 0 : ((f == 0 || f == 320) ? 4096 : 321 * 4096));
    ST16(widen((u32x2){pk2(re[c][0][0], re[c][0][1]), pk2(re[c][0][2], re[c][0][3])}, (u32x2){pk2(re[c][1][0], re[c][1][1]), pk2(re[c][1][2], re[c][1][3])}), ry, T.vy, 0);
    ST16(widen((u32x2){pk2(im[c][0][0], im[c][0][1]), pk2(im[c][0][2], im[c][0][3])}, (u32x2){pk2(im[c][1][0], im[c][1][1]), pk2(im[c][1][2], im[c][1][3])}), ry, T.vy, 320 * 4096);
  }
}

DI void phase2(const Args& A, lptr sm0, unsigned e, const f32x4 (&pq)[2][5] TS_ARG) {
  char* ws = A.ws;
  const int lane = threadIdx.x & 63;
  const int wave8 = __builtin_amdgcn_readfirstlane(threadIdx.x >> 6);
  const int wave = wave8 & 3, team = wave8 >> 2;
  lptr sm = sm0 + team * P3_HALF;
  const unsigned smu = (unsigned)(size_t)sm;
  const int b = blockIdx.x;
  const int rt1 = p1_rt(b), ct1 = p1_ct(b);
  const bool iskv = ct1 >= 4 && ct1 < 12;
  const int v = (ct1 - 4) * 16 + rt1;
  const int r = lane & 15, g = lane >> 4;
  const f32x4 z4 = {0.f, 0.f, 0.f, 0.f};
  const bool isfft = ct1 >= 12 || (iskv && v < 88);
  publish(ws, 1, e);
  TS(5);
  if (isfft) {
    const int fu0 = (iskv ? 128 + 2 * v : 2 * (rt1 * 4 + (ct1 & 3))) + team;
    const bool two = fu0 < 34 && team == 0;
    u32x4 wv[2][2];
    auto loads = [&]() {
#pragma unroll
      for (int c = 0; c < 2; ++c) {
        const int f = min(c ? FSECOND + (fu0 >> 1) : fu0, 320);
        const rsrc_t rx = mk_rsrc(ws + WS_X1 + (size_t)f * 8192, (c && !two) ? 0 : 8192), rw = mk_rsrc(ws + WS_WT + (size_t)f * 8192, (c && !two) ? 0 : 8192);
        dma16_sc1(rx, smu + c * 16384 + wave * 2048, lane * 16, wave * 2048);
        dma16_sc1(rx, smu + c * 16384 + wave * 2048 + 1024, lane * 16, wave * 2048 + 1024);
        wv[c][0] = LD16(rw, lane * 16, wave * 2048);
        wv[c][1] = LD16(rw, lane * 16 + 1024, wave * 2048);
      }
      SCHED();
    };
    await(ws, 1, e, 5, wave8, lane);
    TS(7);
    loads();
    u32x4 fre[4], fim[4], fc[2], fs[2], fn[2];
    f32x2 tc[2][2], tsn[2][2];
    unsigned uo[2][4], ro[4];
    {
      const float a64 = (float)(16 * wave + r) * (1.f / 64.f);
      const unsigned m2 = (r & 2) ? 0x80008000u : 0u, m1 = (r & 1) ? 0x80008000u : 0u;
      const unsigned m3 = ((r + 1) & 2) ? 0x80008000u : 0u;
#pragma unroll
      for (int j = 0; j < 4; ++j) {
        const float ang = a64 * (float)(4 * g + j);
        const float c = __builtin_amdgcn_cosf(ang), s = __builtin_amdgcn_sinf(ang);
        const unsigned p = pk2(c, s), q = pk2(-s, c);
        fre[0][j] = p; fim[0][j] = q;
        fre[1][j] = ((r & 1) ? q : p) ^ m2;
        fim[1][j] = ((r & 1) ? p : q) ^ m3;
        fre[2][j] = p ^ m1; fim[2][j] = q ^ m1;
        fre[3][j] = fre[1][j] ^ m1; fim[3][j] = fim[1][j] ^ m1;
      }
      const float a32 = (float)r * (1.f / 32.f);
#pragma unroll
      for (int j = 0; j < 2; ++j) {
        const float a0 = a32 * (float)(4 * g + 2 * j), a1 = a0 + a32;
        const unsigned c = pk2(__builtin_amdgcn_cosf(a0), __builtin_amdgcn_cosf(a1)), s = pk2(__builtin_amdgcn_sinf(a0), __builtin_amdgcn_sinf(a1));
        fc[0][j] = c; fc[0][2 + j] = c ^ m1; fs[0][j] = s; fs[0][2 + j] = s ^ m1;
      }
#pragma unroll
      for (int j = 0; j < 4; ++j) {
        fc[1][j] = fc[0][j] ^ 0x80000000u; fs[1][j] = fs[0][j] ^ 0x80000000u;
        fn[0][j] = fs[0][j] ^ 0x80008000u; fn[1][j] = fs[0][j] ^ 0x00008000u;
        ro[j] = 8192 + r * 256 + (((4 * j + g) ^ r) * 16);
      }
#pragma unroll
      for (int t = 0; t < 2; ++t)
#pragma unroll
        for (int j = 0; j < 4; ++j) {
          const int s2 = 16 * t + 4 * g + j;
          const float a = a64 * (1.f / 32.f) * (float)s2;
          tc[t][j >> 1][j & 1] = __builtin_amdgcn_cosf(a);
          tsn[t][j >> 1][j & 1] = __builtin_amdgcn_sinf(a);
          uo[t][j] = 8192 + s2 * 256 + (((4 * wave + (r >> 2)) ^ (s2 & 15)) * 16) + (r & 3) * 4;
        }
    }
    unsigned vy = wave * 1024 + r * 64 + (g & 1) * 32 + (g >> 1) * 16;
#pragma unroll
    for (int ks = 0; ks < 4; ++ks) { asm volatile("" : "+v"(fre[ks]), "+v"(fim[ks]), "+v"(ro[ks])); }
#pragma unroll
    for (int t = 0; t < 2; ++t) {
      asm volatile("" : "+v"(fc[t]), "+v"(fs[t]), "+v"(fn[t]), "+v"(tc[t][0]), "+v"(tc[t][1]), "+v"(tsn[t][0]), "+v"(tsn[t][1]));
      asm volatile("" : "+v"(uo[t][0]), "+v"(uo[t][1]), "+v"(uo[t][2]), "+v"(uo[t][3]), "+v"(vy));
    }
    SCHED();
    TS(6);
    FftC T = {fre, fim, fc, fs, fn, tc, tsn, uo, ro, vy};
    WAIT_V(0);
    asm volatile("" : "+v"(wv[0][0]), "+v"(wv[0][1]), "+v"(wv[1][0]), "+v"(wv[1][1]));
    BAR();
    SCHED();
    TS(8);
    if (two) fft_run<2>(ws, sm, T, wv, fu0, wave8, lane);
    else fft_run<1>(ws, sm, T, wv, fu0, wave8, lane);
  } else if (iskv) {
    const int ks = (v - 88) >> 1, cp = 2 * ((v - 88) & 1) + team;
    const int hf = (32 * ks) / 20, e0 = 20 * hf;
    const int q = r >> 2, p = lane & 3;
    const int hsw = ((q >> 1) & 1) | ((g & 1) << 1);
    const int L1 = (160 * cp) >> 7, L2 = (1120 - 160 * cp) >> 7;
    lptr wb = sm;
    {
      const rsrc_t rwo = mk_rsrc(A.wo, 640 * 1280);
      const int line = (wave < 2 ? L1 : L2) + (wave & 1), row = lane >> 3;
#pragma unroll
      for (int kb = 0; kb < 8; ++kb) {
        const int pair = ((lane & 7) >> 1) ^ (((row >> 1) & 1) | ((kb & 1) << 1));
        dma16_asm(rwo, smu + (wave * 8 + kb) * 1024, min(e0 + 8 * kb + row, 639) * 1280 + line * 128 + pair * 32 + (lane & 1) * 16, 0);
      }
    }
    lptr ab = sm0 + 32768;
    if (threadIdx.x < 288) *(LDS_AS u32x4*)(ab + threadIdx.x * 16) = (u32x4){0u, 0u, 0u, 0u};
    const int t5 = min((int)threadIdx.x >> 1, 159), kl = t5 / 5, qd = t5 - kl * 5;
    const int k = 32 * ks + kl, h = k / 20, d = k - 20 * h;
    unsigned ko = h * 1920 + d * 96 + qd * 16 + (threadIdx.x & 1) * 8 * 61440, ao = kl * 144 + (20 * (h - hf) + 4 * qd) * 2;
    unsigned bo[3];
#pragma unroll
    for (int n = 0; n < 3; ++n) {
      const int tl = min(wave + 4 * n, 9);
      const int ctl = tl >= 5 ? 1 : 0, nt = tl - 5 * ctl, ctg = 2 * cp + ctl;
      const int i = 16 * nt + 4 * p;
      const int byte = 2 * (i < 40 ? 40 * ctg + i : 560 - 40 * ctg + i);
      const int pl = i < 40 ? (byte >> 7) - L1 : 2 + (byte >> 7) - L2;
      bo[n] = (pl * 8 + g) * 1024 + q * 128 + ((((byte >> 5) & 3) ^ hsw) * 32) + (byte & 31);
      asm volatile("" : "+v"(bo[n]));
    }
    asm volatile("" : "+v"(ko), "+v"(ao));
    WAIT_V(0);
    WAIT_L0();
    TS(6);
    await(ws, 1, e, 2, wave8, lane);
    TS(7);
    f32x4 pv[8];
    const rsrc_t rk = mk_rsrc(ws + WS_KVP, wave8 < 5 ? 983040 : 0);
#pragma unroll
    for (int i = 0; i < 8; ++i) pv[i] = __builtin_bit_cast(f32x4, LD16(rk, ko, i * 61440));
    SCHED();
    bf16x8 bw[3][2];
#pragma unroll
    for (int n = 0; n < 3; ++n)
#pragma unroll
      for (int s = 0; s < 2; ++s) {
        const s16x4 lo = TRREAD(wb + bo[n] + s * 4096), hi = TRREAD(wb + bo[n] + s * 4096 + 512);
        bw[n][s] = __builtin_shufflevector(lo, hi, 0, 1, 2, 3, 4, 5, 6, 7);
      }
    SCHED();
    f32x4 sum = ((pv[0] + pv[1]) + (pv[2] + pv[3])) + ((pv[4] + pv[5]) + (pv[6] + pv[7]));
#pragma unroll
    for (int j = 0; j < 4; ++j) sum[j] += __uint_as_float(__builtin_amdgcn_update_dpp(0u, __float_as_uint(sum[j]), 0xB1, 0xf, 0xf, false));
    if (threadIdx.x < 320 && !(threadIdx.x & 1)) *(LDS_AS u32x2*)(ab + ao) = (u32x2){pk2(sum[0], sum[1]), pk2(sum[2], sum[3])};
    WAIT_L0();
    BAR();
    TS(8);
    bf16x8 a[2][2];
#pragma unroll
    for (int t = 0; t < 2; ++t)
#pragma unroll
      for (int s = 0; s < 2; ++s) a[t][s] = *(LDS_AS bf16x8*)(ab + (8 * (r >> 2) + (r & 3) + 4 * t) * 144 + s * 64 + g * 16);
    const rsrc_t rm = mk_rsrc(ws + WS_M + (size_t)(10 * cp) * 20480 + ks * 1024, 9 * 20480 + 1024);
#pragma unroll
    for (int n = 0; n < 3; ++n) {
      f32x4 d1 = MFMA16(a[0][0], bw[n][0], z4), d2 = MFMA16(a[1][0], bw[n][0], z4);
      d1 = MFMA16(a[0][1], bw[n][1], d1); d2 = MFMA16(a[1][1], bw[n][1], d2);
      ST16(__builtin_bit_cast(u32x4, pack8(d1, d2)), rm, lane * 16, (wave + 4 * n) * 20480);
    }
  } else if (ct1 < 4) {
    const int t8 = threadIdx.x, wm = wave8 >> 1, wn = wave8 & 1;
    const int gb = rt1 * 4 + ct1;
    pf_asm(mk_rsrc(&GT, 491520), (unsigned)(size_t)sm0 + K1_JUNK, t8 < 60 ? (gb * 60 + t8) * 128 : 0x7fffffffu, 0);
      const rsrc_t rx = mk_rsrc(A.x + (size_t)gb * 32 * 640, 32 * 1280), rg = mk_rsrc(A.gw, 1280);
    u32x4 gx[5], gg[5];
#pragma unroll
      for (int i = 0; i < 5; ++i) {
        gx[i] = __builtin_amdgcn_raw_buffer_load_b128(rx, t8 * 80 + i * 16, 0, 0);
        gg[i] = __builtin_amdgcn_raw_buffer_load_b128(rg, (t8 & 15) * 80 + i * 16, 0, 0);
      }
    u32x2 pk[2][5];
    unsigned zm[5];
#pragma unroll
    for (int nt = 0; nt < 5; ++nt) {
#pragma unroll
      for (int mt = 0; mt < 2; ++mt) {
        pk[mt][nt] = (u32x2){pk2(pq[mt][nt][0], pq[mt][nt][1]), pk2(pq[mt][nt][2], pq[mt][nt][3])};
        asm volatile("" : "+v"(pk[mt][nt]));
      }
      zm[nt] = ((4 * nt + g) / 5 == (r & 3)) ? 0xffffffffu : 0u;
      asm volatile("" : "+v"(zm[nt]));
    }
    const rsrc_t rz = mk_rsrc(ws + WS_ZP + ct1 * 640, 40960);
    const rsrc_t rp = mk_rsrc(ws + WS_P + (size_t)(8 * rt1 + 2 * wm) * 20480 + (20 * ct1 + 10 * wn) * 256, 2 * 20480);
    const unsigned vo = (g & 1) * 20480 + (g >> 1) * 256 + r * 16;
    SCHED();
    TS(6);
    await(ws, 1, e, 2, wave8, lane);
    TS(7);
    if (wave8 < 3) {
      float zv[16];
#pragma unroll
      for (int i = 0; i < 16; ++i) zv[i] = __uint_as_float(__builtin_amdgcn_raw_buffer_load_b32(rz, min(t8, 159) * 4, i * 2560, LD_AUX));
#pragma unroll
      for (int i = 0; i < 8; ++i) zv[i] += zv[i + 8];
      const float zs = ((zv[0] + zv[1]) + (zv[2] + zv[3])) + ((zv[4] + zv[5]) + (zv[6] + zv[7]));
      *(LDS_AS u16*)(sm0 + t8 * 2) = (u16)pk2(zs, zs);
    }
    WAIT_L0();
    BAR();
    TS(8);
    u32x2 zq[6];
#pragma unroll
    for (int nt = 0; nt < 5; ++nt) { zq[nt] = *(LDS_AS u32x2*)(sm0 + wn * 160 + nt * 32 + g * 8); zq[nt][0] &= zm[nt]; zq[nt][1] &= zm[nt]; }
    zq[5] = (u32x2){0u, 0u};
    u32x2 po[2][5];
#pragma unroll
    for (int mt = 0; mt < 2; ++mt) {
      f32x4 den = {kEps, kEps, kEps, kEps};
#pragma unroll
      for (int m = 0; m < 3; ++m) {
        const u32x4 za = {zq[2 * m][0], zq[2 * m][1], zq[2 * m + 1][0], zq[2 * m + 1][1]};
        const u32x2 p1 = m < 2 ? pk[mt][2 * m + 1] : (u32x2){0u, 0u};
        const u32x4 pb = {pk[mt][2 * m][0], pk[mt][2 * m][1], p1[0], p1[1]};
        den = MFMA16(BC(za), BC(pb), den);
      }
      f32x4 inv;
#pragma unroll
      for (int j = 0; j < 4; ++j) inv[j] = __builtin_amdgcn_rcpf(den[j]);
#pragma unroll
      for (int nt = 0; nt < 5; ++nt) {
        const float iv = nt == 0 ? inv[0] : nt == 4 ? inv[3] : (g < nt ? inv[nt - 1] : inv[nt]);
        const f32x4 w = pq[mt][nt] * iv;
        po[mt][nt] = (u32x2){pk2(w[0], w[1]), pk2(w[2], w[3])};
      }
    }
#pragma unroll
    for (int nt = 0; nt < 5; ++nt) ST16(widen(po[0][nt], po[1][nt]), rp, vo + nt * 512, 0);
    SCHED();
    {
      float s0 = 0.f, s1 = 0.f;
#pragma unroll
      for (int i = 0; i < 5; ++i) { s0 = dot2(gx[i][0], gg[i][0], s0); s1 = dot2(gx[i][1], gg[i][1], s1); s0 = dot2(gx[i][2], gg[i][2], s0); s1 = dot2(gx[i][3], gg[i][3], s1); }
      float s = s0 + s1;
#pragma unroll
      for (int o = 8; o > 0; o >>= 1) s += __shfl_xor(s, o);
      if ((t8 & 15) == 0) __builtin_amdgcn_raw_buffer_store_b32(__float_as_uint(1.f / (1.f + __expf(-s))), mk_rsrc(ws + WS_G, 8192), (gb * 32 + (t8 >> 4)) * 4, 0, 16);
    }
  }
  TS(9);
}

constexpr int LDS_ALL = K1_LDS;
static_assert(81920 + (2 * 64 * 84 + 2 * 64 * 52 + 64) * 4 <= LDS_ALL, "lds");

DI void phase3(const Args& A, lptr sm, unsigned e TS_ARG) {
  char* ws = A.ws;
  const int tid = threadIdx.x, lane = tid & 63;
  const int wave = __builtin_amdgcn_readfirstlane(tid >> 6);
  const int b = blockIdx.x, xcd = b & 7, idx = b >> 3;
  const int rt = xcd * 4 + (idx & 3);
  const int ct = idx >> 2;
  const int r = lane & 15, g = lane >> 4;
  LDS_AS float* red = (LDS_AS float*)(sm + 81920);
  constexpr int RA = 64 * 84, RE = 64 * 52, RG = 2 * RA + 2 * RE;
  const bool rev = P3_REV && (((idx >> 3) ^ idx) & 1);
  const int k0 = rev ? 9 : 0, kd = rev ? -1 : 1;
  u32x4 fg[10][3];
  if (wave == 2 || wave == 3) {
    const rsrc_t rg = mk_rsrc((const char*)&GT + (size_t)(2 * ct + wave - 2) * 30720, 30720);
#pragma unroll
    for (int i = 0; i < 30; ++i) fg[i / 3][i % 3] = __builtin_amdgcn_raw_buffer_load_b128(rg, lane * 16 + (i % 3) * 1024, (k0 + kd * (i / 3)) * 3072, 0);
  }
  await(ws, 2, e, 7, wave, lane);
  TS(11);
  if (wave < 2) {
    const rsrc_t ra = mk_rsrc(ws + WS_P + (size_t)(4 * rt) * 20480 + wave * 10240, 4 * 20480);
    const rsrc_t rb = mk_rsrc(ws + WS_M + (size_t)(5 * ct) * 20480 + wave * 10240, 5 * 20480);
    const unsigned vo = lane * 16;
    f32x4 acc[4][5];
#pragma unroll
    for (int mt = 0; mt < 4; ++mt)
#pragma unroll
      for (int nt = 0; nt < 5; ++nt) acc[mt][nt] = (f32x4){0.f, 0.f, 0.f, 0.f};
    u32x4 fa[10][4], fb[10][5];
    constexpr int PF = P3_PF;
    auto ld = [&](int ks) {
#pragma unroll
      for (int nt = 0; nt < 5; ++nt) fb[ks][nt] = LD16(rb, vo, nt * 20480 + (k0 + kd * ks) * 1024);
#pragma unroll
      for (int mt = 0; mt < 4; ++mt) fa[ks][mt] = LD16(ra, vo, mt * 20480 + (k0 + kd * ks) * 1024);
    };
#pragma unroll
    for (int ks = 0; ks < PF; ++ks) { ld(ks); SCHED(); }
#pragma unroll
    for (int ks = 0; ks < 10; ++ks) {
#pragma unroll
      for (int mt = 0; mt < 4; ++mt)
#pragma unroll
        for (int nt = 0; nt < 5; ++nt) acc[mt][nt] = MFMA16(BC(fb[ks][nt]), BC(fa[ks][mt]), acc[mt][nt]);
      if (ks < 9) SCHED();
      if (ks + PF < 10) ld(ks + PF);
      if (ks < 9) SCHED();
    }
    TS(12);
#pragma unroll
    for (int mt = 0; mt < 4; ++mt)
#pragma unroll
      for (int nt = 0; nt < 5; ++nt) *(LDS_AS f32x4*)(red + wave * RA + (16 * mt + r) * 84 + 16 * nt + 4 * g) = acc[mt][nt];
  } else if (wave < 4) {
    const int od = wave - 2;
    lptr yb = sm + od * 40960;
    const unsigned ybu = (unsigned)(size_t)yb;
    const rsrc_t ry = mk_rsrc(ws + WS_Y + (size_t)od * 320 * 4096 + rt * 128, 320 * 4096);
    const int row = lane >> 3;
    const unsigned vy0 = row * 4096 + ((((lane & 7) >> 1) ^ ((row >> 1) & 1)) * 32) + (lane & 1) * 16;
    const unsigned vy1 = vy0 ^ 64;
    auto issue = [&](int ks) {
#pragma unroll
      for (int i = 0; i < 4; ++i) dma16_sc1(ry, ybu + (ks * 4 + i) * 1024, (i & 1) ? vy1 : vy0, ((k0 + kd * ks) * 4 + i) * 32768);
    };
#pragma unroll
    for (int i = 0; i < 30; ++i) asm volatile("" : "+v"(fg[i / 3][i % 3]));
    issue(0); issue(1); issue(2);
    if (P3_YW == 5) { issue(3); issue(4); }
    const int q = r >> 2, p = lane & 3;
    const int hsw = ((q >> 1) & 1) | ((g & 1) << 1);
    unsigned yo[4];
#pragma unroll
    for (int mt = 0; mt < 4; ++mt) yo[mt] = g * 1024 + q * 128 + ((mt ^ hsw) * 32) + p * 8;
    f32x4 acc[4][3];
#pragma unroll
    for (int mt = 0; mt < 4; ++mt)
#pragma unroll
      for (int nt = 0; nt < 3; ++nt) acc[mt][nt] = (f32x4){0.f, 0.f, 0.f, 0.f};
#define K3_STEP(ks, n)                                                                                         \
    {                                                                                                          \
      WAIT_V(n);                                                                                               \
      SCHED();                                                                                                 \
      bf16x8 fy[4];                                                                                            \
      _Pragma("unroll") for (int mt = 0; mt < 4; ++mt) {                                                       \
        const s16x4 lo = TRREAD(yb + yo[mt] + ks * 4096), hi = TRREAD(yb + yo[mt] + ks * 4096 + 512);          \
        fy[mt] = __builtin_shufflevector(lo, hi, 0, 1, 2, 3, 4, 5, 6, 7);                                      \
      }                                                                                                        \
      SCHED();                                                                                                 \
      if (ks + P3_YW < 10) issue(ks + P3_YW);                                                                          \
      SCHED();                                                                                                 \
      _Pragma("unroll") for (int mt = 0; mt < 4; ++mt)                                                         \
        _Pragma("unroll") for (int nt = 0; nt < 3; ++nt) acc[mt][nt] = MFMA16(BC(fg[ks][nt]), fy[mt], acc[mt][nt]);   \
      SCHED();                                                                                                 \
    }
    K3_STEP(0, 8) K3_STEP(1, 8) K3_STEP(2, 8) K3_STEP(3, 8) K3_STEP(4, 8)
    K3_STEP(5, 8) K3_STEP(6, 8) K3_STEP(7, 8) K3_STEP(8, 4) K3_STEP(9, 0)
    TS(12);
#pragma unroll
    for (int mt = 0; mt < 4; ++mt)
#pragma unroll
      for (int nt = 0; nt < 3; ++nt) *(LDS_AS f32x4*)(red + 2 * RA + od * RE + (16 * mt + r) * 52 + 16 * nt + 4 * g) = acc[mt][nt];
  } else {
    if (wave == 4) red[RG + lane] = __uint_as_float(__builtin_amdgcn_raw_buffer_load_b32(mk_rsrc(ws + WS_G + rt * 256, 256), lane * 4, 0, LD_AUX));
  }
  const rsrc_t ro = mk_rsrc(A.out + (size_t)(64 * rt) * 640, 64 * 1280);
  unsigned oa[2], oe[2], oq[2], og[2], or_[2];
#pragma unroll
  for (int i = 0; i < 2; ++i) {
    const int it = i ? 512 + wave * 16 + (lane & 15) : tid;
    const int row = it / 10, gr = it - row * 10;
    oa[i] = (row * 84 + 8 * gr) * 4;
    oe[i] = (2 * RA + row * 52) * 4;
    oq[i] = oe[i] + (gr < 5 ? 8 * gr : 72 - 8 * gr) * 4;
    og[i] = (i && lane >= 16) ? 0x7fffffffu : row * 1280 + (gr < 5 ? 80 * ct : 1120 - 80 * ct) + 16 * gr;
    or_[i] = (RG + row) * 4;
    oa[i] |= (gr < 5) ? 0u : 0x80000000u;
    asm volatile("" : "+v"(oa[i]), "+v"(oe[i]), "+v"(oq[i]), "+v"(og[i]), "+v"(or_[i]));
  }
  WAIT_L0();
  BAR();
  TS(13);
#pragma unroll
  for (int i = 0; i < 2; ++i) {
    const bool lo = (int)oa[i] >= 0;
    lptr rb = sm + 81920, pa = rb + (oa[i] & 0x7fffffffu), pq = rb + oq[i];
    const float gq = *(LDS_AS float*)(rb + or_[i]);
    const f32x4 a0 = *(LDS_AS f32x4*)pa + *(LDS_AS f32x4*)(pa + RA * 4);
    const f32x4 a1 = *(LDS_AS f32x4*)(pa + 16) + *(LDS_AS f32x4*)(pa + RA * 4 + 16);
    const float yn = *(LDS_AS float*)(rb + oe[i] + (RE + 41) * 4);
    const f32x4 e0 = *(LDS_AS f32x4*)pq, e1 = *(LDS_AS f32x4*)(pq + 16), o0 = *(LDS_AS f32x4*)(pq + RE * 4), o1 = *(LDS_AS f32x4*)(pq + RE * 4 + 16);
    const float p8 = *(LDS_AS float*)(pq + 32) + *(LDS_AS float*)(pq + RE * 4 + 32);
    const f32x4 m0 = e0 - o0, m1 = e1 - o1, p0 = e0 + o0, p1 = e1 + o1;
    const f32x4 yy = {yn, -yn, yn, -yn};
    const f32x4 b0 = (lo ? m0 : (f32x4){p8, p1[3], p1[2], p1[1]}) + yy;
    const f32x4 b1 = (lo ? m1 : (f32x4){p1[0], p0[3], p0[2], p0[1]}) + yy;
    const f32x4 r0 = b0 + gq * (a0 - b0), r1 = b1 + gq * (a1 - b1);
    __builtin_amdgcn_raw_buffer_store_b128(__builtin_bit_cast(u32x4, pack8(r0, r1)), ro, og[i], 0, OUT_AUX);
  }
  TS(14);
}

#define TS_END()

__global__ __launch_bounds__(512, 2) void fused_kernel(Args A) {
  extern __shared__ __attribute__((aligned(16))) char smem[];
  lptr sm = (lptr)smem;
  TS_DECL;
  TS(0);
  {
    unsigned long long pc; unsigned len;
    asm volatile("s_getpc_b64 %0\n.Lfk_start:\n\ts_mov_b32 %1, .Lfk_end-.Lfk_start" : "=s"(pc), "=s"(len));
    pf_asm(mk_rsrc((const void*)pc, len), (unsigned)(size_t)sm + K1_JUNK + 1792, (threadIdx.x * 32 + (blockIdx.x >> 3)) * 128, 0);
  }
  const rsrc_t re = mk_rsrc(A.ws + WS_EPOCH, 4);
  const unsigned e = *(const __attribute__((address_space(4))) unsigned*)(A.ws + WS_EPOCH);
  f32x4 pq[2][5];
  phase1(A, sm, pq, e TS_PASS);
  phase2(A, sm, e, pq TS_PASS);
  publish(A.ws, 2, e);
  TS(10);
  phase3(A, sm, e TS_PASS);
  if (blockIdx.x == 0 && threadIdx.x == 0) __builtin_amdgcn_raw_buffer_store_b32(e + 1u, re, 0, 0, 16);
  TS_END();
  asm volatile(".Lfk_end:");
}

extern "C" void kernel_launch(void* const* d_in, const int* in_sizes, int n_in, void* d_out, int out_size, void* d_ws,
                              size_t ws_size, hipStream_t stream) {
  Args A;
  A.x = (const u16*)d_in[0]; A.wqkv = (const u16*)d_in[1]; A.wo = (const u16*)d_in[2];
  A.wr = (const u16*)d_in[4]; A.wi = (const u16*)d_in[5]; A.gw = (const u16*)d_in[6];
  A.ws = (char*)d_ws; A.out = (u16*)d_out;
  (void)hipFuncSetAttribute((const void*)fused_kernel, hipFuncAttributeMaxDynamicSharedMemorySize, LDS_ALL);
  fused_kernel<<<256, 512, LDS_ALL, stream>>>(A);
}
```

```cpp
#include <hip/hip_runtime.h>
#include <hip/hip_bf16.h>
#include <utility>

typedef unsigned short u16;
typedef __attribute__((ext_vector_type(8))) short bf16x8;
typedef __attribute__((ext_vector_type(4))) short s16x4;
typedef __attribute__((ext_vector_type(4))) float f32x4;
typedef __attribute__((ext_vector_type(2))) float f32x2;
typedef __attribute__((ext_vector_type(2))) __bf16 bf16x2_t;
typedef __attribute__((ext_vector_type(4))) unsigned u32x4;
typedef __attribute__((ext_vector_type(2))) unsigned u32x2;
#define LDS_AS __attribute__((address_space(3)))
#define DI __device__ __forceinline__
#define MFMA16(a, b, c) __builtin_amdgcn_mfma_f32_16x16x32_bf16((a), (b), (c), 0, 0, 0)
typedef LDS_AS char* lptr;
typedef __amdgpu_buffer_rsrc_t rsrc_t;

#define ST_AUX 16
#define OUT_AUX 16
#define K1_ROT 5
#define POLL_SLEEP 2
#define CODE_PF 1
#define P3_REV 0
#define P3_PF 3
#define P3_YW 3
#define P3_MAP 1
#define LD_AUX 16

constexpr double kPi = 3.14159265358979323846;
constexpr double ccos_s(double x) { double x2 = x * x, t = 1, s = 1; for (int i = 1; i < 12; ++i) { t *= -x2 / ((2 * i - 1) * (2 * i)); s += t; } return s; }
constexpr double csin_s(double x) { double x2 = x * x, t = x, s = x; for (int i = 1; i < 12; ++i) { t *= -x2 / ((2 * i) * (2 * i + 1)); s += t; } return s; }
constexpr double cos640(int m) {
  m %= 640; if (m > 320) m = 640 - m;
  bool neg = false; if (m > 160) { m = 320 - m; neg = true; }
  double v = (m <= 80) ? ccos_s(2 * kPi * m / 640) : csin_s(2 * kPi * (160 - m) / 640);
  return neg ? -v : v;
}
constexpr u16 to_bf16c(double v) { float f = (float)v; unsigned u = __builtin_bit_cast(unsigned, f); u += 0x7fffu + ((u >> 16) & 1u); return (u16)(u >> 16); }
struct C640 { u16 v[640]; };
constexpr C640 make_c640() { C640 c{}; for (int i = 0; i < 640; ++i) c.v[i] = to_bf16c(cos640(i)); return c; }
constexpr C640 COS640 = make_c640();

struct alignas(128) FRow { u16 v[10 * 512]; };
constexpr FRow make_frow(int id) {
  FRow t{};
  const int cq = id / 10, st = id % 10;
  for (int par = 0; par < 2; ++par) for (int nt = 0; nt < 5; ++nt) for (int l = 0; l < 64; ++l) {
    const int kk = 80 * cq + 16 * nt + (l & 15);
    for (int j = 0; j < 8; ++j) {
      const int d = 64 * st + 32 * (j >> 2) + 8 * (l >> 4) + 2 * (j & 3) + par;
      const int m = (kk == 1) ? (d * 160 + 160 * par) % 640 : (d * (kk >> 1) + 160 * (kk & 1)) % 640;
      t.v[((par * 5 + nt) * 64 + l) * 8 + j] = COS640.v[m];
    }
  }
  return t;
}
template <int I> constexpr FRow FROW = make_frow(I);
struct alignas(128) FTab { FRow r[40]; };
template <int... I> constexpr FTab make_ftab(std::integer_sequence<int, I...>) { return FTab{{FROW<I>...}}; }
__device__ const FTab FT = make_ftab(std::make_integer_sequence<int, 40>{});

struct alignas(128) GRow { u16 v[10 * 3 * 512]; };
constexpr GRow make_grow(int id) {
  GRow t{};
  const int ct = id >> 1, od = id & 1;
  for (int ks = 0; ks < 10; ++ks) for (int nt = 0; nt < 3; ++nt) for (int l = 0; l < 64; ++l) {
    const int ci = 16 * nt + (l & 15), d = 40 * ct + ci;
    for (int j = 0; j < 8; ++j) {
      const int f = 32 * ks + 8 * (l >> 4) + j;
      t.v[((ks * 3 + nt) * 64 + l) * 8 + j] = (ci <= 40) ? COS640.v[(f * d + (od ? 160 : 0)) % 640] : (u16)((od && ci == 41 && f == 0) ? 0x3f80 : 0);
    }
  }
  return t;
}
template <int I> constexpr GRow GROW = make_grow(I);
struct alignas(128) GTab { GRow r[16]; };
template <int... I> constexpr GTab make_gtab(std::integer_sequence<int, I...>) { return GTab{{GROW<I>...}}; }
__device__ const GTab GT = make_gtab(std::make_integer_sequence<int, 16>{});

constexpr size_t WS_P   = 0;
constexpr size_t WS_Y   = WS_P + 2621440;
constexpr size_t WS_X1  = WS_Y + 2621440;
constexpr size_t WS_WT  = WS_X1 + 321 * 8192;
constexpr size_t WS_KVP = WS_WT + 352 * 8192;
constexpr size_t WS_M   = WS_KVP + 983040;
constexpr size_t WS_G   = WS_M + 819200;
constexpr size_t WS_ZP  = WS_G + 8192;
constexpr size_t WS_SYN = WS_ZP + 40960;

DI rsrc_t mk_rsrc(const void* p, unsigned bytes) { return __builtin_amdgcn_make_buffer_rsrc((void*)p, 0, bytes, 0x00020000); }
DI unsigned pk2(float a, float b) { f32x2 v = {a, b}; return __builtin_bit_cast(unsigned, __builtin_convertvector(v, bf16x2_t)); }
DI float bf_lo(unsigned u) { return __uint_as_float(u << 16); }
DI float bf_hi(unsigned u) { return __uint_as_float(u & 0xffff0000u); }
DI float dot2(unsigned a, unsigned b, float c) { return __builtin_amdgcn_fdot2_f32_bf16(__builtin_bit_cast(bf16x2_t, a), __builtin_bit_cast(bf16x2_t, b), c, false); }
constexpr float kEps = 1e-6f, kL2E = 1.4426950408889634f;
DI float phi_f(float a) {
  float e;
  const float u = __builtin_fmaf(a, kL2E, -kEps * kL2E);
  asm("v_exp_f32_e64 %0, %1 clamp" : "=v"(e) : "v"(u));
  return __builtin_amdgcn_fmed3f(a, kEps, __builtin_inff()) + e;
}
DI bf16x8 pack8(const f32x4& a, const f32x4& b) {
  u32x4 p = {pk2(a[0], a[1]), pk2(a[2], a[3]), pk2(b[0], b[1]), pk2(b[2], b[3])};
  return __builtin_bit_cast(bf16x8, p);
}
DI u32x4 widen(u32x2 a, u32x2 b) {
  auto x = __builtin_amdgcn_permlane16_swap(a[0], b[0], false, false);
  auto y = __builtin_amdgcn_permlane16_swap(a[1], b[1], false, false);
  return (u32x4){x[0], y[0], x[1], y[1]};
}
#define WAIT_V_(n) asm volatile("s_waitcnt vmcnt(" #n ")" ::: "memory")
#define WAIT_V(n) WAIT_V_(n)
#define BC(v) __builtin_bit_cast(bf16x8, (v))
#define WAIT_L0() __builtin_amdgcn_s_waitcnt(0xC07F)
#define BAR() __builtin_amdgcn_s_barrier()
#define SCHED() __builtin_amdgcn_sched_barrier(0)
#define TRREAD(p) __builtin_amdgcn_ds_read_tr16_b64_v4i16((LDS_AS s16x4*)(p))
#define ST16(v, rs, vo, so) __builtin_amdgcn_raw_buffer_store_b128((v), (rs), (vo), (so), ST_AUX)

#define TS_DECL
#define TS(i)
#define TC(i)
#define TS_FLUSH(kern)
#define TS_FLUSH2()
#define TS_ARG
#define TS_PASS

constexpr int K1_STAGE = 40960;
constexpr int K1_WF = 3 * K1_STAGE;
constexpr int K1_JUNK = K1_WF + 33792;
constexpr int K1_LDS = K1_JUNK + 2048;

struct Frags { bf16x8 a[2][2]; bf16x8 b[2][5]; };
struct K1Ctx { rsrc_t ra, rb; unsigned va[2], vb[3], ao[3][2], bo[3][5], ldsa, ldsb; int st, rs, wave; };

template <int I0, int I1> DI void k1_read(Frags& F, lptr sm, const unsigned (&ao)[2], const unsigned (&bo)[5]) {
#pragma unroll
  for (int i = I0; i < I1; ++i) {
    const int k = i / 7, j = i % 7;
    if (j < 2) F.a[k][j] = *(LDS_AS bf16x8*)(sm + ao[k] + j * 2048);
    else {
      const s16x4 lo = TRREAD(sm + bo[j - 2] + k * 12288), hi = TRREAD(sm + bo[j - 2] + k * 12288 + 512);
      F.b[k][j - 2] = __builtin_shufflevector(lo, hi, 0, 1, 2, 3, 4, 5, 6, 7);
    }
  }
}
DI void dma16_asm(rsrc_t rs, unsigned lds, unsigned vo, int so) {
  asm volatile("s_mov_b32 m0, %2\n\ts_nop 0\n\tbuffer_load_dwordx4 %0, %1, %3 offen lds" :: "v"(vo), "s"(rs), "s"(lds), "s"(so));
}
DI void pf_asm(rsrc_t rs, unsigned lds, unsigned vo, int so) {
  asm volatile("s_mov_b32 m0, %2\n\ts_nop 0\n\tbuffer_load_dword %0, %1, %3 offen lds" :: "v"(vo), "s"(rs), "s"(lds), "s"(so));
}
template <int BUF, int I> DI void k1_issue1(const K1Ctx& c) {
  if (I < 2) dma16_asm(c.ra, c.ldsa + BUF * K1_STAGE + I * 1024, c.va[I < 2 ? I : 0], c.st * 128);
  else dma16_asm(c.rb, c.ldsb + BUF * K1_STAGE + (I - 2) * 1024, c.vb[I < 2 ? 0 : I - 2], c.st * 64 * c.rs);
}
template <int BUF> DI void k1_issue(K1Ctx& c) {
  k1_issue1<BUF, 0>(c); k1_issue1<BUF, 1>(c); k1_issue1<BUF, 2>(c); k1_issue1<BUF, 3>(c); k1_issue1<BUF, 4>(c);
  c.st = (c.st == 9) ? 0 : c.st + 1;
}
template <bool SWAP, int I0, int I1> DI void k1_mma(f32x4 (&acc)[2][5], const Frags& F) {
#pragma unroll
  for (int i = I0; i < I1; ++i) {
    const int k = i / 10, mt = (i / 5) & 1, nt = i % 5;
    acc[mt][nt] = SWAP ? MFMA16(F.b[k][nt], F.a[k][mt], acc[mt][nt]) : MFMA16(F.a[k][mt], F.b[k][nt], acc[mt][nt]);
  }
}
template <bool SWAP, int N, int SEG> DI void k1_seg(f32x4 (&acc)[2][5], Frags& Fn, const Frags& Fp, lptr sm, K1Ctx& c) {
  constexpr int R0[6] = {0, 4, 7, 10, 12, 14};
  k1_read<R0[SEG], R0[SEG + 1]>(Fn, sm, c.ao[N % 3], c.bo[N % 3]);
  if (N > 0) {
    k1_mma<SWAP, 4 * SEG, 4 * SEG + 4>(acc, Fp);
    if (SEG < 2) {
      __builtin_amdgcn_sched_group_barrier(0x008, 1, 0); __builtin_amdgcn_sched_group_barrier(0x100, 2, 0);
      __builtin_amdgcn_sched_group_barrier(0x008, 1, 0); __builtin_amdgcn_sched_group_barrier(0x100, 2, 0);
      __builtin_amdgcn_sched_group_barrier(0x008, 1, 0); __builtin_amdgcn_sched_group_barrier(0x100, 2, 0);
      __builtin_amdgcn_sched_group_barrier(0x008, 1, 0);
    } else {
#pragma unroll
      for (int i = 0; i < 4; ++i) { __builtin_amdgcn_sched_group_barrier(0x008, 1, 0); __builtin_amdgcn_sched_group_barrier(0x100, 2, 0); }
    }
  }
  SCHED();
  if (N + 2 <= 9) k1_issue1<(N + 2) % 3, SEG>(c);
  SCHED();
}
template <bool SWAP, int N> DI void k1_step(f32x4 (&acc)[2][5], Frags& Fn, const Frags& Fp, lptr sm, K1Ctx& c) {
  WAIT_L0();
  BAR();
  SCHED();
  k1_seg<SWAP, N, 0>(acc, Fn, Fp, sm, c);
  k1_seg<SWAP, N, 1>(acc, Fn, Fp, sm, c);
  k1_seg<SWAP, N, 2>(acc, Fn, Fp, sm, c);
  k1_seg<SWAP, N, 3>(acc, Fn, Fp, sm, c);
  k1_seg<SWAP, N, 4>(acc, Fn, Fp, sm, c);
  c.st = (c.st == 9) ? 0 : c.st + 1;
}

struct DFrags { u32x4 a[2][2]; bf16x8 b[2][3]; };
constexpr int KD_STAGE = 26624;
template <int BUF, int I> DI void kd_issue1(const K1Ctx& c) {
  if (I < 2) dma16_asm(c.ra, c.ldsa + BUF * KD_STAGE + I * 1024, c.va[I & 1], c.st * 128);
  else if (I == 2) dma16_asm(c.rb, c.ldsb + BUF * KD_STAGE, c.vb[0], c.st * 10240);
  else dma16_asm(c.rb, c.wave < 2 ? c.ldsb + BUF * KD_STAGE + 8192 : c.ldsa - 2048 * c.wave + K1_JUNK, c.vb[1], c.st * 10240);
}
template <int BUF> DI void kd_issue(K1Ctx& c) {
  kd_issue1<BUF, 0>(c); kd_issue1<BUF, 2>(c); kd_issue1<BUF, 1>(c); kd_issue1<BUF, 3>(c);
  c.st = (c.st == 9) ? 0 : c.st + 1;
}
template <int NT> DI void kd_mma(f32x4 (&acc)[2][2][3], const DFrags& F) {
#pragma unroll
  for (int mt = 0; mt < 2; ++mt) {
    u32x4 ev, od;
#pragma unroll
    for (int k = 0; k < 2; ++k)
#pragma unroll
      for (int i = 0; i < 2; ++i) {
        ev[2 * k + i] = __builtin_amdgcn_perm(F.a[mt][k][2 * i + 1], F.a[mt][k][2 * i], 0x05040100u);
        od[2 * k + i] = __builtin_amdgcn_perm(F.a[mt][k][2 * i + 1], F.a[mt][k][2 * i], 0x07060302u);
      }
#pragma unroll
    for (int nt = 0; nt < NT; ++nt) {
      acc[0][mt][nt] = MFMA16(F.b[0][nt], BC(ev), acc[0][mt][nt]);
      acc[1][mt][nt] = MFMA16(F.b[1][nt], BC(od), acc[1][mt][nt]);
    }
  }
}
template <int NT, int N> DI void kd_step(f32x4 (&acc)[2][2][3], DFrags& Fn, const DFrags& Fp, lptr sm, K1Ctx& c) {
  WAIT_L0();
  BAR();
  SCHED();
  if (N + 3 <= 9) kd_issue<(N + 3) % 4>(c);
  SCHED();
  constexpr int H = (N % 4) >> 1, O = (N & 1) * KD_STAGE;
#pragma unroll
  for (int mt = 0; mt < 2; ++mt)
#pragma unroll
    for (int k = 0; k < 2; ++k) Fn.a[mt][k] = *(LDS_AS u32x4*)(sm + c.ao[H][k] + O + mt * 2048);
#pragma unroll
  for (int par = 0; par < 2; ++par)
#pragma unroll
    for (int nt = 0; nt < NT; ++nt) Fn.b[par][nt] = *(LDS_AS bf16x8*)(sm + c.bo[H][0] + O + (par * 5 + nt) * 1024);
  if (N > 0) {
    kd_mma<NT>(acc, Fp);
    __builtin_amdgcn_sched_group_barrier(0x100, 2, 0);
    __builtin_amdgcn_sched_group_barrier(0x002, 8, 0);
#pragma unroll
    for (int i = 0; i < 2 * NT; ++i) { __builtin_amdgcn_sched_group_barrier(0x008, 1, 0); __builtin_amdgcn_sched_group_barrier(0x100, 1, 0); __builtin_amdgcn_sched_group_barrier(0x002, 2, 0); }
  }
  SCHED();
}

DI u32x2 make_tag(unsigned e) { return (u32x2){e + 1u, (e + 1u) * 0x9E3779B9u ^ 0x5bd1e995u}; }
DI int p1_rt(int b) { return (b & 1) * 8 + ((b >> 3) & 7); }
DI int p1_ct(int b) { const int x2 = (b & 7) >> 1, j = b >> 6; return j == 0 ? x2 : j == 3 ? 12 + x2 : 3 + 2 * x2 + j; }
DI int flag_slot(int b) { return p1_ct(b) * 16 + p1_rt(b); }
constexpr size_t WS_EPOCH = WS_SYN + 3 * 65536;
DI void flag_store(char* ws, int which, unsigned e, int lane) {
  __builtin_amdgcn_raw_buffer_store_b64(make_tag(e), mk_rsrc(ws + WS_SYN + which * 65536, 65536), lane * 2048 + flag_slot(blockIdx.x) * 8, 0, 16);
}
template <int NT, class FL, class FS> DI void kd_main(char* ws, lptr sm, K1Ctx& c, int rt, int cq, unsigned e, int lane, FL filt_load, FS filt_store TS_ARG) {
  const int wm = c.wave & 3, r = lane & 15, g = lane >> 4;
  constexpr int T0 = NT == 3 ? 0 : 3;
  DFrags D0, D1;
  f32x4 acc[2][2][3];
#pragma unroll
  for (int u = 0; u < 2; ++u) {
    c.ao[u][0] = 2 * u * KD_STAGE + (32 * wm + r) * 128 + ((g ^ (r >> 1)) * 16);
    c.ao[u][1] = 2 * u * KD_STAGE + (32 * wm + r) * 128 + (((4 + g) ^ (r >> 1)) * 16);
    c.bo[u][0] = 2 * u * KD_STAGE + 16384 + T0 * 1024 + lane * 16;
    asm volatile("" : "+v"(c.ao[u][0]), "+v"(c.ao[u][1]), "+v"(c.bo[u][0]));
  }
#pragma unroll
  for (int i = 0; i < 4 * NT; ++i) { acc[i / (2 * NT)][(i / NT) & 1][i % NT] = (f32x4){0.f, 0.f, 0.f, 0.f}; asm volatile("" : "+v"(acc[i / (2 * NT)][(i / NT) & 1][i % NT])); }
  TS(1);
  WAIT_V(8); kd_step<NT, 0>(acc, D0, D1, sm, c);
  TS(2);
  WAIT_V(8); kd_step<NT, 1>(acc, D1, D0, sm, c);
  WAIT_V(8); kd_step<NT, 2>(acc, D0, D1, sm, c);
  WAIT_V(8); kd_step<NT, 3>(acc, D1, D0, sm, c);
  filt_load();
  WAIT_V(13); kd_step<NT, 4>(acc, D0, D1, sm, c);
  WAIT_V(13); kd_step<NT, 5>(acc, D1, D0, sm, c);
  WAIT_V(13); kd_step<NT, 6>(acc, D0, D1, sm, c);
  WAIT_V(8); kd_step<NT, 7>(acc, D1, D0, sm, c);
  WAIT_V(4); kd_step<NT, 8>(acc, D0, D1, sm, c);
  filt_store();
  WAIT_V(3); kd_step<NT, 9>(acc, D1, D0, sm, c);
  kd_mma<NT>(acc, D1);
  TS(3);
  const rsrc_t rx = mk_rsrc(ws + WS_X1 + (wm >> 1) * 4096 + (rt >> 2) * 1024 + (rt & 3) * 256 + (wm & 1) * 128, 321 * 8192);
  const bool up = r >= 8;
  const int fb = 40 * cq + 8 * T0 + 2 * g;
  const bool sp = (fb == 0);
#pragma unroll
  for (int nt = 0; nt < NT; ++nt) {
    unsigned it[4][2];
#pragma unroll
    for (int mt = 0; mt < 2; ++mt) {
      const f32x4 E = acc[0][mt][nt], O = acc[1][mt][nt], P = E + O, M = E - O;
      it[0][mt] = pk2(P[0], P[1]); it[1][mt] = pk2(P[2], P[3]); it[2][mt] = pk2(M[0], -M[1]); it[3][mt] = pk2(M[2], -M[3]);
      if (NT == 3 && nt == 0) {
        it[0][mt] = sp ? (it[0][mt] & 0xffffu) : it[0][mt]; it[2][mt] = sp ? (it[2][mt] & 0xffffu) : it[2][mt];
        __builtin_amdgcn_raw_buffer_store_b32(pk2(E[1], O[1]), rx, sp ? 160 * 8192 + (r & 7) * 16 + (r >> 3) * 8 + mt * 4 : 0x7fffffff, 0, ST_AUX);
      }
    }
#pragma unroll
    for (int h = 0; h < 2; ++h) {
      const unsigned t0 = __builtin_amdgcn_update_dpp(0u, up ? it[2 * h][0] : it[2 * h + 1][0], 0x128, 0xf, 0xf, false);
      const unsigned t1 = __builtin_amdgcn_update_dpp(0u, up ? it[2 * h][1] : it[2 * h + 1][1], 0x128, 0xf, 0xf, false);
      const u32x4 o = {up ? t0 : it[2 * h][0], up ? t1 : it[2 * h][1], up ? it[2 * h + 1][0] : t0, up ? it[2 * h + 1][1] : t1};
      const int f0 = fb + 8 * nt + (r >> 3);
      ST16(o, rx, (h ? 320 - f0 : f0) * 8192 + (r & 7) * 16, 0);
    }
  }
}

struct Args { const u16 *x, *wqkv, *wo, *wr, *wi, *gw; char* ws; u16* out; };

DI void phase1(const Args& A, lptr sm, f32x4 (&pq)[2][5], unsigned e TS_ARG) {
  const u16* x = A.x; const u16* wqkv = A.wqkv; const u16* wr = A.wr; const u16* wi = A.wi; char* ws = A.ws;
  const int tid = threadIdx.x, lane = tid & 63;
  const int wave = __builtin_amdgcn_readfirstlane(tid >> 6);
  const int b = blockIdx.x;
  const int xcd = b & 7, idx = b >> 3;
  const int rt = p1_rt(b), ct = p1_ct(b);
  (void)xcd;
  const bool dft = ct >= 12, iskv = ct >= 4 && ct < 12;
  const int c0 = iskv ? 80 * (ct - 4) : 160 * (ct & 3);
  const int L0 = c0 >> 6, p0 = (c0 & 63) >> 4;
  const int xs = (p0 == 0 || p0 == 3) ? 1 : 2;
  const int bigB = p0 >= 2 ? 1 : 0;

  K1Ctx c;
  c.wave = wave;
  c.ra = mk_rsrc(x, 2048 * 1280);
  c.rb = dft ? mk_rsrc((const char*)&FT + (ct & 3) * 102400, 102400) : mk_rsrc(wqkv, 640 * 3840);
  c.rs = 3840;
  auto grow = [&](int row) { return dft ? 32 * (4 * rt + 2 * ((row >> 3) & 1) + ((row >> 4) & 1)) + 8 * (row >> 5) + (row & 7) : 128 * rt + row; };
#pragma unroll
  for (int i = 0; i < 2; ++i) {
    const int row = 16 * wave + 8 * i + (lane >> 3);
    c.va[i] = grow(row) * 1280 + ((lane & 7) ^ ((row >> 1) & 7)) * 16;
  }
  c.ldsa = (unsigned)(size_t)sm + 2 * wave * 1024;
  c.st = (((((idx & 7) * 5 + (idx >> 3) * 10) >> 2) % 10) * K1_ROT / 10) * (10 / K1_ROT);
  k1_issue1<0, 0>(c); k1_issue1<0, 1>(c);
  SCHED();
  {
    const int row = lane >> 3, hf = lane & 1;
    const int pair = ((lane & 7) >> 1) ^ (((row >> 1) & 1) | ((wave & 1) << 1));
    const unsigned rowb = (8 * wave + row) * c.rs;
    const bool kown = bigB ? (pair >= p0) : (pair <= p0);
    c.vb[0] = rowb + (iskv ? 1280 + (L0 + bigB) * 128 : L0 * 128) + pair * 32 + hf * 16;
    c.vb[1] = rowb + (iskv ? 2560 + (L0 + bigB) * 128 + (hf ^ 1) * 16 : (L0 + 1) * 128 + hf * 16) + pair * 32;
    c.vb[2] = rowb + (iskv ? (L0 + 1 - bigB) * 128 + (kown ? 1280 + pair * 32 + hf * 16 : 2560 + (pair ^ xs) * 32 + (hf ^ 1) * 16)
                           : (L0 + 2) * 128 + pair * 32 + hf * 16);
  }
  c.ldsb = (unsigned)(size_t)sm + 16384 + (dft ? 1 : 3) * wave * 1024;
  if (dft) { c.vb[0] = wave * 1024 + lane * 16; c.vb[1] = wave < 2 ? c.vb[0] + 8192 : 0x7fffffffu; }
  if (dft) { kd_issue1<0, 2>(c); kd_issue1<0, 3>(c); }
  else { k1_issue1<0, 2>(c); k1_issue1<0, 3>(c); k1_issue1<0, 4>(c); }
  c.st = (c.st == 9) ? 0 : c.st + 1;
  BAR();
  if (dft) { kd_issue<1>(c); kd_issue<2>(c); }
  else k1_issue<1>(c);

  const int wm = wave >> 1, wn = wave & 1;
  const int r = lane & 15, g = lane >> 4, q = (lane & 15) >> 2, p = lane & 3;
  const int hsw = ((q >> 1) & 1) | ((g & 1) << 1);
  const unsigned bbase = 16384 + g * 3072 + q * 128;
  c.ao[0][0] = (32 * wm + r) * 128 + ((g ^ (r >> 1)) * 16);
  c.ao[0][1] = (32 * wm + r) * 128 + (((4 + g) ^ (r >> 1)) * 16);
  f32x4 acc[2][5];
  Frags F0, F1;

#define K1_HEAD(SWAP, HOOK, W2)                                                                          \
  _Pragma("unroll") for (int u = 1; u < 3; ++u) {                                              \
    _Pragma("unroll") for (int k = 0; k < 2; ++k) { c.ao[u][k] = c.ao[0][k] + u * K1_STAGE; asm volatile("" : "+v"(c.ao[u][k])); }     \
    _Pragma("unroll") for (int k = 0; k < 5; ++k) { c.bo[u][k] = c.bo[0][k] + u * K1_STAGE; asm volatile("" : "+v"(c.bo[u][k])); }     \
  }                                                                                            \
  _Pragma("unroll") for (int mt = 0; mt < 2; ++mt)                                             \
    _Pragma("unroll") for (int nt = 0; nt < 5; ++nt) asm volatile("" : "+v"(acc[mt][nt]));     \
  TS(1);                                                                                       \
  WAIT_V(5); k1_step<SWAP, 0>(acc, F0, F1, sm, c);                                           \
  TS(2);                                                                                       \
  WAIT_V(5); k1_step<SWAP, 1>(acc, F1, F0, sm, c); HOOK;                                             \
  WAIT_V(W2); k1_step<SWAP, 2>(acc, F0, F1, sm, c);

  if (iskv) {
#pragma unroll
    for (int nt = 0; nt < 5; ++nt) {
      const int u = 4 * nt + p;
      const int isv = u >= 10 ? 1 : 0;
      const int pc = 4 * p0 + 10 * wn + u - 10 * isv;
      const int gp = pc >> 2, big = ((gp >> 2) == bigB);
      const int slot = (gp & 3) ^ ((!big && isv) ? xs : 0);
      c.bo[0][nt] = bbase + (big ? isv : 2) * 1024 + ((slot ^ hsw) * 32) + (((pc & 3) ^ (isv << 1)) * 8);
      const float i0 = ((nt < 2) || (nt == 2 && r < 8)) ? kEps : 0.f;
#pragma unroll
      for (int mt = 0; mt < 2; ++mt) acc[mt][nt] = (f32x4){i0, i0, i0, i0};
    }
    unsigned ko[8];
    constexpr int TI[8] = {0, 0, 1, 1, 1, 2, 2, 2}, TJ[8] = {2, 3, 2, 3, 4, 3, 4, 2};
#pragma unroll
    for (int i = 0; i < 8; ++i) {
      const int ti = TI[i], tj = TJ[i];
      const int ur = 16 * ti + r, hk = ur >= 20 ? 1 : 0;
      const int uc = 16 * tj + 4 * g - 40, hv = uc >= 20 ? 1 : 0;
      const bool isz = tj == 2 && g == 0;
      const bool valid = ur < 40 && (isz || (uc >= 0 && hk == hv));
      const int off = wave * 3840 + hk * 1920 + (ur - 20 * hk) * 96 + (isz ? 80 : (uc - 20 * hv) * 4);
      ko[i] = K1_STAGE + (valid ? off : 8 * 3840 + lane * 16);
      asm volatile("" : "+v"(ko[i]));
    }
    K1_HEAD(false, , 5)
    WAIT_V(5); k1_step<false, 3>(acc, F1, F0, sm, c);
    WAIT_V(5); k1_step<false, 4>(acc, F0, F1, sm, c);
    WAIT_V(5); k1_step<false, 5>(acc, F1, F0, sm, c);
    WAIT_V(5); k1_step<false, 6>(acc, F0, F1, sm, c);
    WAIT_V(5); k1_step<false, 7>(acc, F1, F0, sm, c);
    WAIT_V(5); k1_step<false, 8>(acc, F0, F1, sm, c);
    WAIT_V(0); k1_step<false, 9>(acc, F1, F0, sm, c);
    k1_mma<false, 0, 20>(acc, F1);
    TS(3);
    bf16x8 fr[5];
#pragma unroll
    for (int nt = 0; nt < 5; ++nt) {
#pragma unroll
      for (int m = 0; m < 2; ++m)
#pragma unroll
        for (int j = 0; j < 4; ++j) {
          if (nt < 2) acc[m][nt][j] = phi_f(acc[m][nt][j]);
          if (nt == 2) { const float ph = phi_f(acc[m][nt][j]); acc[m][nt][j] = (r < 8) ? ph : acc[m][nt][j]; }
        }
      fr[nt] = pack8(acc[0][nt], acc[1][nt]);
    }
    const bf16x8 ones = {0x3f80, 0x3f80, 0x3f80, 0x3f80, 0x3f80, 0x3f80, 0x3f80, 0x3f80};
    {
      const f32x4 zz = {0.f, 0.f, 0.f, 0.f};
      const bf16x8 v2 = (r < 8) ? ones : fr[2];
      f32x4 d[8];
#pragma unroll
      for (int i = 0; i < 8; ++i) d[i] = MFMA16(TJ[i] == 2 ? v2 : fr[TJ[i]], fr[TI[i]], zz);
#pragma unroll
      for (int i = 0; i < 8; ++i) *(LDS_AS f32x4*)(sm + ko[i]) = d[i];
    }
    WAIT_L0();
    BAR();
    const rsrc_t rk = mk_rsrc(ws + WS_KVP + ((size_t)rt * 32 + 4 * (ct - 4)) * 1920, 7680);
    LDS_AS f32x4* src = (LDS_AS f32x4*)(sm + K1_STAGE);
    const f32x4 s0 = (src[tid] + src[tid + 480]) + (src[tid + 960] + src[tid + 1440]);
    ST16(__builtin_bit_cast(u32x4, s0), rk, tid * 16, 0);
    if (wave >= 6) {
      LDS_AS float* zs = (LDS_AS float*)(sm + K1_STAGE) + min(tid - 384, 79) * 24 + 20;
      const float z = (zs[0] + zs[1920]) + (zs[3840] + zs[5760]);
      __builtin_amdgcn_raw_buffer_store_b32(__float_as_uint(z), mk_rsrc(ws + WS_ZP + rt * 2560 + (ct - 4) * 320, 320), (tid - 384) * 4, 0, ST_AUX);
    }
  } else {
    const int un = (dft ? 64 : 0) + rt * 4 + (ct & 3), mo = un >> 1, fh = un & 1;
    auto filt_load = [&]() {
      const rsrc_t rf = mk_rsrc((wave >> 2) ? wi : wr, 2048 * 642);
      const int kb = 16 * (mo >> 4) + 8 * (mo & 1) + 64 * (16 * ((mo >> 3) & 1) + 4 * ((mo >> 1) & 3) + (wave & 3));
      const unsigned v0 = lane < 21 ? lane * 16 : (lane >= 25 && lane < 46) ? 640 + (lane - 25) * 16 : 0x7fffffffu;
#pragma unroll
      for (int i = 0; i < 4; ++i)
        dma16_asm(rf, (unsigned)(size_t)sm + K1_WF + (wave * 4 + i) * 1056, v0, kb * 642 + 320 * fh + i * 1280);
      pf_asm(wave == 0 ? mk_rsrc(A.wo, 819200) : mk_rsrc(&GT, wave == 1 ? 491520 : 0), (unsigned)(size_t)sm + K1_JUNK + 1024,
             lane < (wave ? 30 : 50) ? (un * (wave ? 30 : 50) + lane) * 128 : 0x7fffffffu, 0);
    };
    auto filt_store = [&]() {
      const rsrc_t rw = mk_rsrc(ws + WS_WT + (size_t)fh * 160 * 8192 + mo * 128, (160 + fh) * 8192 - mo * 128);
      const int i8 = lane & 7;
#pragma unroll
      for (int rd = 0; rd < 3; ++rd) {
        const int fl = (rd * 8 + wave) * 8 + (lane >> 3);
        lptr src = sm + K1_WF + (i8 >> 1) * 1056 + (i8 & 1) * 400 + i8 * 2 + min(fl, 160) * 2;
        u32x4 o;
#pragma unroll
        for (int j = 0; j < 4; ++j) o[j] = (unsigned)*(LDS_AS u16*)(src + j * 4224) | ((unsigned)*(LDS_AS u16*)(src + 16896 + j * 4224) << 16);
        ST16(o, rw, fl * 8192 + i8 * 16, 0);
      }
      SCHED();
    };
    if (!dft) {
#pragma unroll
      for (int nt = 0; nt < 5; ++nt) {
        const int gp = p0 + 5 * wn + nt;
        c.bo[0][nt] = bbase + (gp >> 2) * 1024 + (((gp & 3) ^ hsw) * 32) + p * 8;
      }
#pragma unroll
      for (int mt = 0; mt < 2; ++mt)
#pragma unroll
        for (int nt = 0; nt < 5; ++nt) acc[mt][nt] = (f32x4){kEps, kEps, kEps, kEps};
      K1_HEAD(true, , 5)
      WAIT_V(5); k1_step<true, 3>(acc, F1, F0, sm, c);
      filt_load();
      WAIT_V(10); k1_step<true, 4>(acc, F0, F1, sm, c);
      WAIT_V(10); k1_step<true, 5>(acc, F1, F0, sm, c);
      WAIT_V(5); k1_step<true, 6>(acc, F0, F1, sm, c);
      filt_store();
      WAIT_V(8); k1_step<true, 7>(acc, F1, F0, sm, c);
      WAIT_V(8); k1_step<true, 8>(acc, F0, F1, sm, c);
      WAIT_V(0); k1_step<true, 9>(acc, F1, F0, sm, c);
      if (wave == 0) flag_store(ws, 1, e, lane);
      k1_mma<true, 0, 20>(acc, F1);
      TS(3);
#pragma unroll
      for (int mt = 0; mt < 2; ++mt)
#pragma unroll
        for (int nt = 0; nt < 5; ++nt)
#pragma unroll
          for (int j = 0; j < 4; ++j) pq[mt][nt][j] = phi_f(acc[mt][nt][j]);
    } else {
      if (wave < 4) kd_main<3>(ws, sm, c, rt, ct & 3, e, lane, filt_load, filt_store TS_PASS);
      else kd_main<2>(ws, sm, c, rt, ct & 3, e, lane, filt_load, filt_store TS_PASS);
    }
  }
  TS(4);
}

DI void publish(char* ws, int which, unsigned e) {
  WAIT_V(0);
  BAR();
  if (threadIdx.x < 64) flag_store(ws, which, e, threadIdx.x);
}
DI bool poll(char* ws, int which, unsigned e, bool n0, bool n1, int lane, unsigned tries = 1u << 17) {
  const rsrc_t rf = mk_rsrc(ws + WS_SYN + which * 65536 + (blockIdx.x & 31) * 2048, 2048);
  const u32x2 t = make_tag(e);
  for (unsigned spin = 0; spin < tries; ++spin) {
    const u32x4 f0 = __builtin_amdgcn_raw_buffer_load_b128(rf, n0 ? lane * 16 : 0x7fffffffu, 0, 16);
    const u32x4 f1 = __builtin_amdgcn_raw_buffer_load_b128(rf, n1 ? lane * 16 + 1024 : 0x7fffffffu, 0, 16);
    n0 = n0 && !(f0[0] == t[0] && f0[1] == t[1] && f0[2] == t[0] && f0[3] == t[1]);
    n1 = n1 && !(f1[0] == t[0] && f1[1] == t[1] && f1[2] == t[0] && f1[3] == t[1]);
    if (!__any(n0 || n1)) return true;
    __builtin_amdgcn_s_sleep(POLL_SLEEP);
  }
  return false;
}
DI void await(char* ws, int which, unsigned e, int need, int wave, int lane) {
  if (wave == 7) poll(ws, which, e, (need >> (lane < 32 ? 0 : 1)) & 1, (need >> (lane < 32 ? 1 : 2)) & 1, lane);
  asm volatile("" ::: "memory");
  BAR();
  asm volatile("" ::: "memory");
}
DI void dma16_sc1(rsrc_t rs, unsigned lds, unsigned vo, int so) {
  asm volatile("s_mov_b32 m0, %2\n\ts_nop 0\n\tbuffer_load_dwordx4 %0, %1, %3 offen sc1 lds" :: "v"(vo), "s"(rs), "s"(lds), "s"(so));
}
#define LD16(rs, vo, so) __builtin_amdgcn_raw_buffer_load_b128((rs), (vo), (so), LD_AUX)

constexpr int P3_HALF = 71680;

struct FftC {
  const u32x4 (&fre)[4]; const u32x4 (&fim)[4]; const u32x4 (&fc)[2]; const u32x4 (&fs)[2]; const u32x4 (&fn)[2];
  const f32x2 (&tc)[2][2]; const f32x2 (&tsn)[2][2]; const unsigned (&uo)[2][4]; const unsigned (&ro)[4]; unsigned vy;
};
DI f32x2 half2(const f32x4& v, int h) { return h ? (f32x2){v[2], v[3]} : (f32x2){v[0], v[1]}; }
constexpr int FSECOND = 304;
template <int NC> DI void fft_run(char* ws, lptr sm, const FftC& T, const u32x4 (&wv)[2][2], int fu0, int wave8, int lane) {
  const f32x4 z4 = {0.f, 0.f, 0.f, 0.f};
  f32x2 wre[NC][2][2], wim[NC][2][2];
#pragma unroll
  for (int c = 0; c < NC; ++c) {
    const int f = min((c ? FSECOND + (fu0 >> 1) : fu0), 320);
    const float sc = ((f == 0 || f == 320) ? 1.f : 2.f) / (2048.f * 640.f);
#pragma unroll
    for (int t = 0; t < 2; ++t)
#pragma unroll
      for (int h = 0; h < 2; ++h) {
        wre[c][t][h] = (f32x2){bf_lo(wv[c][t][2 * h]), bf_lo(wv[c][t][2 * h + 1])} * sc;
        wim[c][t][h] = (f32x2){bf_hi(wv[c][t][2 * h]), bf_hi(wv[c][t][2 * h + 1])} * sc;
      }
  }
#define FOR_C _Pragma("unroll") for (int c = 0; c < NC; ++c)
#define FOR_T _Pragma("unroll") for (int t = 0; t < 2; ++t)
#define FOR_H _Pragma("unroll") for (int h = 0; h < 2; ++h)
#define FOR_K _Pragma("unroll") for (int ks = 0; ks < 4; ++ks)
  f32x4 re[NC][2], im[NC][2];
  u32x4 bre[NC], bim[NC];
  {
    bf16x8 xa[NC][2][4];
    FOR_K FOR_C FOR_T xa[c][t][ks] = *(LDS_AS bf16x8*)(sm + c * 16384 + lane * 16 + (t * 4 + ks) * 1024);
    FOR_K FOR_C FOR_T {
      re[c][t] = MFMA16(xa[c][t][ks], BC(T.fre[ks]), ks ? re[c][t] : z4);
      im[c][t] = MFMA16(xa[c][t][ks], BC(T.fim[ks]), ks ? im[c][t] : z4);
    }
  }
  FOR_C FOR_T FOR_H {
    const f32x2 a = half2(re[c][t], h), d = half2(im[c][t], h);
    const f32x2 x = a * T.tc[t][h] + d * T.tsn[t][h], y = d * T.tc[t][h] - a * T.tsn[t][h];
    bre[c][2 * t + h] = pk2(x[0], x[1]); bim[c][2 * t + h] = pk2(y[0], y[1]);
  }
  FOR_C FOR_T { re[c][t] = MFMA16(BC(T.fc[t]), BC(bre[c]), z4); im[c][t] = MFMA16(BC(T.fc[t]), BC(bim[c]), z4); }
  FOR_C FOR_T { re[c][t] = MFMA16(BC(T.fs[t]), BC(bim[c]), re[c][t]); im[c][t] = MFMA16(BC(T.fn[t]), BC(bre[c]), im[c][t]); }
  FOR_C FOR_T FOR_H {
    const f32x2 a = half2(re[c][t], h), d = half2(im[c][t], h);
    const f32x2 x = a * wre[c][t][h] - d * wim[c][t][h], y = a * wim[c][t][h] + d * wre[c][t][h];
    bre[c][2 * t + h] = pk2(x[0], x[1]); bim[c][2 * t + h] = pk2(y[0], y[1]);
  }
  FOR_C FOR_T { re[c][t] = MFMA16(BC(T.fc[t]), BC(bre[c]), z4); im[c][t] = MFMA16(BC(T.fc[t]), BC(bim[c]), z4); }
  FOR_C FOR_T { re[c][t] = MFMA16(BC(T.fn[t]), BC(bim[c]), re[c][t]); im[c][t] = MFMA16(BC(T.fs[t]), BC(bre[c]), im[c][t]); }
  FOR_C FOR_T FOR_H {
    const f32x2 a = half2(re[c][t], h), d = half2(im[c][t], h);
    const f32x2 x = a * T.tc[t][h] - d * T.tsn[t][h], y = -(d * T.tc[t][h]) - a * T.tsn[t][h];
    *(LDS_AS unsigned*)(sm + c * 16384 + T.uo[t][2 * h]) = pk2(x[0], y[0]);
    *(LDS_AS unsigned*)(sm + c * 16384 + T.uo[t][2 * h + 1]) = pk2(x[1], y[1]);
  }
  WAIT_L0();
  BAR();
  SCHED();
  {
    bf16x8 ua[NC][2][4];
    FOR_K FOR_C FOR_T ua[c][t][ks] = *(LDS_AS bf16x8*)(sm + c * 16384 + T.ro[ks] + t * 4096);
    FOR_K FOR_C FOR_T {
      re[c][t] = MFMA16(ua[c][t][ks], BC(T.fre[ks]), ks ? re[c][t] : z4);
      im[c][t] = MFMA16(ua[c][t][ks], BC(T.fim[ks]), ks ? im[c][t] : z4);
    }
  }
  FOR_C {
    const int fu = (c ? FSECOND + (fu0 >> 1) : fu0), f = min(fu, 320);
    const rsrc_t ry = mk_rsrc(ws + WS_Y + (size_t)f * 4096, fu > 320 ? 0 : ((f == 0 || f == 320) ? 4096 : 321 * 4096));
    ST16(widen((u32x2){pk2(re[c][0][0], re[c][0][1]), pk2(re[c][0][2], re[c][0][3])}, (u32x2){pk2(re[c][1][0], re[c][1][1]), pk2(re[c][1][2], re[c][1][3])}), ry, T.vy, 0);
    ST16(widen((u32x2){pk2(im[c][0][0], im[c][0][1]), pk2(im[c][0][2], im[c][0][3])}, (u32x2){pk2(im[c][1][0], im[c][1][1]), pk2(im[c][1][2], im[c][1][3])}), ry, T.vy, 320 * 4096);
  }
}

DI void phase2(const Args& A, lptr sm0, unsigned e, const f32x4 (&pq)[2][5] TS_ARG) {
  char* ws = A.ws;
  const int lane = threadIdx.x & 63;
  const int wave8 = __builtin_amdgcn_readfirstlane(threadIdx.x >> 6);
  const int wave = wave8 & 3, team = wave8 >> 2;
  lptr sm = sm0 + team * P3_HALF;
  const unsigned smu = (unsigned)(size_t)sm;
  const int b = blockIdx.x;
  const int rt1 = p1_rt(b), ct1 = p1_ct(b);
  const bool iskv = ct1 >= 4 && ct1 < 12;
  const int v = (ct1 - 4) * 16 + rt1;
  const int r = lane & 15, g = lane >> 4;
  const f32x4 z4 = {0.f, 0.f, 0.f, 0.f};
  const bool isfft = ct1 >= 12 || (iskv && v < 88);
  publish(ws, 1, e);
  TS(5);
  if (isfft) {
    const int fu0 = (iskv ? 128 + 2 * v : 2 * (rt1 * 4 + (ct1 & 3))) + team;
    const bool two = fu0 < 34 && team == 0;
    u32x4 wv[2][2];
    auto loads = [&]() {
#pragma unroll
      for (int c = 0; c < 2; ++c) {
        const int f = min(c ? FSECOND + (fu0 >> 1) : fu0, 320);
        const rsrc_t rx = mk_rsrc(ws + WS_X1 + (size_t)f * 8192, (c && !two) ? 0 : 8192), rw = mk_rsrc(ws + WS_WT + (size_t)f * 8192, (c && !two) ? 0 : 8192);
        dma16_sc1(rx, smu + c * 16384 + wave * 2048, lane * 16, wave * 2048);
        dma16_sc1(rx, smu + c * 16384 + wave * 2048 + 1024, lane * 16, wave * 2048 + 1024);
        wv[c][0] = LD16(rw, lane * 16, wave * 2048);
        wv[c][1] = LD16(rw, lane * 16 + 1024, wave * 2048);
      }
      SCHED();
    };
    await(ws, 1, e, 5, wave8, lane);
    TS(7);
    loads();
    u32x4 fre[4], fim[4], fc[2], fs[2], fn[2];
    f32x2 tc[2][2], tsn[2][2];
    unsigned uo[2][4], ro[4];
    {
      const float a64 = (float)(16 * wave + r) * (1.f / 64.f);
      const unsigned m2 = (r & 2) ? 0x80008000u : 0u, m1 = (r & 1) ? 0x80008000u : 0u;
      const unsigned m3 = ((r + 1) & 2) ? 0x80008000u : 0u;
#pragma unroll
      for (int j = 0; j < 4; ++j) {
        const float ang = a64 * (float)(4 * g + j);
        const float c = __builtin_amdgcn_cosf(ang), s = __builtin_amdgcn_sinf(ang);
        const unsigned p = pk2(c, s), q = pk2(-s, c);
        fre[0][j] = p; fim[0][j] = q;
        fre[1][j] = ((r & 1) ? q : p) ^ m2;
        fim[1][j] = ((r & 1) ? p : q) ^ m3;
        fre[2][j] = p ^ m1; fim[2][j] = q ^ m1;
        fre[3][j] = fre[1][j] ^ m1; fim[3][j] = fim[1][j] ^ m1;
      }
      const float a32 = (float)r * (1.f / 32.f);
#pragma unroll
      for (int j = 0; j < 2; ++j) {
        const float a0 = a32 * (float)(4 * g + 2 * j), a1 = a0 + a32;
        const unsigned c = pk2(__builtin_amdgcn_cosf(a0), __builtin_amdgcn_cosf(a1)), s = pk2(__builtin_amdgcn_sinf(a0), __builtin_amdgcn_sinf(a1));
        fc[0][j] = c; fc[0][2 + j] = c ^ m1; fs[0][j] = s; fs[0][2 + j] = s ^ m1;
      }
#pragma unroll
      for (int j = 0; j < 4; ++j) {
        fc[1][j] = fc[0][j] ^ 0x80000000u; fs[1][j] = fs[0][j] ^ 0x80000000u;
        fn[0][j] = fs[0][j] ^ 0x80008000u; fn[1][j] = fs[0][j] ^ 0x00008000u;
        ro[j] = 8192 + r * 256 + (((4 * j + g) ^ r) * 16);
      }
#pragma unroll
      for (int t = 0; t < 2; ++t)
#pragma unroll
        for (int j = 0; j < 4; ++j) {
          const int s2 = 16 * t + 4 * g + j;
          const float a = a64 * (1.f / 32.f) * (float)s2;
          tc[t][j >> 1][j & 1] = __builtin_amdgcn_cosf(a);
          tsn[t][j >> 1][j & 1] = __builtin_amdgcn_sinf(a);
          uo[t][j] = 8192 + s2 * 256 + (((4 * wave + (r >> 2)) ^ (s2 & 15)) * 16) + (r & 3) * 4;
        }
    }
    unsigned vy = wave * 1024 + r * 64 + (g & 1) * 32 + (g >> 1) * 16;
#pragma unroll
    for (int ks = 0; ks < 4; ++ks) { asm volatile("" : "+v"(fre[ks]), "+v"(fim[ks]), "+v"(ro[ks])); }
#pragma unroll
    for (int t = 0; t < 2; ++t) {
      asm volatile("" : "+v"(fc[t]), "+v"(fs[t]), "+v"(fn[t]), "+v"(tc[t][0]), "+v"(tc[t][1]), "+v"(tsn[t][0]), "+v"(tsn[t][1]));
      asm volatile("" : "+v"(uo[t][0]), "+v"(uo[t][1]), "+v"(uo[t][2]), "+v"(uo[t][3]), "+v"(vy));
    }
    SCHED();
    TS(6);
    FftC T = {fre, fim, fc, fs, fn, tc, tsn, uo, ro, vy};
    WAIT_V(0);
    asm volatile("" : "+v"(wv[0][0]), "+v"(wv[0][1]), "+v"(wv[1][0]), "+v"(wv[1][1]));
    BAR();
    SCHED();
    TS(8);
    if (two) fft_run<2>(ws, sm, T, wv, fu0, wave8, lane);
    else fft_run<1>(ws, sm, T, wv, fu0, wave8, lane);
  } else if (iskv) {
    const int ks = (v - 88) >> 1, cp = 2 * ((v - 88) & 1) + team;
    const int hf = (32 * ks) / 20, e0 = 20 * hf;
    const int q = r >> 2, p = lane & 3;
    const int hsw = ((q >> 1) & 1) | ((g & 1) << 1);
    const int L1 = (160 * cp) >> 7, L2 = (1120 - 160 * cp) >> 7;
    lptr wb = sm;
    {
      const rsrc_t rwo = mk_rsrc(A.wo, 640 * 1280);
      const int line = (wave < 2 ? L1 : L2) + (wave & 1), row = lane >> 3;
#pragma unroll
      for (int kb = 0; kb < 8; ++kb) {
        const int pair = ((lane & 7) >> 1) ^ (((row >> 1) & 1) | ((kb & 1) << 1));
        dma16_asm(rwo, smu + (wave * 8 + kb) * 1024, min(e0 + 8 * kb + row, 639) * 1280 + line * 128 + pair * 32 + (lane & 1) * 16, 0);
      }
    }
    lptr ab = sm0 + 32768;
    if (threadIdx.x < 288) *(LDS_AS u32x4*)(ab + threadIdx.x * 16) = (u32x4){0u, 0u, 0u, 0u};
    const int t5 = min((int)threadIdx.x >> 1, 159), kl = t5 / 5, qd = t5 - kl * 5;
    const int k = 32 * ks + kl, h = k / 20, d = k - 20 * h;
    unsigned ko = h * 1920 + d * 96 + qd * 16 + (threadIdx.x & 1) * 8 * 61440, ao = kl * 144 + (20 * (h - hf) + 4 * qd) * 2;
    unsigned bo[3];
#pragma unroll
    for (int n = 0; n < 3; ++n) {
      const int tl = min(wave + 4 * n, 9);
      const int ctl = tl >= 5 ? 1 : 0, nt = tl - 5 * ctl, ctg = 2 * cp + ctl;
      const int i = 16 * nt + 4 * p;
      const int byte = 2 * (i < 40 ? 40 * ctg + i : 560 - 40 * ctg + i);
      const int pl = i < 40 ? (byte >> 7) - L1 : 2 + (byte >> 7) - L2;
      bo[n] = (pl * 8 + g) * 1024 + q * 128 + ((((byte >> 5) & 3) ^ hsw) * 32) + (byte & 31);
      asm volatile("" : "+v"(bo[n]));
    }
    asm volatile("" : "+v"(ko), "+v"(ao));
    WAIT_V(0);
    WAIT_L0();
    TS(6);
    await(ws, 1, e, 2, wave8, lane);
    TS(7);
    f32x4 pv[8];
    const rsrc_t rk = mk_rsrc(ws + WS_KVP, wave8 < 5 ? 983040 : 0);
#pragma unroll
    for (int i = 0; i < 8; ++i) pv[i] = __builtin_bit_cast(f32x4, LD16(rk, ko, i * 61440));
    SCHED();
    bf16x8 bw[3][2];
#pragma unroll
    for (int n = 0; n < 3; ++n)
#pragma unroll
      for (int s = 0; s < 2; ++s) {
        const s16x4 lo = TRREAD(wb + bo[n] + s * 4096), hi = TRREAD(wb + bo[n] + s * 4096 + 512);
        bw[n][s] = __builtin_shufflevector(lo, hi, 0, 1, 2, 3, 4, 5, 6, 7);
      }
    SCHED();
    f32x4 sum = ((pv[0] + pv[1]) + (pv[2] + pv[3])) + ((pv[4] + pv[5]) + (pv[6] + pv[7]));
#pragma unroll
    for (int j = 0; j < 4; ++j) sum[j] += __uint_as_float(__builtin_amdgcn_update_dpp(0u, __float_as_uint(sum[j]), 0xB1, 0xf, 0xf, false));
    if (threadIdx.x < 320 && !(threadIdx.x & 1)) *(LDS_AS u32x2*)(ab + ao) = (u32x2){pk2(sum[0], sum[1]), pk2(sum[2], sum[3])};
    WAIT_L0();
    BAR();
    TS(8);
    bf16x8 a[2][2];
#pragma unroll
    for (int t = 0; t < 2; ++t)
#pragma unroll
      for (int s = 0; s < 2; ++s) a[t][s] = *(LDS_AS bf16x8*)(ab + (8 * (r >> 2) + (r & 3) + 4 * t) * 144 + s * 64 + g * 16);
    const rsrc_t rm = mk_rsrc(ws + WS_M + (size_t)(10 * cp) * 20480 + ks * 1024, 9 * 20480 + 1024);
#pragma unroll
    for (int n = 0; n < 3; ++n) {
      f32x4 d1 = MFMA16(a[0][0], bw[n][0], z4), d2 = MFMA16(a[1][0], bw[n][0], z4);
      d1 = MFMA16(a[0][1], bw[n][1], d1); d2 = MFMA16(a[1][1], bw[n][1], d2);
      ST16(__builtin_bit_cast(u32x4, pack8(d1, d2)), rm, lane * 16, (wave + 4 * n) * 20480);
    }
  } else if (ct1 < 4) {
    const int t8 = threadIdx.x, wm = wave8 >> 1, wn = wave8 & 1;
    const int gb = rt1 * 4 + ct1;
      const rsrc_t rx = mk_rsrc(A.x + (size_t)gb * 32 * 640, 32 * 1280), rg = mk_rsrc(A.gw, 1280);
    u32x4 gx[5], gg[5];
#pragma unroll
      for (int i = 0; i < 5; ++i) {
        gx[i] = __builtin_amdgcn_raw_buffer_load_b128(rx, t8 * 80 + i * 16, 0, 0);
        gg[i] = __builtin_amdgcn_raw_buffer_load_b128(rg, (t8 & 15) * 80 + i * 16, 0, 0);
      }
    u32x2 pk[2][5];
    unsigned zm[5];
#pragma unroll
    for (int nt = 0; nt < 5; ++nt) {
#pragma unroll
      for (int mt = 0; mt < 2; ++mt) {
        pk[mt][nt] = (u32x2){pk2(pq[mt][nt][0], pq[mt][nt][1]), pk2(pq[mt][nt][2], pq[mt][nt][3])};
        asm volatile("" : "+v"(pk[mt][nt]));
      }
      zm[nt] = ((4 * nt + g) / 5 == (r & 3)) ? 0xffffffffu : 0u;
      asm volatile("" : "+v"(zm[nt]));
    }
    const rsrc_t rz = mk_rsrc(ws + WS_ZP + ct1 * 640, 40960);
    const rsrc_t rp = mk_rsrc(ws + WS_P + (size_t)(8 * rt1 + 2 * wm) * 20480 + (20 * ct1 + 10 * wn) * 256, 2 * 20480);
    const unsigned vo = (g & 1) * 20480 + (g >> 1) * 256 + r * 16;
    SCHED();
    TS(6);
    await(ws, 1, e, 2, wave8, lane);
    TS(7);
    if (wave8 < 3) {
      float zv[16];
#pragma unroll
      for (int i = 0; i < 16; ++i) zv[i] = __uint_as_float(__builtin_amdgcn_raw_buffer_load_b32(rz, min(t8, 159) * 4, i * 2560, LD_AUX));
#pragma unroll
      for (int i = 0; i < 8; ++i) zv[i] += zv[i + 8];
      const float zs = ((zv[0] + zv[1]) + (zv[2] + zv[3])) + ((zv[4] + zv[5]) + (zv[6] + zv[7]));
      *(LDS_AS u16*)(sm0 + t8 * 2) = (u16)pk2(zs, zs);
    }
    WAIT_L0();
    BAR();
    TS(8);
    u32x2 zq[6];
#pragma unroll
    for (int nt = 0; nt < 5; ++nt) { zq[nt] = *(LDS_AS u32x2*)(sm0 + wn * 160 + nt * 32 + g * 8); zq[nt][0] &= zm[nt]; zq[nt][1] &= zm[nt]; }
    zq[5] = (u32x2){0u, 0u};
    u32x2 po[2][5];
#pragma unroll
    for (int mt = 0; mt < 2; ++mt) {
      f32x4 den = {kEps, kEps, kEps, kEps};
#pragma unroll
      for (int m = 0; m < 3; ++m) {
        const u32x4 za = {zq[2 * m][0], zq[2 * m][1], zq[2 * m + 1][0], zq[2 * m + 1][1]};
        const u32x2 p1 = m < 2 ? pk[mt][2 * m + 1] : (u32x2){0u, 0u};
        const u32x4 pb = {pk[mt][2 * m][0], pk[mt][2 * m][1], p1[0], p1[1]};
        den = MFMA16(BC(za), BC(pb), den);
      }
      f32x4 inv;
#pragma unroll
      for (int j = 0; j < 4; ++j) inv[j] = __builtin_amdgcn_rcpf(den[j]);
#pragma unroll
      for (int nt = 0; nt < 5; ++nt) {
        const float iv = nt == 0 ? inv[0] : nt == 4 ? inv[3] : (g < nt ? inv[nt - 1] : inv[nt]);
        const f32x4 w = pq[mt][nt] * iv;
        po[mt][nt] = (u32x2){pk2(w[0], w[1]), pk2(w[2], w[3])};
      }
    }
#pragma unroll
    for (int nt = 0; nt < 5; ++nt) ST16(widen(po[0][nt], po[1][nt]), rp, vo + nt * 512, 0);
    SCHED();
    {
      float s0 = 0.f, s1 = 0.f;
#pragma unroll
      for (int i = 0; i < 5; ++i) { s0 = dot2(gx[i][0], gg[i][0], s0); s1 = dot2(gx[i][1], gg[i][1], s1); s0 = dot2(gx[i][2], gg[i][2], s0); s1 = dot2(gx[i][3], gg[i][3], s1); }
      float s = s0 + s1;
#pragma unroll
      for (int o = 8; o > 0; o >>= 1) s += __shfl_xor(s, o);
      if ((t8 & 15) == 0) __builtin_amdgcn_raw_buffer_store_b32(__float_as_uint(1.f / (1.f + __expf(-s))), mk_rsrc(ws + WS_G, 8192), (gb * 32 + (t8 >> 4)) * 4, 0, 16);
    }
  }
  TS(9);
}

constexpr int LDS_ALL = K1_LDS;
static_assert(81920 + (2 * 64 * 84 + 2 * 64 * 52 + 64) * 4 <= LDS_ALL, "lds");

DI void phase3(const Args& A, lptr sm, unsigned e TS_ARG) {
  char* ws = A.ws;
  const int tid = threadIdx.x, lane = tid & 63;
  const int wave = __builtin_amdgcn_readfirstlane(tid >> 6);
  const int b = blockIdx.x, xcd = b & 7, idx = b >> 3;
  const int rt = xcd * 4 + (idx & 3);
  const int ct = idx >> 2;
  const int r = lane & 15, g = lane >> 4;
  LDS_AS float* red = (LDS_AS float*)(sm + 81920);
  constexpr int RA = 64 * 84, RE = 64 * 52, RG = 2 * RA + 2 * RE;
  const bool rev = P3_REV && (((idx >> 3) ^ idx) & 1);
  const int k0 = rev ? 9 : 0, kd = rev ? -1 : 1;
  u32x4 fg[10][3];
  if (wave == 2 || wave == 3) {
    const rsrc_t rg = mk_rsrc((const char*)&GT + (size_t)(2 * ct + wave - 2) * 30720, 30720);
#pragma unroll
    for (int i = 0; i < 30; ++i) fg[i / 3][i % 3] = __builtin_amdgcn_raw_buffer_load_b128(rg, lane * 16 + (i % 3) * 1024, (k0 + kd * (i / 3)) * 3072, 0);
  }
  await(ws, 2, e, 7, wave, lane);
  TS(11);
  if (wave < 2) {
    const rsrc_t ra = mk_rsrc(ws + WS_P + (size_t)(4 * rt) * 20480 + wave * 10240, 4 * 20480);
    const rsrc_t rb = mk_rsrc(ws + WS_M + (size_t)(5 * ct) * 20480 + wave * 10240, 5 * 20480);
    const unsigned vo = lane * 16;
    f32x4 acc[4][5];
#pragma unroll
    for (int mt = 0; mt < 4; ++mt)
#pragma unroll
      for (int nt = 0; nt < 5; ++nt) acc[mt][nt] = (f32x4){0.f, 0.f, 0.f, 0.f};
    u32x4 fa[10][4], fb[10][5];
    constexpr int PF = P3_PF;
    auto ld = [&](int ks) {
#pragma unroll
      for (int nt = 0; nt < 5; ++nt) fb[ks][nt] = LD16(rb, vo, nt * 20480 + (k0 + kd * ks) * 1024);
#pragma unroll
      for (int mt = 0; mt < 4; ++mt) fa[ks][mt] = LD16(ra, vo, mt * 20480 + (k0 + kd * ks) * 1024);
    };
#pragma unroll
    for (int ks = 0; ks < PF; ++ks) { ld(ks); SCHED(); }
#pragma unroll
    for (int ks = 0; ks < 10; ++ks) {
#pragma unroll
      for (int mt = 0; mt < 4; ++mt)
#pragma unroll
        for (int nt = 0; nt < 5; ++nt) acc[mt][nt] = MFMA16(BC(fb[ks][nt]), BC(fa[ks][mt]), acc[mt][nt]);
      if (ks < 9) SCHED();
      if (ks + PF < 10) ld(ks + PF);
      if (ks < 9) SCHED();
    }
    TS(12);
#pragma unroll
    for (int mt = 0; mt < 4; ++mt)
#pragma unroll
      for (int nt = 0; nt < 5; ++nt) *(LDS_AS f32x4*)(red + wave * RA + (16 * mt + r) * 84 + 16 * nt + 4 * g) = acc[mt][nt];
  } else if (wave < 4) {
    const int od = wave - 2;
    lptr yb = sm + od * 40960;
    const unsigned ybu = (unsigned)(size_t)yb;
    const rsrc_t ry = mk_rsrc(ws + WS_Y + (size_t)od * 320 * 4096 + rt * 128, 320 * 4096);
    const int row = lane >> 3;
    const unsigned vy0 = row * 4096 + ((((lane & 7) >> 1) ^ ((row >> 1) & 1)) * 32) + (lane & 1) * 16;
    const unsigned vy1 = vy0 ^ 64;
    auto issue = [&](int ks) {
#pragma unroll
      for (int i = 0; i < 4; ++i) dma16_sc1(ry, ybu + (ks * 4 + i) * 1024, (i & 1) ? vy1 : vy0, ((k0 + kd * ks) * 4 + i) * 32768);
    };
#pragma unroll
    for (int i = 0; i < 30; ++i) asm volatile("" : "+v"(fg[i / 3][i % 3]));
    issue(0); issue(1); issue(2);
    if (P3_YW == 5) { issue(3); issue(4); }
    const int q = r >> 2, p = lane & 3;
    const int hsw = ((q >> 1) & 1) | ((g & 1) << 1);
    unsigned yo[4];
#pragma unroll
    for (int mt = 0; mt < 4; ++mt) yo[mt] = g * 1024 + q * 128 + ((mt ^ hsw) * 32) + p * 8;
    f32x4 acc[4][3];
#pragma unroll
    for (int mt = 0; mt < 4; ++mt)
#pragma unroll
      for (int nt = 0; nt < 3; ++nt) acc[mt][nt] = (f32x4){0.f, 0.f, 0.f, 0.f};
#define K3_STEP(ks, n)                                                                                         \
    {                                                                                                          \
      WAIT_V(n);                                                                                               \
      SCHED();                                                                                                 \
      bf16x8 fy[4];                                                                                            \
      _Pragma("unroll") for (int mt = 0; mt < 4; ++mt) {                                                       \
        const s16x4 lo = TRREAD(yb + yo[mt] + ks * 4096), hi = TRREAD(yb + yo[mt] + ks * 4096 + 512);          \
        fy[mt] = __builtin_shufflevector(lo, hi, 0, 1, 2, 3, 4, 5, 6, 7);                                      \
      }                                                                                                        \
      SCHED();                                                                                                 \
      if (ks + P3_YW < 10) issue(ks + P3_YW);                                                                          \
      SCHED();                                                                                                 \
      _Pragma("unroll") for (int mt = 0; mt < 4; ++mt)                                                         \
        _Pragma("unroll") for (int nt = 0; nt < 3; ++nt) acc[mt][nt] = MFMA16(BC(fg[ks][nt]), fy[mt], acc[mt][nt]);   \
      SCHED();                                                                                                 \
    }
    K3_STEP(0, 8) K3_STEP(1, 8) K3_STEP(2, 8) K3_STEP(3, 8) K3_STEP(4, 8)
    K3_STEP(5, 8) K3_STEP(6, 8) K3_STEP(7, 8) K3_STEP(8, 4) K3_STEP(9, 0)
    TS(12);
#pragma unroll
    for (int mt = 0; mt < 4; ++mt)
#pragma unroll
      for (int nt = 0; nt < 3; ++nt) *(LDS_AS f32x4*)(red + 2 * RA + od * RE + (16 * mt + r) * 52 + 16 * nt + 4 * g) = acc[mt][nt];
  } else {
    if (wave == 4) red[RG + lane] = __uint_as_float(__builtin_amdgcn_raw_buffer_load_b32(mk_rsrc(ws + WS_G + rt * 256, 256), lane * 4, 0, LD_AUX));
  }
  const rsrc_t ro = mk_rsrc(A.out + (size_t)(64 * rt) * 640, 64 * 1280);
  unsigned oa[2], oe[2], oq[2], og[2], or_[2];
#pragma unroll
  for (int i = 0; i < 2; ++i) {
    const int it = i ? 512 + wave * 16 + (lane & 15) : tid;
    const int row = it / 10, gr = it - row * 10;
    oa[i] = (row * 84 + 8 * gr) * 4;
    oe[i] = (2 * RA + row * 52) * 4;
    oq[i] = oe[i] + (gr < 5 ? 8 * gr : 72 - 8 * gr) * 4;
    og[i] = (i && lane >= 16) ? 0x7fffffffu : row * 1280 + (gr < 5 ? 80 * ct : 1120 - 80 * ct) + 16 * gr;
    or_[i] = (RG + row) * 4;
    oa[i] |= (gr < 5) ? 0u : 0x80000000u;
    asm volatile("" : "+v"(oa[i]), "+v"(oe[i]), "+v"(oq[i]), "+v"(og[i]), "+v"(or_[i]));
  }
  WAIT_L0();
  BAR();
  TS(13);
#pragma unroll
  for (int i = 0; i < 2; ++i) {
    const bool lo = (int)oa[i] >= 0;
    lptr rb = sm + 81920, pa = rb + (oa[i] & 0x7fffffffu), pq = rb + oq[i];
    const float gq = *(LDS_AS float*)(rb + or_[i]);
    const f32x4 a0 = *(LDS_AS f32x4*)pa + *(LDS_AS f32x4*)(pa + RA * 4);
    const f32x4 a1 = *(LDS_AS f32x4*)(pa + 16) + *(LDS_AS f32x4*)(pa + RA * 4 + 16);
    const float yn = *(LDS_AS float*)(rb + oe[i] + (RE + 41) * 4);
    const f32x4 e0 = *(LDS_AS f32x4*)pq, e1 = *(LDS_AS f32x4*)(pq + 16), o0 = *(LDS_AS f32x4*)(pq + RE * 4), o1 = *(LDS_AS f32x4*)(pq + RE * 4 + 16);
    const float p8 = *(LDS_AS float*)(pq + 32) + *(LDS_AS float*)(pq + RE * 4 + 32);
    const f32x4 m0 = e0 - o0, m1 = e1 - o1, p0 = e0 + o0, p1 = e1 + o1;
    const f32x4 yy = {yn, -yn, yn, -yn};
    const f32x4 b0 = (lo ? m0 : (f32x4){p8, p1[3], p1[2], p1[1]}) + yy;
    const f32x4 b1 = (lo ? m1 : (f32x4){p1[0], p0[3], p0[2], p0[1]}) + yy;
    const f32x4 r0 = b0 + gq * (a0 - b0), r1 = b1 + gq * (a1 - b1);
    __builtin_amdgcn_raw_buffer_store_b128(__builtin_bit_cast(u32x4, pack8(r0, r1)), ro, og[i], 0, OUT_AUX);
  }
  TS(14);
}

#define TS_END()

__global__ __launch_bounds__(512, 2) void fused_kernel(Args A) {
  extern __shared__ __attribute__((aligned(16))) char smem[];
  lptr sm = (lptr)smem;
  TS_DECL;
  TS(0);
  {
    unsigned long long pc; unsigned len;
    asm volatile("s_getpc_b64 %0\n.Lfk_start:\n\ts_mov_b32 %1, .Lfk_end-.Lfk_start" : "=s"(pc), "=s"(len));
    pf_asm(mk_rsrc((const void*)pc, len), (unsigned)(size_t)sm + K1_JUNK + 1792, (threadIdx.x * 32 + (blockIdx.x >> 3)) * 128, 0);
  }
  const rsrc_t re = mk_rsrc(A.ws + WS_EPOCH, 4);
  const unsigned e = *(const __attribute__((address_space(4))) unsigned*)(A.ws + WS_EPOCH);
  f32x4 pq[2][5];
  phase1(A, sm, pq, e TS_PASS);
  phase2(A, sm, e, pq TS_PASS);
  publish(A.ws, 2, e);
  TS(10);
  phase3(A, sm, e TS_PASS);
  if (blockIdx.x == 0 && threadIdx.x == 0) __builtin_amdgcn_raw_buffer_store_b32(e + 1u, re, 0, 0, 16);
  TS_END();
  asm volatile(".Lfk_end:");
}

extern "C" void kernel_launch(void* const* d_in, const int* in_sizes, int n_in, void* d_out, int out_size, void* d_ws,
                              size_t ws_size, hipStream_t stream) {
  Args A;
  A.x = (const u16*)d_in[0]; A.wqkv = (const u16*)d_in[1]; A.wo = (const u16*)d_in[2];
  A.wr = (const u16*)d_in[4]; A.wi = (const u16*)d_in[5]; A.gw = (const u16*)d_in[6];
  A.ws = (char*)d_ws; A.out = (u16*)d_out;
  (void)hipFuncSetAttribute((const void*)fused_kernel, hipFuncAttributeMaxDynamicSharedMemorySize, LDS_ALL);
  fused_kernel<<<256, 512, LDS_ALL, stream>>>(A);
}
```
